```python
import torch

def _softmax(x, axis=-1):
    return torch.softmax(x, dim=axis)
def _gelu(x):
    return torch.nn.functional.gelu(x, approximate="tanh")
def _silu(x):
    return torch.nn.functional.silu(x)
def _sigmoid(x):
    return torch.sigmoid(x)
def _layernorm(x, axis=-1, eps=1e-5):
    m = x.mean(dim=axis, keepdim=True)
    v = x.var(dim=axis, keepdim=True, unbiased=False)
    return (x - m) / torch.sqrt(v + eps)
def _scatter_add(tgt, idx, src, axis=0):
    return tgt.clone().index_add_(axis, idx, src)
def _segment_sum(src, seg, n):
    out = torch.zeros((n,) + src.shape[1:], dtype=src.dtype)
    return out.index_add_(0, seg, src)
def _pad(x, pw):
    flat = tuple(v for pair in reversed(pw) for v in pair)
    return torch.nn.functional.pad(x, flat)

kernel_name = 'concat_heads__mha_block_cached__gqa_block_cached__near_ridge'

def setup_inputs():

    _w = (lambda a: (lambda t: t.to(torch.bfloat16) if t.is_floating_point() else t)(torch.as_tensor(a)))
    def _A_setup():
        B, Sq, D = 8, 192, 640
        H, Sk, Dh = 32, 1024, 128
        return {
            "x": _w((lambda *s: torch.randn(*s, dtype=torch.bfloat16) * 0.8)(B, Sq, D)),
            "k_cache": _w((lambda *s: torch.randn(*s, dtype=torch.bfloat16) * 0.8)(B, H, Sk, Dh)),
            "v_cache": _w((lambda *s: torch.randn(*s, dtype=torch.bfloat16) * 0.05)(B, H, Sk, Dh)),
            "wq": _w((lambda *s: torch.randn(*s, dtype=torch.bfloat16) * 0.8)(D, H * Dh) * (D ** -0.5)),
            "wo": _w((lambda *s: torch.randn(*s, dtype=torch.bfloat16) * 0.05)(H * Dh, D) * ((H * Dh) ** -0.5)),
        }
    _A_in = _A_setup()

    def _B_setup():
        B, Sq, D = 8, 192, 640
        Hq, Hkv, Sk, Dh = 32, 32, 1024, 128
        assert Hq % Hkv == 0, (Hq, Hkv)
        return {
            "x": _w((lambda *s: torch.randn(*s, dtype=torch.bfloat16) * 0.8)(B, Sq, D)),
            "k_cache": _w((lambda *s: torch.randn(*s, dtype=torch.bfloat16) * 0.8)(B, Hkv, Sk, Dh)),
            "v_cache": _w((lambda *s: torch.randn(*s, dtype=torch.bfloat16) * 0.05)(B, Hkv, Sk, Dh)),
            "wq": _w((lambda *s: torch.randn(*s, dtype=torch.bfloat16) * 0.8)(D, Hq * Dh) * (D ** -0.5)),
            "wo": _w((lambda *s: torch.randn(*s, dtype=torch.bfloat16) * 0.05)(Hq * Dh, D) * ((Hq * Dh) ** -0.5)),
        }
    _B_in = _B_setup()

    return {
        "A_x": _A_in["x"],
        "A_k_cache": _A_in["k_cache"],
        "A_v_cache": _A_in["v_cache"],
        "A_wq": _A_in["wq"],
        "A_wo": _A_in["wo"],
        "B_x": _B_in["x"],
        "B_k_cache": _B_in["k_cache"],
        "B_v_cache": _B_in["v_cache"],
        "B_wq": _B_in["wq"],
        "B_wo": _B_in["wo"],
    }

def reference(A_x, A_k_cache, A_v_cache, A_wq, A_wo, B_x, B_k_cache, B_v_cache, B_wq, B_wo):
    def _A_ref(x, k_cache, v_cache, wq, wo):
        B, Sq, D = x.shape
        H, Dh = 32, 128
        q = (lambda x, *d: x.permute(*d))((x @ wq).reshape(B, Sq, H, Dh), 0, 2, 1, 3)
        scores = torch.einsum("bhqd,bhkd->bhqk", q, k_cache) * (Dh ** -0.5)
        p = _softmax(scores, axis=-1)
        o = torch.einsum("bhqk,bhkd->bhqd", p, v_cache)
        return (lambda x, *d: x.permute(*d))(o, 0, 2, 1, 3).reshape(B, Sq, H * Dh) @ wo

    def _B_ref(x, k_cache, v_cache, wq, wo):
        B, Sq, D = x.shape
        Hq, Dh = 32, 128
        Hkv = k_cache.shape[1]
        rep = Hq // Hkv
        q = (lambda x, *d: x.permute(*d))((x @ wq).reshape(B, Sq, Hq, Dh), 0, 2, 1, 3)
        k = (lambda x, n, axis: torch.repeat_interleave(x, n, dim=axis))(k_cache, rep, axis=1)
        v = (lambda x, n, axis: torch.repeat_interleave(x, n, dim=axis))(v_cache, rep, axis=1)
        scores = torch.einsum("bhqd,bhkd->bhqk", q, k) * (Dh ** -0.5)
        p = _softmax(scores, axis=-1)
        o = torch.einsum("bhqk,bhkd->bhqd", p, v)
        return (lambda x, *d: x.permute(*d))(o, 0, 2, 1, 3).reshape(B, Sq, Hq * Dh) @ wo

    a = _A_ref(A_x, A_k_cache, A_v_cache, A_wq, A_wo)
    b = _B_ref(B_x, B_k_cache, B_v_cache, B_wq, B_wo)
    return torch.concatenate([a, b], axis=-1)
```

```cpp
#include <hip/hip_runtime.h>
#include <hip/hip_bf16.h>
typedef short bf16x8 __attribute__((ext_vector_type(8)));
typedef float f32x16 __attribute__((ext_vector_type(16)));
typedef float f32x2 __attribute__((ext_vector_type(2)));
typedef __bf16 bf16x2 __attribute__((ext_vector_type(2)));
typedef unsigned short u16;
#define DI __device__ __forceinline__
DI unsigned cvtpk(float lo, float hi){ f32x2 v={lo,hi}; bf16x2 b=__builtin_convertvector(v,bf16x2); return __builtin_bit_cast(unsigned,b);}
DI u16 tobf(float x){ return (u16)(cvtpk(x,0.f)&0xffff);}
DI int crow(int r,int h){return (r&3)+8*(r>>2)+4*h;}
__global__ __launch_bounds__(256) void gemm(const u16* A,const u16* B,u16* C,int M,int N,int K,int ldc,float scale){
  int wid=threadIdx.x>>6, lane=threadIdx.x&63, r=lane&31,h=lane>>5;
  int tn=blockIdx.x*4+wid, tm=blockIdx.y;
  if(tn*32>=N) return;
  f32x16 acc={};
  for(int k=0;k<K;k+=16){
    bf16x8 a=*(const bf16x8*)(A+(long)(tm*32+r)*K+k+8*h);
    bf16x8 b; for(int j=0;j<8;j++) b[j]=B[(long)(k+8*h+j)*N+tn*32+r];
    acc=__builtin_amdgcn_mfma_f32_32x32x16_bf16(a,b,acc,0,0,0);
  }
  for(int i=0;i<16;i++) C[(long)(tm*32+crow(i,h))*ldc+tn*32+r]=tobf(acc[i]*scale);
}
__global__ __launch_bounds__(384) void attn(const u16* Q,const u16* K,const u16* V,u16* O){
  int wid=threadIdx.x>>6, lane=threadIdx.x&63, r=lane&31,h=lane>>5;
  int bh=blockIdx.x, b=bh>>5, hd=bh&31;
  const u16* Kp=K+(long)bh*1024*128; const u16* Vp=V+(long)bh*1024*128;
  bf16x8 q[8];
  for(int s=0;s<8;s++) q[s]=*(const bf16x8*)(Q+(long)(b*192+wid*32+r)*4096+hd*128+16*s+8*h);
  f32x16 o[4]={}; float m=-1e30f,l=0.f;
  for(int t=0;t<32;t++){
    f32x16 s={};
    #pragma unroll
    for(int d=0;d<8;d++){ bf16x8 a=*(const bf16x8*)(Kp+(long)(t*32+r)*128+16*d+8*h); s=__builtin_amdgcn_mfma_f32_32x32x16_bf16(a,q[d],s,0,0,0);}
    float pm=s[0]; for(int i=1;i<16;i++) pm=fmaxf(pm,s[i]);
    pm=fmaxf(pm,__shfl_xor(pm,32));
    float mn=fmaxf(m,pm), al=exp2f(m-mn); m=mn;
    float ps=0; for(int i=0;i<16;i++){ s[i]=exp2f(s[i]-mn); ps+=s[i];}
    ps+=__shfl_xor(ps,32); l=l*al+ps;
    bf16x8 p[2];
    for(int ks=0;ks<2;ks++){ for(int j=0;j<8;j++) p[ks][j]=tobf(s[8*ks+j]); }
    #pragma unroll
    for(int db=0;db<4;db++){
      for(int i=0;i<16;i++) o[db][i]*=al;
      #pragma unroll
      for(int ks=0;ks<2;ks++){
        bf16x8 a; for(int j=0;j<8;j++){ int key=16*ks+8*(j>>2)+4*h+(j&3); a[j]=Vp[(long)(t*32+key)*128+32*db+r]; }
        o[db]=__builtin_amdgcn_mfma_f32_32x32x16_bf16(a,p[ks],o[db],0,0,0);
      }
    }
  }
  float il=1.f/l;
  for(int db=0;db<4;db++) for(int i=0;i<16;i++) O[(long)(b*192+wid*32+r)*4096+hd*128+32*db+crow(i,h)]=tobf(o[db][i]*il);
}
extern "C" void kernel_launch(void* const* d_in, const int* in_sizes, int n_in, void* d_out, int out_size, void* d_ws, size_t ws_size, hipStream_t stream){
  u16* ws=(u16*)d_ws; u16* out=(u16*)d_out;
  const float C=0.088388347648318440f*1.4426950408889634f;
  for(int p=0;p<2;p++){
    const u16* x=(const u16*)d_in[5*p+0]; const u16* K=(const u16*)d_in[5*p+1]; const u16* V=(const u16*)d_in[5*p+2];
    const u16* wq=(const u16*)d_in[5*p+3]; const u16* wo=(const u16*)d_in[5*p+4];
    u16* Q=ws+(size_t)p*2*1536*4096; u16* O=Q+(size_t)1536*4096;
    gemm<<<dim3(32,48),256,0,stream>>>(x,wq,Q,1536,4096,640,4096,C);
    attn<<<256,384,0,stream>>>(Q,K,V,O);
    gemm<<<dim3(5,48),256,0,stream>>>(O,wo,out+640*p,1536,640,4096,1280,1.f);
  }
}
```

```cpp
#include <hip/hip_runtime.h>
#include <hip/hip_bf16.h>
typedef unsigned short u16;
typedef short bf16x8 __attribute__((ext_vector_type(8)));
typedef float f32x4 __attribute__((ext_vector_type(4)));
typedef float f32x2 __attribute__((ext_vector_type(2)));
typedef __bf16 bf16x2 __attribute__((ext_vector_type(2)));
typedef unsigned u32x4 __attribute__((ext_vector_type(4)));
typedef unsigned u32x2 __attribute__((ext_vector_type(2)));
typedef __attribute__((address_space(1))) unsigned gu32;
#define DI __device__ __forceinline__
DI unsigned rfl(unsigned x) { return __builtin_amdgcn_readfirstlane(x); }
DI unsigned lane_id() { unsigned l; asm volatile("v_mbcnt_lo_u32_b32 %0, -1, 0\n\tv_mbcnt_hi_u32_b32 %0, -1, %0" : "=v"(l)); return l; }
DI unsigned cvtpk(float lo, float hi) { f32x2 v = {lo, hi}; bf16x2 b = __builtin_convertvector(v, bf16x2); return __builtin_bit_cast(unsigned, b); }
DI u32x4 mksrd(const void* p, unsigned n) { const size_t a = (size_t)p; u32x4 r; r[0] = rfl((unsigned)a); r[1] = rfl((unsigned)(a >> 32) & 0xffffu); r[2] = rfl(n); r[3] = 0x20000u; return r; }
DI void st16_sc1(char* dst, u32x4 w) { asm volatile("global_store_dwordx4 %0, %1, off sc1\n\ts_nop 1" :: "v"(dst), "v"(w) : "memory"); }
#define MFMA16(a, b, c) __builtin_amdgcn_mfma_f32_16x16x32_bf16((a), (b), (c), 0, 0, 0)
constexpr float QSCALE = 0.088388347648318440f * 1.4426950408889634f;
constexpr unsigned VAREA = 81920;
#define XQ ((bid >> 1) & 3u)
#define PP ((bid & 1u) ^ 1u)
#define KROT ((bid >> 3) * 5u + (bid & 7u) * 4u + j * 16u)
constexpr size_t WS_CNT = (size_t)26 << 20, WS_PS = (size_t)32 << 20;

struct OFrag { u32x4 f0, f1, f2, f3; };
static __device__ __attribute__((noinline)) OFrag slow_block(const u16* x, const u16* wq, const u16* kp, const u16* vp, unsigned lane) {
  u32x4 of[4];
  const unsigned i = lane & 15, g = lane >> 4;
  f32x4 qa[8];
#pragma unroll
  for (int T = 0; T < 8; ++T) qa[T] = f32x4{0.f, 0.f, 0.f, 0.f};
  for (int ks = 0; ks < 20; ++ks) {
    const bf16x8 xb = *(const bf16x8*)(x + (size_t)i * 640 + 32 * ks + 8 * g);
#pragma unroll
    for (int T = 0; T < 8; ++T) {
      bf16x8 w;
#pragma unroll
      for (int jj = 0; jj < 8; ++jj) w[jj] = (short)wq[(size_t)(32 * ks + 8 * g + jj) * 4096 + 32 * (T >> 1) + 8 * (i >> 2) + 4 * (T & 1) + (i & 3)];
      qa[T] = MFMA16(w, xb, qa[T]);
    }
  }
  bf16x8 q[4];
#pragma unroll
  for (int s = 0; s < 4; ++s) {
    u32x4 w = {cvtpk(qa[2 * s][0] * QSCALE, qa[2 * s][1] * QSCALE), cvtpk(qa[2 * s][2] * QSCALE, qa[2 * s][3] * QSCALE),
               cvtpk(qa[2 * s + 1][0] * QSCALE, qa[2 * s + 1][1] * QSCALE), cvtpk(qa[2 * s + 1][2] * QSCALE, qa[2 * s + 1][3] * QSCALE)};
    q[s] = __builtin_bit_cast(bf16x8, w);
  }
  f32x4 o[8];
#pragma unroll
  for (int db = 0; db < 8; ++db) o[db] = f32x4{0.f, 0.f, 0.f, 0.f};
  float m = -__builtin_inff(), l = 0.f;
  for (int t = 0; t < 32; ++t) {
    f32x4 s0 = {0.f, 0.f, 0.f, 0.f}, s1 = {0.f, 0.f, 0.f, 0.f};
#pragma unroll
    for (int s = 0; s < 4; ++s) {
      s0 = MFMA16(*(const bf16x8*)(kp + (size_t)(32 * t + i) * 128 + 32 * s + 8 * g), q[s], s0);
      s1 = MFMA16(*(const bf16x8*)(kp + (size_t)(32 * t + 16 + i) * 128 + 32 * s + 8 * g), q[s], s1);
    }
    float pm = fmaxf(fmaxf(fmaxf(s0[0], s0[1]), fmaxf(s0[2], s0[3])), fmaxf(fmaxf(s1[0], s1[1]), fmaxf(s1[2], s1[3])));
    pm = fmaxf(pm, __shfl_xor(pm, 16)); pm = fmaxf(pm, __shfl_xor(pm, 32));
    const float mn = fmaxf(m, pm), al = __builtin_amdgcn_exp2f(m - mn); m = mn;
    float ps = 0.f;
#pragma unroll
    for (int r = 0; r < 4; ++r) { s0[r] = __builtin_amdgcn_exp2f(s0[r] - mn); s1[r] = __builtin_amdgcn_exp2f(s1[r] - mn); ps += s0[r] + s1[r]; }
    ps += __shfl_xor(ps, 16); ps += __shfl_xor(ps, 32); l = l * al + ps;
    const u32x4 pw = {cvtpk(s0[0], s0[1]), cvtpk(s0[2], s0[3]), cvtpk(s1[0], s1[1]), cvtpk(s1[2], s1[3])};
    const bf16x8 pb = __builtin_bit_cast(bf16x8, pw);
#pragma unroll
    for (int db = 0; db < 8; ++db) {
      bf16x8 va;
#pragma unroll
      for (int jj = 0; jj < 8; ++jj) va[jj] = (short)vp[(size_t)(32 * t + 16 * (jj >> 2) + 4 * g + (jj & 3)) * 128 + 16 * db + i];
      o[db] = o[db] * al;
      o[db] = MFMA16(va, pb, o[db]);
    }
  }
  const float il = 1.f / l;
#pragma unroll
  for (int s = 0; s < 4; ++s)
    of[s] = u32x4{cvtpk(o[2 * s][0] * il, o[2 * s][1] * il), cvtpk(o[2 * s][2] * il, o[2 * s][3] * il),
                  cvtpk(o[2 * s + 1][0] * il, o[2 * s + 1][1] * il), cvtpk(o[2 * s + 1][2] * il, o[2 * s + 1][3] * il)};
  return OFrag{of[0], of[1], of[2], of[3]};
}

struct Params { const u16* x[2]; const u16* k[2]; const u16* v[2]; const u16* wq[2]; const u16* wo[2]; char* ws; u16* out; };
typedef const __attribute__((address_space(4))) Params* KArg;
struct Geo {
  unsigned wid, xq, p, ii, b;
  const u16 *x, *wq, *k, *v, *wo; char* ws; u16* out;
  DI Geo(unsigned wid_, unsigned bid) {
    KArg ka = (KArg)__builtin_amdgcn_kernarg_segment_ptr(); asm volatile("" : "+s"(ka), "+s"(wid_), "+s"(bid));
    wid = wid_; xq = XQ; p = PP; ii = bid >> 6; b = (bid >> 3) & 7u;
    x = ka->x[p] + (size_t)b * 192 * 640; wq = ka->wq[p]; k = ka->k[p] + (size_t)b * 32 * 131072; v = ka->v[p] + (size_t)b * 32 * 131072;
    wo = ka->wo[p]; ws = ka->ws; out = ka->out;
  }
  DI unsigned head() const { return xq * 8u + ii * 2u + (wid >> 1); }
  DI char* ofr() const { return ws + (size_t)((p * 8u + b) * 16u + xq * 4u + ii) * 98304u + (wid & 1u) * 49152u + (wid >> 1) * 4096u; }
  DI char* cntC() const { return ws + WS_CNT + (p * 8u + b) * 8192u + xq * 512u; }
  DI char* cntD() const { return ws + WS_CNT + (p * 8u + b) * 8192u + 4096u + ii * 512u; }
  DI char* ps() const { return ws + WS_PS + (size_t)((p * 8u + b) * 4u + ii) * 245760u; }
  DI u32x4 srd_wo() const { const unsigned o = (xq * 1024u + (wid >> 1) * 32u + (wid & 1u) * 16u) * 1280u + ii * 320u; return mksrd((const char*)wo + o - 4096, 4096u * 1280u - o + 4096u); }
  DI void slow_pair(unsigned row, unsigned col, unsigned h0, unsigned h1, unsigned lane, f32x4& a0, f32x4& a1) const {
    a0 = f32x4{0.f, 0.f, 0.f, 0.f}; a1 = a0;
    for (unsigned hd = h0; hd < h1; ++hd) {
      const OFrag r = slow_block(x + (size_t)row * 640, wq + hd * 128u, k + (size_t)hd * 131072, v + (size_t)hd * 131072, lane);
      const u32x4 of[4] = {r.f0, r.f1, r.f2, r.f3};
#pragma unroll
      for (int s = 0; s < 4; ++s) {
        bf16x8 w0, w1;
#pragma unroll
        for (int jj = 0; jj < 8; ++jj) {
          const u16* w = wo + (size_t)(hd * 128u + 32 * s + 16 * (jj >> 2) + 4 * (lane >> 4) + (jj & 3)) * 640 + col + (lane & 15u);
          w0[jj] = (short)w[0]; w1[jj] = (short)w[16];
        }
        a0 = MFMA16(w0, __builtin_bit_cast(bf16x8, of[s]), a0); a1 = MFMA16(w1, __builtin_bit_cast(bf16x8, of[s]), a1);
      }
    }
  }
  DI void store_pair(unsigned row, unsigned col, unsigned lane, const f32x4& a0, const f32x4& a1) const {
    const unsigned g = lane >> 4;
    const auto r0 = __builtin_amdgcn_permlane16_swap(cvtpk(a0[0], a0[1]), cvtpk(a1[0], a1[1]), false, false);
    const auto r1 = __builtin_amdgcn_permlane16_swap(cvtpk(a0[2], a0[3]), cvtpk(a1[2], a1[3]), false, false);
    st16_sc1((char*)(out + (size_t)(b * 192u + row + (lane & 15u)) * 1280u + p * 640u + col + (g & 1u) * 16u + (g & 2u) * 4u), u32x4{r0[0], r1[0], r0[1], r1[1]});
  }
};

__global__ __launch_bounds__(256, 1) void fused_attn(Params) {
  extern __shared__ __attribute__((aligned(16))) char lds[];
  const unsigned wid0 = rfl(threadIdx.x >> 6), bid = blockIdx.x;
  const unsigned lb = (unsigned)(size_t)lds;
  unsigned flag, samp, samp2;
  unsigned w0, w1, w2, w3, w4, l16, opb, rd0, rd1, ocb, dO, dP, iiv, rot2, om0;
  u32x4 sWO, sO; u32x2 sCnt, sOut; unsigned dC;
  u32x4 sX, sW; unsigned xd, wd, xso, wso, xm0, wm0, wme, wml, wsl, xsl, xml;
  {
    KArg ka = (KArg)__builtin_amdgcn_kernarg_segment_ptr();
    const unsigned p = PP, h = XQ * 8u + (bid >> 6) * 2u + (wid0 >> 1), L0 = threadIdx.x & 63u, r8 = L0 >> 3, g = L0 >> 4;
    sX = mksrd(ka->x[p] + (size_t)((bid >> 3) & 7u) * 192 * 640, 245760u); sW = mksrd(ka->wq[p] + h * 128u, 640u * 8192u - h * 256u);
    xd = r8 * 1280u + (((L0 & 7u) ^ (r8 & 6u)) * 16u); wd = g * 8192u + (((L0 & 15u) ^ (4u * g)) * 16u);
    xso = wid0 * 61440u; wso = (wid0 & 1u) * 131072u; xm0 = lb + VAREA + wid0 * 6144u; wm0 = lb + (wid0 >> 1) * 8192u + (wid0 & 1u) * 4096u;
    { const unsigned be = ((bid >> 3) & 1u), ga = ((bid >> 6) & 1u);
      wme = wm0 + be * 2048u; wml = wm0 + (1u - be) * 2048u; wsl = wso + (1u - be) * 65536u; wso += be * 65536u;
      xsl = xso + (1u - ga) * 30720u; xml = xm0 + (1u - ga) * 3072u; xso += ga * 30720u; xm0 += ga * 3072u; }
    asm volatile(
    "s_add_u32 m0, s65, 0xc000\n"
    "s_add_u32 s62, s64, 0x0\n"
    "buffer_load_dwordx4 v254, s[40:43], s62 offen lds\n"
    "s_add_u32 m0, s65, 0xc400\n"
    "s_add_u32 s62, s64, 0x2800\n"
    "buffer_load_dwordx4 v254, s[40:43], s62 offen lds\n"
    "s_add_u32 m0, s65, 0xc800\n"
    "s_add_u32 s62, s64, 0x5000\n"
    "buffer_load_dwordx4 v254, s[40:43], s62 offen lds\n"
    "s_add_u32 m0, s74, 0xc000\n"
    "s_add_u32 s62, s73, 0x0\n"
    "buffer_load_dwordx4 v254, s[40:43], s62 offen lds\n"
    "s_add_u32 m0, s74, 0xc400\n"
    "s_add_u32 s62, s73, 0x2800\n"
    "buffer_load_dwordx4 v254, s[40:43], s62 offen lds\n"
    "s_add_u32 m0, s74, 0xc800\n"
    "s_add_u32 s62, s73, 0x5000\n"
    "buffer_load_dwordx4 v254, s[40:43], s62 offen lds\n"
    "s_add_u32 m0, s71, 0x0\n"
    "s_add_u32 s62, s66, 0x0\n"
    "buffer_load_dwordx4 v255, s[44:47], s62 offen lds\n"
    "s_add_u32 m0, s71, 0x400\n"
    "s_add_u32 s62, s66, 0x8000\n"
    "buffer_load_dwordx4 v255, s[44:47], s62 offen lds\n"
    "s_add_u32 m0, s72, 0x0\n"
    "s_add_u32 s62, s84, 0x0\n"
    "buffer_load_dwordx4 v255, s[44:47], s62 offen lds\n"
    "s_add_u32 m0, s72, 0x400\n"
    "s_add_u32 s62, s84, 0x8000\n"
    "buffer_load_dwordx4 v255, s[44:47], s62 offen lds\n"
      :
      : "{s[40:43]}"(sX), "{s[44:47]}"(sW), "{v254}"(xd), "{v255}"(wd), "{s64}"(xso), "{s66}"(wso), "{s65}"(xm0), "{s67}"(wm0),
        "{s71}"(wme), "{s72}"(wml), "{s84}"(wsl), "{s73}"(xsl), "{s74}"(xml)
      : "s62", "scc", "memory");
  }
  {
    unsigned L = threadIdx.x & 63u; asm volatile("" : "+v"(L));
    const Geo G(wid0, bid);
    const unsigned wid = G.wid, rh = wid & 1u, j = wid >> 1, h = G.head();
    u32x4 sK = mksrd((const char*)(G.k + (size_t)h * 131072) - 896, 262144u + 896u), sV = mksrd((const char*)(G.v + (size_t)h * 131072) - 896, 262144u + 896u);
    const u32x4 sWT = mksrd((const char*)G.wo + (G.xq * 1024u + G.b * 128u + wid * 32u) * 1280u + ((G.ii * 320u) & ~127u), 31u * 1280u + 384u);
    const size_t cc = (size_t)G.cntC();
    sCnt = u32x2{rfl((unsigned)cc), rfl((unsigned)(cc >> 32))};
    sO = mksrd(G.ofr(), 5u * 8192u + 4096u); sWO = G.srd_wo();
    dO = rfl(wid * 24576u - (G.ii * 98304u + rh * 49152u + j * 4096u)); dP = rfl((unsigned)(G.ps() + (G.xq * 4u + wid) * 15360u - G.ofr()));
    { const size_t oa = (size_t)(G.out + (size_t)G.b * 192u * 1280u + G.p * 640u + G.ii * 160u); sOut = u32x2{rfl((unsigned)oa), rfl((unsigned)(oa >> 32) & 0xffffu)}; }
    dC = rfl((unsigned)(G.cntD() - G.cntC()));
    iiv = rfl(G.ii); rot2 = rfl(G.xq * 2u); om0 = rfl(lb + wid * 5120u);
    const unsigned i = L & 15u, g = L >> 4, q4 = i >> 2, pp = L & 3u;
    unsigned kr0 = lb + j * 8192u + (i >> 3) * 2048u + (i & 7u) * 128u + ((g ^ (i & 6u)) * 16u);
    unsigned kr1 = lb + j * 8192u + (i >> 3) * 2048u + (i & 7u) * 128u + (((4u + g) ^ (i & 6u)) * 16u);
    const unsigned vrb = lb + VAREA + j * 8192u + (g >> 1) * 2048u + (4u * (g & 1u) + q4) * 64u + 8u * pp;
    unsigned vr0 = vrb + (g & 1u) * 32u, vr1 = vrb + ((g & 1u) ^ 1u) * 32u;
    const unsigned r8 = L >> 3, sw8 = ((L & 7u) ^ (r8 & 6u)) * 16u;
    unsigned kd1 = r8 * 256u + sw8, kd0 = kd1 + 896u;
    const unsigned vr8 = (L >> 2) & 7u;
    unsigned vd1 = vr8 * 256u + (L >> 5) * 64u + (((L & 3u) ^ ((vr8 >> 2) << 1)) * 16u), vd0 = vd1 + 896u;
    const unsigned xrb = lb + VAREA + (12u * rh + (i >> 3)) * 1024u + (i & 7u) * 128u;
    unsigned xr0 = xrb + ((g ^ (i & 6u)) * 16u), xr1 = xrb + (((4u + g) ^ (i & 6u)) * 16u);
    const unsigned wrb = lb + j * 8192u + (8u * g + q4) * 256u + 16u * pp;
    unsigned wr0 = wrb + (q4 ^ 0u) * 64u, wr1 = wrb + (q4 ^ 1u) * 64u, wr2 = wrb + (q4 ^ 2u) * 64u, wr3 = wrb + (q4 ^ 3u) * 64u;
    unsigned phase = rfl(G.p);
    unsigned ct = wid == 0 ? ((bid >> 3) * 9u + (L < 8u ? L : 8u)) * 128u : 0u;
    unsigned vz = (unsigned)(G.cntD() - G.cntC()) - 2048u;
    const u32x2 sc = {__float_as_uint(QSCALE), __float_as_uint(QSCALE)};
    l16 = xr0; opb = wr0;
    asm volatile(
    "v_mov_b32 v238, 0\n"
    "s_getpc_b64 s[82:83]\n"
    "global_load_dword a254, v233, s[82:83]\n"
    "global_load_dword a252, v238, s[78:79] sc1\n"
    "global_load_dword a253, v227, s[78:79] offset:2048 sc1\n"
    "s_add_u32 m0, s71, 0x4000\n"
    "s_add_u32 s62, s66, 0x40000\n"
    "buffer_load_dwordx4 v255, s[44:47], s62 offen lds\n"
    "s_add_u32 m0, s71, 0x4400\n"
    "s_add_u32 s62, s66, 0x48000\n"
    "buffer_load_dwordx4 v255, s[44:47], s62 offen lds\n"
    "s_add_u32 m0, s65, 0x0\n"
    "s_add_u32 s62, s64, 0x80\n"
    "buffer_load_dwordx4 v254, s[40:43], s62 offen lds\n"
    "s_add_u32 m0, s65, 0x400\n"
    "s_add_u32 s62, s64, 0x2880\n"
    "buffer_load_dwordx4 v254, s[40:43], s62 offen lds\n"
    "s_add_u32 m0, s65, 0x800\n"
    "s_add_u32 s62, s64, 0x5080\n"
    "buffer_load_dwordx4 v254, s[40:43], s62 offen lds\n"
    "s_add_u32 m0, s72, 0x4000\n"
    "s_add_u32 s62, s84, 0x40000\n"
    "buffer_load_dwordx4 v255, s[44:47], s62 offen lds\n"
    "s_add_u32 m0, s72, 0x4400\n"
    "s_add_u32 s62, s84, 0x48000\n"
    "buffer_load_dwordx4 v255, s[44:47], s62 offen lds\n"
    "s_add_u32 m0, s71, 0x8000\n"
    "s_add_u32 s62, s66, 0x80000\n"
    "buffer_load_dwordx4 v255, s[44:47], s62 offen lds\n"
    "s_add_u32 m0, s71, 0x8400\n"
    "s_add_u32 s62, s66, 0x88000\n"
    "buffer_load_dwordx4 v255, s[44:47], s62 offen lds\n"
    "s_add_u32 m0, s71, 0xc000\n"
    "s_add_u32 s62, s66, 0xc0000\n"
    "buffer_load_dwordx4 v255, s[44:47], s62 offen lds\n"
    "v_mov_b32 v192, 0x3f803f80\n"
    "s_nop 0\n"
    "v_mbcnt_lo_u32_b32 v193, -1, 0\n"
    "v_mbcnt_hi_u32_b32 v193, -1, v193\n"
    "v_and_b32 v193, 15, v193\n"
    "v_cmp_eq_u32 vcc, 0, v193\n"
    "s_nop 1\n"
    "v_cndmask_b32 v192, 0, v192, vcc\n"
    "v_accvgpr_write_b32 a248, v192\n"
    "v_accvgpr_write_b32 a249, v192\n"
    "v_accvgpr_write_b32 a250, v192\n"
    "v_accvgpr_write_b32 a251, v192\n"
    "s_waitcnt vmcnt(13)\n"
    "s_barrier\n"
    "ds_read_b128 v[192:195], v248 offset:49152\n"
    "ds_read_b128 v[196:199], v248 offset:51200\n"
    "ds_read_b128 v[200:203], v248 offset:53248\n"
    "ds_read_b128 v[204:207], v248 offset:55296\n"
    "ds_read_b128 v[208:211], v248 offset:57344\n"
    "ds_read_b128 v[212:215], v248 offset:59392\n"
    "ds_read_b64_tr_b16 a[216:217], v250 offset:0\n"
    "ds_read_b64_tr_b16 a[218:219], v250 offset:1024\n"
    "ds_read_b64_tr_b16 a[220:221], v250 offset:8\n"
    "ds_read_b64_tr_b16 a[222:223], v250 offset:1032\n"
    "ds_read_b64_tr_b16 a[224:225], v251 offset:0\n"
    "ds_read_b64_tr_b16 a[226:227], v251 offset:1024\n"
    "ds_read_b64_tr_b16 a[228:229], v251 offset:8\n"
    "ds_read_b64_tr_b16 a[230:231], v251 offset:1032\n"
    "ds_read_b64_tr_b16 a[232:233], v252 offset:0\n"
    "ds_read_b64_tr_b16 a[234:235], v252 offset:1024\n"
    "ds_read_b64_tr_b16 a[236:237], v252 offset:8\n"
    "ds_read_b64_tr_b16 a[238:239], v252 offset:1032\n"
    "ds_read_b64_tr_b16 a[240:241], v253 offset:0\n"
    "ds_read_b64_tr_b16 a[242:243], v253 offset:1024\n"
    "s_waitcnt lgkmcnt(8)\n"
    "v_mfma_f32_16x16x32_bf16 v[0:3], a[216:219], v[192:195], 0\n"
    "ds_read_b64_tr_b16 a[244:245], v253 offset:8\n"
    "v_mfma_f32_16x16x32_bf16 v[32:35], a[216:219], v[196:199], 0\n"
    "ds_read_b64_tr_b16 a[246:247], v253 offset:1032\n"
    "v_mfma_f32_16x16x32_bf16 v[64:67], a[216:219], v[200:203], 0\n"
    "s_add_u32 m0, s71, 0xc400\n"
    "v_mfma_f32_16x16x32_bf16 v[96:99], a[216:219], v[204:207], 0\n"
    "s_add_u32 s62, s66, 0xc8000\n"
    "v_mfma_f32_16x16x32_bf16 v[128:131], a[216:219], v[208:211], 0\n"
    "buffer_load_dwordx4 v255, s[44:47], s62 offen lds\n"
    "v_mfma_f32_16x16x32_bf16 v[160:163], a[216:219], v[212:215], 0\n"
    "v_mfma_f32_16x16x32_bf16 v[4:7], a[220:223], v[192:195], 0\n"
    "s_add_u32 m0, s74, 0x0\n"
    "v_mfma_f32_16x16x32_bf16 v[36:39], a[220:223], v[196:199], 0\n"
    "s_add_u32 s62, s73, 0x80\n"
    "v_mfma_f32_16x16x32_bf16 v[68:71], a[220:223], v[200:203], 0\n"
    "buffer_load_dwordx4 v254, s[40:43], s62 offen lds\n"
    "v_mfma_f32_16x16x32_bf16 v[100:103], a[220:223], v[204:207], 0\n"
    "v_mfma_f32_16x16x32_bf16 v[132:135], a[220:223], v[208:211], 0\n"
    "s_waitcnt vmcnt(5) lgkmcnt(0)\n"
    "s_barrier\n"
    "v_mfma_f32_16x16x32_bf16 v[164:167], a[220:223], v[212:215], 0\n"
    "ds_read_b64_tr_b16 a[216:217], v250 offset:16384\n"
    "s_add_u32 m0, s74, 0x400\n"
    "s_add_u32 s62, s73, 0x2880\n"
    "v_mfma_f32_16x16x32_bf16 v[8:11], a[224:227], v[192:195], 0\n"
    "buffer_load_dwordx4 v254, s[40:43], s62 offen lds\n"
    "v_mfma_f32_16x16x32_bf16 v[40:43], a[224:227], v[196:199], 0\n"
    "ds_read_b64_tr_b16 a[218:219], v250 offset:17408\n"
    "v_mfma_f32_16x16x32_bf16 v[72:75], a[224:227], v[200:203], 0\n"
    "ds_read_b64_tr_b16 a[220:221], v250 offset:16392\n"
    "s_add_u32 m0, s74, 0x800\n"
    "v_mfma_f32_16x16x32_bf16 v[104:107], a[224:227], v[204:207], 0\n"
    "ds_read_b64_tr_b16 a[222:223], v250 offset:17416\n"
    "s_add_u32 s62, s73, 0x5080\n"
    "v_mfma_f32_16x16x32_bf16 v[136:139], a[224:227], v[208:211], 0\n"
    "buffer_load_dwordx4 v254, s[40:43], s62 offen lds\n"
    "v_mfma_f32_16x16x32_bf16 v[168:171], a[224:227], v[212:215], 0\n"
    "ds_read_b128 v[216:219], v249 offset:49152\n"
    "v_mfma_f32_16x16x32_bf16 v[12:15], a[228:231], v[192:195], 0\n"
    "ds_read_b128 v[220:223], v249 offset:51200\n"
    "s_add_u32 m0, s72, 0x8000\n"
    "v_mfma_f32_16x16x32_bf16 v[44:47], a[228:231], v[196:199], 0\n"
    "ds_read_b128 v[224:227], v249 offset:53248\n"
    "s_add_u32 s62, s84, 0x80000\n"
    "v_mfma_f32_16x16x32_bf16 v[76:79], a[228:231], v[200:203], 0\n"
    "buffer_load_dwordx4 v255, s[44:47], s62 offen lds\n"
    "v_mfma_f32_16x16x32_bf16 v[108:111], a[228:231], v[204:207], 0\n"
    "ds_read_b128 v[228:231], v249 offset:55296\n"
    "v_mfma_f32_16x16x32_bf16 v[140:143], a[228:231], v[208:211], 0\n"
    "ds_read_b128 v[232:235], v249 offset:57344\n"
    "s_add_u32 m0, s72, 0x8400\n"
    "v_mfma_f32_16x16x32_bf16 v[172:175], a[228:231], v[212:215], 0\n"
    "ds_read_b128 v[236:239], v249 offset:59392\n"
    "s_add_u32 s62, s84, 0x88000\n"
    "v_mfma_f32_16x16x32_bf16 v[16:19], a[232:235], v[192:195], 0\n"
    "buffer_load_dwordx4 v255, s[44:47], s62 offen lds\n"
    "v_mfma_f32_16x16x32_bf16 v[48:51], a[232:235], v[196:199], 0\n"
    "ds_read_b64_tr_b16 a[224:225], v251 offset:16384\n"
    "v_mfma_f32_16x16x32_bf16 v[80:83], a[232:235], v[200:203], 0\n"
    "ds_read_b64_tr_b16 a[226:227], v251 offset:17408\n"
    "s_add_u32 m0, s71, 0x10000\n"
    "v_mfma_f32_16x16x32_bf16 v[112:115], a[232:235], v[204:207], 0\n"
    "ds_read_b64_tr_b16 a[228:229], v251 offset:16392\n"
    "s_add_u32 s62, s66, 0x100000\n"
    "v_mfma_f32_16x16x32_bf16 v[144:147], a[232:235], v[208:211], 0\n"
    "buffer_load_dwordx4 v255, s[44:47], s62 offen lds\n"
    "v_mfma_f32_16x16x32_bf16 v[176:179], a[232:235], v[212:215], 0\n"
    "ds_read_b64_tr_b16 a[230:231], v251 offset:17416\n"
    "v_mfma_f32_16x16x32_bf16 v[20:23], a[236:239], v[192:195], 0\n"
    "ds_read_b64_tr_b16 a[232:233], v252 offset:16384\n"
    "v_mfma_f32_16x16x32_bf16 v[52:55], a[236:239], v[196:199], 0\n"
    "ds_read_b64_tr_b16 a[234:235], v252 offset:17408\n"
    "s_add_u32 m0, s71, 0x10400\n"
    "v_mfma_f32_16x16x32_bf16 v[84:87], a[236:239], v[200:203], 0\n"
    "s_add_u32 s62, s66, 0x108000\n"
    "v_mfma_f32_16x16x32_bf16 v[116:119], a[236:239], v[204:207], 0\n"
    "buffer_load_dwordx4 v255, s[44:47], s62 offen lds\n"
    "v_mfma_f32_16x16x32_bf16 v[148:151], a[236:239], v[208:211], 0\n"
    "s_add_u32 m0, s65, 0x6000\n"
    "v_mfma_f32_16x16x32_bf16 v[180:183], a[236:239], v[212:215], 0\n"
    "s_add_u32 s62, s64, 0x100\n"
    "v_mfma_f32_16x16x32_bf16 v[24:27], a[240:243], v[192:195], 0\n"
    "buffer_load_dwordx4 v254, s[40:43], s62 offen lds\n"
    "v_mfma_f32_16x16x32_bf16 v[56:59], a[240:243], v[196:199], 0\n"
    "ds_read_b64_tr_b16 a[236:237], v252 offset:16392\n"
    "v_mfma_f32_16x16x32_bf16 v[88:91], a[240:243], v[200:203], 0\n"
    "ds_read_b64_tr_b16 a[238:239], v252 offset:17416\n"
    "s_add_u32 m0, s65, 0x6400\n"
    "v_mfma_f32_16x16x32_bf16 v[120:123], a[240:243], v[204:207], 0\n"
    "s_add_u32 s62, s64, 0x2900\n"
    "v_mfma_f32_16x16x32_bf16 v[152:155], a[240:243], v[208:211], 0\n"
    "buffer_load_dwordx4 v254, s[40:43], s62 offen lds\n"
    "v_mfma_f32_16x16x32_bf16 v[184:187], a[240:243], v[212:215], 0\n"
    "v_mfma_f32_16x16x32_bf16 v[28:31], a[244:247], v[192:195], 0\n"
    "ds_read_b64_tr_b16 a[240:241], v253 offset:16384\n"
    "v_mfma_f32_16x16x32_bf16 v[60:63], a[244:247], v[196:199], 0\n"
    "ds_read_b64_tr_b16 a[242:243], v253 offset:17408\n"
    "s_add_u32 m0, s65, 0x6800\n"
    "v_mfma_f32_16x16x32_bf16 v[92:95], a[244:247], v[200:203], 0\n"
    "s_add_u32 s62, s64, 0x5100\n"
    "v_mfma_f32_16x16x32_bf16 v[124:127], a[244:247], v[204:207], 0\n"
    "buffer_load_dwordx4 v254, s[40:43], s62 offen lds\n"
    "v_mfma_f32_16x16x32_bf16 v[156:159], a[244:247], v[208:211], 0\n"
    "v_mfma_f32_16x16x32_bf16 v[188:191], a[244:247], v[212:215], 0\n"
    "s_waitcnt lgkmcnt(8)\n"
    "v_mfma_f32_16x16x32_bf16 v[0:3], a[216:219], v[216:219], v[0:3]\n"
    "ds_read_b64_tr_b16 a[244:245], v253 offset:16392\n"
    "v_mfma_f32_16x16x32_bf16 v[32:35], a[216:219], v[220:223], v[32:35]\n"
    "ds_read_b64_tr_b16 a[246:247], v253 offset:17416\n"
    "v_mfma_f32_16x16x32_bf16 v[64:67], a[216:219], v[224:227], v[64:67]\n"
    "s_add_u32 m0, s72, 0xc000\n"
    "v_mfma_f32_16x16x32_bf16 v[96:99], a[216:219], v[228:231], v[96:99]\n"
    "s_add_u32 s62, s84, 0xc0000\n"
    "v_mfma_f32_16x16x32_bf16 v[128:131], a[216:219], v[232:235], v[128:131]\n"
    "buffer_load_dwordx4 v255, s[44:47], s62 offen lds\n"
    "v_mfma_f32_16x16x32_bf16 v[160:163], a[216:219], v[236:239], v[160:163]\n"
    "v_mfma_f32_16x16x32_bf16 v[4:7], a[220:223], v[216:219], v[4:7]\n"
    "s_add_u32 m0, s72, 0xc400\n"
    "v_mfma_f32_16x16x32_bf16 v[36:39], a[220:223], v[220:223], v[36:39]\n"
    "s_add_u32 s62, s84, 0xc8000\n"
    "v_mfma_f32_16x16x32_bf16 v[68:71], a[220:223], v[224:227], v[68:71]\n"
    "buffer_load_dwordx4 v255, s[44:47], s62 offen lds\n"
    "v_mfma_f32_16x16x32_bf16 v[100:103], a[220:223], v[228:231], v[100:103]\n"
    "v_mfma_f32_16x16x32_bf16 v[132:135], a[220:223], v[232:235], v[132:135]\n"
    "s_waitcnt vmcnt(7) lgkmcnt(0)\n"
    "s_barrier\n"
    "v_mfma_f32_16x16x32_bf16 v[164:167], a[220:223], v[236:239], v[164:167]\n"
    "ds_read_b64_tr_b16 a[216:217], v250 offset:32768\n"
    "s_add_u32 m0, s71, 0x0\n"
    "s_add_u32 s62, s66, 0x140000\n"
    "v_mfma_f32_16x16x32_bf16 v[8:11], a[224:227], v[216:219], v[8:11]\n"
    "buffer_load_dwordx4 v255, s[44:47], s62 offen lds\n"
    "v_mfma_f32_16x16x32_bf16 v[40:43], a[224:227], v[220:223], v[40:43]\n"
    "ds_read_b64_tr_b16 a[218:219], v250 offset:33792\n"
    "v_mfma_f32_16x16x32_bf16 v[72:75], a[224:227], v[224:227], v[72:75]\n"
    "ds_read_b64_tr_b16 a[220:221], v250 offset:32776\n"
    "s_add_u32 m0, s71, 0x400\n"
    "v_mfma_f32_16x16x32_bf16 v[104:107], a[224:227], v[228:231], v[104:107]\n"
    "ds_read_b64_tr_b16 a[222:223], v250 offset:33800\n"
    "s_add_u32 s62, s66, 0x148000\n"
    "v_mfma_f32_16x16x32_bf16 v[136:139], a[224:227], v[232:235], v[136:139]\n"
    "buffer_load_dwordx4 v255, s[44:47], s62 offen lds\n"
    "v_mfma_f32_16x16x32_bf16 v[168:171], a[224:227], v[236:239], v[168:171]\n"
    "ds_read_b128 v[192:195], v248 offset:0\n"
    "v_mfma_f32_16x16x32_bf16 v[12:15], a[228:231], v[216:219], v[12:15]\n"
    "ds_read_b128 v[196:199], v248 offset:2048\n"
    "s_add_u32 m0, s72, 0x10000\n"
    "v_mfma_f32_16x16x32_bf16 v[44:47], a[228:231], v[220:223], v[44:47]\n"
    "ds_read_b128 v[200:203], v248 offset:4096\n"
    "s_add_u32 s62, s84, 0x100000\n"
    "v_mfma_f32_16x16x32_bf16 v[76:79], a[228:231], v[224:227], v[76:79]\n"
    "buffer_load_dwordx4 v255, s[44:47], s62 offen lds\n"
    "v_mfma_f32_16x16x32_bf16 v[108:111], a[228:231], v[228:231], v[108:111]\n"
    "ds_read_b128 v[204:207], v248 offset:6144\n"
    "v_mfma_f32_16x16x32_bf16 v[140:143], a[228:231], v[232:235], v[140:143]\n"
    "ds_read_b128 v[208:211], v248 offset:8192\n"
    "s_add_u32 m0, s72, 0x10400\n"
    "v_mfma_f32_16x16x32_bf16 v[172:175], a[228:231], v[236:239], v[172:175]\n"
    "ds_read_b128 v[212:215], v248 offset:10240\n"
    "s_add_u32 s62, s84, 0x108000\n"
    "v_mfma_f32_16x16x32_bf16 v[16:19], a[232:235], v[216:219], v[16:19]\n"
    "buffer_load_dwordx4 v255, s[44:47], s62 offen lds\n"
    "v_mfma_f32_16x16x32_bf16 v[48:51], a[232:235], v[220:223], v[48:51]\n"
    "ds_read_b64_tr_b16 a[224:225], v251 offset:32768\n"
    "v_mfma_f32_16x16x32_bf16 v[80:83], a[232:235], v[224:227], v[80:83]\n"
    "ds_read_b64_tr_b16 a[226:227], v251 offset:33792\n"
    "s_add_u32 m0, s71, 0x4000\n"
    "v_mfma_f32_16x16x32_bf16 v[112:115], a[232:235], v[228:231], v[112:115]\n"
    "ds_read_b64_tr_b16 a[228:229], v251 offset:32776\n"
    "s_add_u32 s62, s66, 0x180000\n"
    "v_mfma_f32_16x16x32_bf16 v[144:147], a[232:235], v[232:235], v[144:147]\n"
    "buffer_load_dwordx4 v255, s[44:47], s62 offen lds\n"
    "v_mfma_f32_16x16x32_bf16 v[176:179], a[232:235], v[236:239], v[176:179]\n"
    "ds_read_b64_tr_b16 a[230:231], v251 offset:33800\n"
    "v_mfma_f32_16x16x32_bf16 v[20:23], a[236:239], v[216:219], v[20:23]\n"
    "ds_read_b64_tr_b16 a[232:233], v252 offset:32768\n"
    "v_mfma_f32_16x16x32_bf16 v[52:55], a[236:239], v[220:223], v[52:55]\n"
    "ds_read_b64_tr_b16 a[234:235], v252 offset:33792\n"
    "s_add_u32 m0, s71, 0x4400\n"
    "v_mfma_f32_16x16x32_bf16 v[84:87], a[236:239], v[224:227], v[84:87]\n"
    "s_add_u32 s62, s66, 0x188000\n"
    "v_mfma_f32_16x16x32_bf16 v[116:119], a[236:239], v[228:231], v[116:119]\n"
    "buffer_load_dwordx4 v255, s[44:47], s62 offen lds\n"
    "v_mfma_f32_16x16x32_bf16 v[148:151], a[236:239], v[232:235], v[148:151]\n"
    "s_add_u32 m0, s74, 0x6000\n"
    "v_mfma_f32_16x16x32_bf16 v[180:183], a[236:239], v[236:239], v[180:183]\n"
    "s_add_u32 s62, s73, 0x100\n"
    "v_mfma_f32_16x16x32_bf16 v[24:27], a[240:243], v[216:219], v[24:27]\n"
    "buffer_load_dwordx4 v254, s[40:43], s62 offen lds\n"
    "v_mfma_f32_16x16x32_bf16 v[56:59], a[240:243], v[220:223], v[56:59]\n"
    "ds_read_b64_tr_b16 a[236:237], v252 offset:32776\n"
    "v_mfma_f32_16x16x32_bf16 v[88:91], a[240:243], v[224:227], v[88:91]\n"
    "ds_read_b64_tr_b16 a[238:239], v252 offset:33800\n"
    "s_add_u32 m0, s74, 0x6400\n"
    "v_mfma_f32_16x16x32_bf16 v[120:123], a[240:243], v[228:231], v[120:123]\n"
    "s_add_u32 s62, s73, 0x2900\n"
    "v_mfma_f32_16x16x32_bf16 v[152:155], a[240:243], v[232:235], v[152:155]\n"
    "buffer_load_dwordx4 v254, s[40:43], s62 offen lds\n"
    "v_mfma_f32_16x16x32_bf16 v[184:187], a[240:243], v[236:239], v[184:187]\n"
    "v_mfma_f32_16x16x32_bf16 v[28:31], a[244:247], v[216:219], v[28:31]\n"
    "ds_read_b64_tr_b16 a[240:241], v253 offset:32768\n"
    "v_mfma_f32_16x16x32_bf16 v[60:63], a[244:247], v[220:223], v[60:63]\n"
    "ds_read_b64_tr_b16 a[242:243], v253 offset:33792\n"
    "s_add_u32 m0, s74, 0x6800\n"
    "v_mfma_f32_16x16x32_bf16 v[92:95], a[244:247], v[224:227], v[92:95]\n"
    "s_add_u32 s62, s73, 0x5100\n"
    "v_mfma_f32_16x16x32_bf16 v[124:127], a[244:247], v[228:231], v[124:127]\n"
    "buffer_load_dwordx4 v254, s[40:43], s62 offen lds\n"
    "v_mfma_f32_16x16x32_bf16 v[156:159], a[244:247], v[232:235], v[156:159]\n"
    "v_mfma_f32_16x16x32_bf16 v[188:191], a[244:247], v[236:239], v[188:191]\n"
    "s_waitcnt lgkmcnt(8)\n"
    "v_mfma_f32_16x16x32_bf16 v[0:3], a[216:219], v[192:195], v[0:3]\n"
    "ds_read_b64_tr_b16 a[244:245], v253 offset:32776\n"
    "v_mfma_f32_16x16x32_bf16 v[32:35], a[216:219], v[196:199], v[32:35]\n"
    "ds_read_b64_tr_b16 a[246:247], v253 offset:33800\n"
    "v_mfma_f32_16x16x32_bf16 v[64:67], a[216:219], v[200:203], v[64:67]\n"
    "s_add_u32 m0, s65, 0xc000\n"
    "v_mfma_f32_16x16x32_bf16 v[96:99], a[216:219], v[204:207], v[96:99]\n"
    "s_add_u32 s62, s64, 0x180\n"
    "v_mfma_f32_16x16x32_bf16 v[128:131], a[216:219], v[208:211], v[128:131]\n"
    "buffer_load_dwordx4 v254, s[40:43], s62 offen lds\n"
    "v_mfma_f32_16x16x32_bf16 v[160:163], a[216:219], v[212:215], v[160:163]\n"
    "v_mfma_f32_16x16x32_bf16 v[4:7], a[220:223], v[192:195], v[4:7]\n"
    "s_add_u32 m0, s65, 0xc400\n"
    "v_mfma_f32_16x16x32_bf16 v[36:39], a[220:223], v[196:199], v[36:39]\n"
    "s_add_u32 s62, s64, 0x2980\n"
    "v_mfma_f32_16x16x32_bf16 v[68:71], a[220:223], v[200:203], v[68:71]\n"
    "buffer_load_dwordx4 v254, s[40:43], s62 offen lds\n"
    "v_mfma_f32_16x16x32_bf16 v[100:103], a[220:223], v[204:207], v[100:103]\n"
    "v_mfma_f32_16x16x32_bf16 v[132:135], a[220:223], v[208:211], v[132:135]\n"
    "s_waitcnt vmcnt(11) lgkmcnt(0)\n"
    "s_barrier\n"
    "v_mfma_f32_16x16x32_bf16 v[164:167], a[220:223], v[212:215], v[164:167]\n"
    "ds_read_b64_tr_b16 a[216:217], v250 offset:49152\n"
    "s_add_u32 m0, s65, 0xc800\n"
    "s_add_u32 s62, s64, 0x5180\n"
    "v_mfma_f32_16x16x32_bf16 v[8:11], a[224:227], v[192:195], v[8:11]\n"
    "buffer_load_dwordx4 v254, s[40:43], s62 offen lds\n"
    "v_mfma_f32_16x16x32_bf16 v[40:43], a[224:227], v[196:199], v[40:43]\n"
    "ds_read_b64_tr_b16 a[218:219], v250 offset:50176\n"
    "v_mfma_f32_16x16x32_bf16 v[72:75], a[224:227], v[200:203], v[72:75]\n"
    "ds_read_b64_tr_b16 a[220:221], v250 offset:49160\n"
    "s_add_u32 m0, s72, 0x0\n"
    "v_mfma_f32_16x16x32_bf16 v[104:107], a[224:227], v[204:207], v[104:107]\n"
    "ds_read_b64_tr_b16 a[222:223], v250 offset:50184\n"
    "s_add_u32 s62, s84, 0x140000\n"
    "v_mfma_f32_16x16x32_bf16 v[136:139], a[224:227], v[208:211], v[136:139]\n"
    "buffer_load_dwordx4 v255, s[44:47], s62 offen lds\n"
    "v_mfma_f32_16x16x32_bf16 v[168:171], a[224:227], v[212:215], v[168:171]\n"
    "ds_read_b128 v[216:219], v249 offset:0\n"
    "v_mfma_f32_16x16x32_bf16 v[12:15], a[228:231], v[192:195], v[12:15]\n"
    "ds_read_b128 v[220:223], v249 offset:2048\n"
    "s_add_u32 m0, s72, 0x400\n"
    "v_mfma_f32_16x16x32_bf16 v[44:47], a[228:231], v[196:199], v[44:47]\n"
    "ds_read_b128 v[224:227], v249 offset:4096\n"
    "s_add_u32 s62, s84, 0x148000\n"
    "v_mfma_f32_16x16x32_bf16 v[76:79], a[228:231], v[200:203], v[76:79]\n"
    "buffer_load_dwordx4 v255, s[44:47], s62 offen lds\n"
    "v_mfma_f32_16x16x32_bf16 v[108:111], a[228:231], v[204:207], v[108:111]\n"
    "ds_read_b128 v[228:231], v249 offset:6144\n"
    "v_mfma_f32_16x16x32_bf16 v[140:143], a[228:231], v[208:211], v[140:143]\n"
    "ds_read_b128 v[232:235], v249 offset:8192\n"
    "s_add_u32 m0, s71, 0x8000\n"
    "v_mfma_f32_16x16x32_bf16 v[172:175], a[228:231], v[212:215], v[172:175]\n"
    "ds_read_b128 v[236:239], v249 offset:10240\n"
    "s_add_u32 s62, s66, 0x1c0000\n"
    "v_mfma_f32_16x16x32_bf16 v[16:19], a[232:235], v[192:195], v[16:19]\n"
    "buffer_load_dwordx4 v255, s[44:47], s62 offen lds\n"
    "v_mfma_f32_16x16x32_bf16 v[48:51], a[232:235], v[196:199], v[48:51]\n"
    "ds_read_b64_tr_b16 a[224:225], v251 offset:49152\n"
    "v_mfma_f32_16x16x32_bf16 v[80:83], a[232:235], v[200:203], v[80:83]\n"
    "ds_read_b64_tr_b16 a[226:227], v251 offset:50176\n"
    "s_add_u32 m0, s71, 0x8400\n"
    "v_mfma_f32_16x16x32_bf16 v[112:115], a[232:235], v[204:207], v[112:115]\n"
    "ds_read_b64_tr_b16 a[228:229], v251 offset:49160\n"
    "s_add_u32 s62, s66, 0x1c8000\n"
    "v_mfma_f32_16x16x32_bf16 v[144:147], a[232:235], v[208:211], v[144:147]\n"
    "buffer_load_dwordx4 v255, s[44:47], s62 offen lds\n"
    "v_mfma_f32_16x16x32_bf16 v[176:179], a[232:235], v[212:215], v[176:179]\n"
    "ds_read_b64_tr_b16 a[230:231], v251 offset:50184\n"
    "v_mfma_f32_16x16x32_bf16 v[20:23], a[236:239], v[192:195], v[20:23]\n"
    "ds_read_b64_tr_b16 a[232:233], v252 offset:49152\n"
    "v_mfma_f32_16x16x32_bf16 v[52:55], a[236:239], v[196:199], v[52:55]\n"
    "ds_read_b64_tr_b16 a[234:235], v252 offset:50176\n"
    "v_mfma_f32_16x16x32_bf16 v[84:87], a[236:239], v[200:203], v[84:87]\n"
    "v_mfma_f32_16x16x32_bf16 v[116:119], a[236:239], v[204:207], v[116:119]\n"
    "v_mfma_f32_16x16x32_bf16 v[148:151], a[236:239], v[208:211], v[148:151]\n"
    "v_mfma_f32_16x16x32_bf16 v[180:183], a[236:239], v[212:215], v[180:183]\n"
    "v_mfma_f32_16x16x32_bf16 v[24:27], a[240:243], v[192:195], v[24:27]\n"
    "v_mfma_f32_16x16x32_bf16 v[56:59], a[240:243], v[196:199], v[56:59]\n"
    "ds_read_b64_tr_b16 a[236:237], v252 offset:49160\n"
    "v_mfma_f32_16x16x32_bf16 v[88:91], a[240:243], v[200:203], v[88:91]\n"
    "ds_read_b64_tr_b16 a[238:239], v252 offset:50184\n"
    "v_mfma_f32_16x16x32_bf16 v[120:123], a[240:243], v[204:207], v[120:123]\n"
    "v_mfma_f32_16x16x32_bf16 v[152:155], a[240:243], v[208:211], v[152:155]\n"
    "v_mfma_f32_16x16x32_bf16 v[184:187], a[240:243], v[212:215], v[184:187]\n"
    "v_mfma_f32_16x16x32_bf16 v[28:31], a[244:247], v[192:195], v[28:31]\n"
    "ds_read_b64_tr_b16 a[240:241], v253 offset:49152\n"
    "v_mfma_f32_16x16x32_bf16 v[60:63], a[244:247], v[196:199], v[60:63]\n"
    "ds_read_b64_tr_b16 a[242:243], v253 offset:50176\n"
    "v_mfma_f32_16x16x32_bf16 v[92:95], a[244:247], v[200:203], v[92:95]\n"
    "v_mfma_f32_16x16x32_bf16 v[124:127], a[244:247], v[204:207], v[124:127]\n"
    "v_mfma_f32_16x16x32_bf16 v[156:159], a[244:247], v[208:211], v[156:159]\n"
    "v_mfma_f32_16x16x32_bf16 v[188:191], a[244:247], v[212:215], v[188:191]\n"
    "s_waitcnt lgkmcnt(8)\n"
    "v_mfma_f32_16x16x32_bf16 v[0:3], a[216:219], v[216:219], v[0:3]\n"
    "ds_read_b64_tr_b16 a[244:245], v253 offset:49160\n"
    "v_mfma_f32_16x16x32_bf16 v[32:35], a[216:219], v[220:223], v[32:35]\n"
    "ds_read_b64_tr_b16 a[246:247], v253 offset:50184\n"
    "v_mfma_f32_16x16x32_bf16 v[64:67], a[216:219], v[224:227], v[64:67]\n"
    "v_mfma_f32_16x16x32_bf16 v[96:99], a[216:219], v[228:231], v[96:99]\n"
    "v_mfma_f32_16x16x32_bf16 v[128:131], a[216:219], v[232:235], v[128:131]\n"
    "v_mfma_f32_16x16x32_bf16 v[160:163], a[216:219], v[236:239], v[160:163]\n"
    "v_add_u32 v250, 0x10000, v250\n"
    "v_add_u32 v251, 0x10000, v251\n"
    "v_mfma_f32_16x16x32_bf16 v[4:7], a[220:223], v[216:219], v[4:7]\n"
    "v_mfma_f32_16x16x32_bf16 v[36:39], a[220:223], v[220:223], v[36:39]\n"
    "v_mfma_f32_16x16x32_bf16 v[68:71], a[220:223], v[224:227], v[68:71]\n"
    "v_mfma_f32_16x16x32_bf16 v[100:103], a[220:223], v[228:231], v[100:103]\n"
    "v_add_u32 v252, 0x10000, v252\n"
    "v_add_u32 v253, 0x10000, v253\n"
    "v_mfma_f32_16x16x32_bf16 v[132:135], a[220:223], v[232:235], v[132:135]\n"
    "s_waitcnt vmcnt(7) lgkmcnt(0)\n"
    "s_barrier\n"
    "v_mfma_f32_16x16x32_bf16 v[164:167], a[220:223], v[236:239], v[164:167]\n"
    "ds_read_b64_tr_b16 a[216:217], v250 offset:0\n"
    "s_add_u32 m0, s72, 0x4000\n"
    "s_add_u32 s62, s84, 0x180000\n"
    "v_mfma_f32_16x16x32_bf16 v[8:11], a[224:227], v[216:219], v[8:11]\n"
    "buffer_load_dwordx4 v255, s[44:47], s62 offen lds\n"
    "v_mfma_f32_16x16x32_bf16 v[40:43], a[224:227], v[220:223], v[40:43]\n"
    "ds_read_b64_tr_b16 a[218:219], v250 offset:1024\n"
    "v_mfma_f32_16x16x32_bf16 v[72:75], a[224:227], v[224:227], v[72:75]\n"
    "ds_read_b64_tr_b16 a[220:221], v250 offset:8\n"
    "s_add_u32 m0, s72, 0x4400\n"
    "v_mfma_f32_16x16x32_bf16 v[104:107], a[224:227], v[228:231], v[104:107]\n"
    "ds_read_b64_tr_b16 a[222:223], v250 offset:1032\n"
    "s_add_u32 s62, s84, 0x188000\n"
    "v_mfma_f32_16x16x32_bf16 v[136:139], a[224:227], v[232:235], v[136:139]\n"
    "buffer_load_dwordx4 v255, s[44:47], s62 offen lds\n"
    "v_mfma_f32_16x16x32_bf16 v[168:171], a[224:227], v[236:239], v[168:171]\n"
    "ds_read_b128 v[192:195], v248 offset:24576\n"
    "v_mfma_f32_16x16x32_bf16 v[12:15], a[228:231], v[216:219], v[12:15]\n"
    "ds_read_b128 v[196:199], v248 offset:26624\n"
    "s_add_u32 m0, s71, 0xc000\n"
    "v_mfma_f32_16x16x32_bf16 v[44:47], a[228:231], v[220:223], v[44:47]\n"
    "ds_read_b128 v[200:203], v248 offset:28672\n"
    "s_add_u32 s62, s66, 0x200000\n"
    "v_mfma_f32_16x16x32_bf16 v[76:79], a[228:231], v[224:227], v[76:79]\n"
    "buffer_load_dwordx4 v255, s[44:47], s62 offen lds\n"
    "v_mfma_f32_16x16x32_bf16 v[108:111], a[228:231], v[228:231], v[108:111]\n"
    "ds_read_b128 v[204:207], v248 offset:30720\n"
    "v_mfma_f32_16x16x32_bf16 v[140:143], a[228:231], v[232:235], v[140:143]\n"
    "ds_read_b128 v[208:211], v248 offset:32768\n"
    "s_add_u32 m0, s71, 0xc400\n"
    "v_mfma_f32_16x16x32_bf16 v[172:175], a[228:231], v[236:239], v[172:175]\n"
    "ds_read_b128 v[212:215], v248 offset:34816\n"
    "s_add_u32 s62, s66, 0x208000\n"
    "v_mfma_f32_16x16x32_bf16 v[16:19], a[232:235], v[216:219], v[16:19]\n"
    "buffer_load_dwordx4 v255, s[44:47], s62 offen lds\n"
    "v_mfma_f32_16x16x32_bf16 v[48:51], a[232:235], v[220:223], v[48:51]\n"
    "ds_read_b64_tr_b16 a[224:225], v251 offset:0\n"
    "v_mfma_f32_16x16x32_bf16 v[80:83], a[232:235], v[224:227], v[80:83]\n"
    "ds_read_b64_tr_b16 a[226:227], v251 offset:1024\n"
    "s_add_u32 m0, s74, 0xc000\n"
    "v_mfma_f32_16x16x32_bf16 v[112:115], a[232:235], v[228:231], v[112:115]\n"
    "ds_read_b64_tr_b16 a[228:229], v251 offset:8\n"
    "s_add_u32 s62, s73, 0x180\n"
    "v_mfma_f32_16x16x32_bf16 v[144:147], a[232:235], v[232:235], v[144:147]\n"
    "buffer_load_dwordx4 v254, s[40:43], s62 offen lds\n"
    "v_mfma_f32_16x16x32_bf16 v[176:179], a[232:235], v[236:239], v[176:179]\n"
    "ds_read_b64_tr_b16 a[230:231], v251 offset:1032\n"
    "v_mfma_f32_16x16x32_bf16 v[20:23], a[236:239], v[216:219], v[20:23]\n"
    "ds_read_b64_tr_b16 a[232:233], v252 offset:0\n"
    "v_mfma_f32_16x16x32_bf16 v[52:55], a[236:239], v[220:223], v[52:55]\n"
    "ds_read_b64_tr_b16 a[234:235], v252 offset:1024\n"
    "s_add_u32 m0, s74, 0xc400\n"
    "v_mfma_f32_16x16x32_bf16 v[84:87], a[236:239], v[224:227], v[84:87]\n"
    "s_add_u32 s62, s73, 0x2980\n"
    "v_mfma_f32_16x16x32_bf16 v[116:119], a[236:239], v[228:231], v[116:119]\n"
    "buffer_load_dwordx4 v254, s[40:43], s62 offen lds\n"
    "v_mfma_f32_16x16x32_bf16 v[148:151], a[236:239], v[232:235], v[148:151]\n"
    "s_add_u32 m0, s74, 0xc800\n"
    "v_mfma_f32_16x16x32_bf16 v[180:183], a[236:239], v[236:239], v[180:183]\n"
    "s_add_u32 s62, s73, 0x5180\n"
    "v_mfma_f32_16x16x32_bf16 v[24:27], a[240:243], v[216:219], v[24:27]\n"
    "buffer_load_dwordx4 v254, s[40:43], s62 offen lds\n"
    "v_mfma_f32_16x16x32_bf16 v[56:59], a[240:243], v[220:223], v[56:59]\n"
    "ds_read_b64_tr_b16 a[236:237], v252 offset:8\n"
    "v_mfma_f32_16x16x32_bf16 v[88:91], a[240:243], v[224:227], v[88:91]\n"
    "ds_read_b64_tr_b16 a[238:239], v252 offset:1032\n"
    "s_add_u32 m0, s65, 0x0\n"
    "v_mfma_f32_16x16x32_bf16 v[120:123], a[240:243], v[228:231], v[120:123]\n"
    "s_add_u32 s62, s64, 0x200\n"
    "v_mfma_f32_16x16x32_bf16 v[152:155], a[240:243], v[232:235], v[152:155]\n"
    "buffer_load_dwordx4 v254, s[40:43], s62 offen lds\n"
    "v_mfma_f32_16x16x32_bf16 v[184:187], a[240:243], v[236:239], v[184:187]\n"
    "v_mfma_f32_16x16x32_bf16 v[28:31], a[244:247], v[216:219], v[28:31]\n"
    "ds_read_b64_tr_b16 a[240:241], v253 offset:0\n"
    "v_mfma_f32_16x16x32_bf16 v[60:63], a[244:247], v[220:223], v[60:63]\n"
    "ds_read_b64_tr_b16 a[242:243], v253 offset:1024\n"
    "s_add_u32 m0, s65, 0x400\n"
    "v_mfma_f32_16x16x32_bf16 v[92:95], a[244:247], v[224:227], v[92:95]\n"
    "s_add_u32 s62, s64, 0x2a00\n"
    "v_mfma_f32_16x16x32_bf16 v[124:127], a[244:247], v[228:231], v[124:127]\n"
    "buffer_load_dwordx4 v254, s[40:43], s62 offen lds\n"
    "v_mfma_f32_16x16x32_bf16 v[156:159], a[244:247], v[232:235], v[156:159]\n"
    "v_mfma_f32_16x16x32_bf16 v[188:191], a[244:247], v[236:239], v[188:191]\n"
    "s_waitcnt lgkmcnt(8)\n"
    "v_mfma_f32_16x16x32_bf16 v[0:3], a[216:219], v[192:195], v[0:3]\n"
    "ds_read_b64_tr_b16 a[244:245], v253 offset:8\n"
    "v_mfma_f32_16x16x32_bf16 v[32:35], a[216:219], v[196:199], v[32:35]\n"
    "ds_read_b64_tr_b16 a[246:247], v253 offset:1032\n"
    "v_mfma_f32_16x16x32_bf16 v[64:67], a[216:219], v[200:203], v[64:67]\n"
    "s_add_u32 m0, s65, 0x800\n"
    "v_mfma_f32_16x16x32_bf16 v[96:99], a[216:219], v[204:207], v[96:99]\n"
    "s_add_u32 s62, s64, 0x5200\n"
    "v_mfma_f32_16x16x32_bf16 v[128:131], a[216:219], v[208:211], v[128:131]\n"
    "buffer_load_dwordx4 v254, s[40:43], s62 offen lds\n"
    "v_mfma_f32_16x16x32_bf16 v[160:163], a[216:219], v[212:215], v[160:163]\n"
    "v_add_u32 v250, s75, v250\n"
    "v_add_u32 v251, s75, v251\n"
    "v_mfma_f32_16x16x32_bf16 v[4:7], a[220:223], v[192:195], v[4:7]\n"
    "v_mfma_f32_16x16x32_bf16 v[36:39], a[220:223], v[196:199], v[36:39]\n"
    "v_mfma_f32_16x16x32_bf16 v[68:71], a[220:223], v[200:203], v[68:71]\n"
    "v_mfma_f32_16x16x32_bf16 v[100:103], a[220:223], v[204:207], v[100:103]\n"
    "v_add_u32 v252, s75, v252\n"
    "v_add_u32 v253, s75, v253\n"
    "v_mfma_f32_16x16x32_bf16 v[132:135], a[220:223], v[208:211], v[132:135]\n"
    "s_waitcnt vmcnt(12) lgkmcnt(0)\n"
    "s_barrier\n"
    "v_mfma_f32_16x16x32_bf16 v[164:167], a[220:223], v[212:215], v[164:167]\n"
    "ds_read_b64_tr_b16 a[216:217], v250 offset:0\n"
    "s_add_u32 m0, s72, 0x8000\n"
    "s_add_u32 s62, s84, 0x1c0000\n"
    "v_mfma_f32_16x16x32_bf16 v[8:11], a[224:227], v[192:195], v[8:11]\n"
    "buffer_load_dwordx4 v255, s[44:47], s62 offen lds\n"
    "v_mfma_f32_16x16x32_bf16 v[40:43], a[224:227], v[196:199], v[40:43]\n"
    "ds_read_b64_tr_b16 a[218:219], v250 offset:1024\n"
    "v_mfma_f32_16x16x32_bf16 v[72:75], a[224:227], v[200:203], v[72:75]\n"
    "ds_read_b64_tr_b16 a[220:221], v250 offset:8\n"
    "s_add_u32 m0, s72, 0x8400\n"
    "v_mfma_f32_16x16x32_bf16 v[104:107], a[224:227], v[204:207], v[104:107]\n"
    "ds_read_b64_tr_b16 a[222:223], v250 offset:1032\n"
    "s_add_u32 s62, s84, 0x1c8000\n"
    "v_mfma_f32_16x16x32_bf16 v[136:139], a[224:227], v[208:211], v[136:139]\n"
    "buffer_load_dwordx4 v255, s[44:47], s62 offen lds\n"
    "v_mfma_f32_16x16x32_bf16 v[168:171], a[224:227], v[212:215], v[168:171]\n"
    "ds_read_b128 v[216:219], v249 offset:24576\n"
    "v_mfma_f32_16x16x32_bf16 v[12:15], a[228:231], v[192:195], v[12:15]\n"
    "ds_read_b128 v[220:223], v249 offset:26624\n"
    "s_add_u32 m0, s71, 0x10000\n"
    "v_mfma_f32_16x16x32_bf16 v[44:47], a[228:231], v[196:199], v[44:47]\n"
    "ds_read_b128 v[224:227], v249 offset:28672\n"
    "s_add_u32 s62, s66, 0x240000\n"
    "v_mfma_f32_16x16x32_bf16 v[76:79], a[228:231], v[200:203], v[76:79]\n"
    "buffer_load_dwordx4 v255, s[44:47], s62 offen lds\n"
    "v_mfma_f32_16x16x32_bf16 v[108:111], a[228:231], v[204:207], v[108:111]\n"
    "ds_read_b128 v[228:231], v249 offset:30720\n"
    "v_mfma_f32_16x16x32_bf16 v[140:143], a[228:231], v[208:211], v[140:143]\n"
    "ds_read_b128 v[232:235], v249 offset:32768\n"
    "s_add_u32 m0, s71, 0x10400\n"
    "v_mfma_f32_16x16x32_bf16 v[172:175], a[228:231], v[212:215], v[172:175]\n"
    "ds_read_b128 v[236:239], v249 offset:34816\n"
    "s_add_u32 s62, s66, 0x248000\n"
    "v_mfma_f32_16x16x32_bf16 v[16:19], a[232:235], v[192:195], v[16:19]\n"
    "buffer_load_dwordx4 v255, s[44:47], s62 offen lds\n"
    "v_mfma_f32_16x16x32_bf16 v[48:51], a[232:235], v[196:199], v[48:51]\n"
    "ds_read_b64_tr_b16 a[224:225], v251 offset:0\n"
    "v_mfma_f32_16x16x32_bf16 v[80:83], a[232:235], v[200:203], v[80:83]\n"
    "ds_read_b64_tr_b16 a[226:227], v251 offset:1024\n"
    "v_mfma_f32_16x16x32_bf16 v[112:115], a[232:235], v[204:207], v[112:115]\n"
    "ds_read_b64_tr_b16 a[228:229], v251 offset:8\n"
    "v_mfma_f32_16x16x32_bf16 v[144:147], a[232:235], v[208:211], v[144:147]\n"
    "v_mfma_f32_16x16x32_bf16 v[176:179], a[232:235], v[212:215], v[176:179]\n"
    "ds_read_b64_tr_b16 a[230:231], v251 offset:1032\n"
    "v_mfma_f32_16x16x32_bf16 v[20:23], a[236:239], v[192:195], v[20:23]\n"
    "ds_read_b64_tr_b16 a[232:233], v252 offset:0\n"
    "v_mfma_f32_16x16x32_bf16 v[52:55], a[236:239], v[196:199], v[52:55]\n"
    "ds_read_b64_tr_b16 a[234:235], v252 offset:1024\n"
    "v_mfma_f32_16x16x32_bf16 v[84:87], a[236:239], v[200:203], v[84:87]\n"
    "v_mfma_f32_16x16x32_bf16 v[116:119], a[236:239], v[204:207], v[116:119]\n"
    "v_mfma_f32_16x16x32_bf16 v[148:151], a[236:239], v[208:211], v[148:151]\n"
    "v_mfma_f32_16x16x32_bf16 v[180:183], a[236:239], v[212:215], v[180:183]\n"
    "v_mfma_f32_16x16x32_bf16 v[24:27], a[240:243], v[192:195], v[24:27]\n"
    "v_mfma_f32_16x16x32_bf16 v[56:59], a[240:243], v[196:199], v[56:59]\n"
    "ds_read_b64_tr_b16 a[236:237], v252 offset:8\n"
    "v_mfma_f32_16x16x32_bf16 v[88:91], a[240:243], v[200:203], v[88:91]\n"
    "ds_read_b64_tr_b16 a[238:239], v252 offset:1032\n"
    "v_mfma_f32_16x16x32_bf16 v[120:123], a[240:243], v[204:207], v[120:123]\n"
    "v_mfma_f32_16x16x32_bf16 v[152:155], a[240:243], v[208:211], v[152:155]\n"
    "v_mfma_f32_16x16x32_bf16 v[184:187], a[240:243], v[212:215], v[184:187]\n"
    "v_mfma_f32_16x16x32_bf16 v[28:31], a[244:247], v[192:195], v[28:31]\n"
    "ds_read_b64_tr_b16 a[240:241], v253 offset:0\n"
    "v_mfma_f32_16x16x32_bf16 v[60:63], a[244:247], v[196:199], v[60:63]\n"
    "ds_read_b64_tr_b16 a[242:243], v253 offset:1024\n"
    "v_mfma_f32_16x16x32_bf16 v[92:95], a[244:247], v[200:203], v[92:95]\n"
    "v_mfma_f32_16x16x32_bf16 v[124:127], a[244:247], v[204:207], v[124:127]\n"
    "v_mfma_f32_16x16x32_bf16 v[156:159], a[244:247], v[208:211], v[156:159]\n"
    "v_mfma_f32_16x16x32_bf16 v[188:191], a[244:247], v[212:215], v[188:191]\n"
    "s_waitcnt lgkmcnt(8)\n"
    "v_mfma_f32_16x16x32_bf16 v[0:3], a[216:219], v[216:219], v[0:3]\n"
    "ds_read_b64_tr_b16 a[244:245], v253 offset:8\n"
    "v_mfma_f32_16x16x32_bf16 v[32:35], a[216:219], v[220:223], v[32:35]\n"
    "ds_read_b64_tr_b16 a[246:247], v253 offset:1032\n"
    "v_mfma_f32_16x16x32_bf16 v[64:67], a[216:219], v[224:227], v[64:67]\n"
    "v_mfma_f32_16x16x32_bf16 v[96:99], a[216:219], v[228:231], v[96:99]\n"
    "v_mfma_f32_16x16x32_bf16 v[128:131], a[216:219], v[232:235], v[128:131]\n"
    "v_mfma_f32_16x16x32_bf16 v[160:163], a[216:219], v[236:239], v[160:163]\n"
    "v_mfma_f32_16x16x32_bf16 v[4:7], a[220:223], v[216:219], v[4:7]\n"
    "v_mfma_f32_16x16x32_bf16 v[36:39], a[220:223], v[220:223], v[36:39]\n"
    "v_mfma_f32_16x16x32_bf16 v[68:71], a[220:223], v[224:227], v[68:71]\n"
    "v_mfma_f32_16x16x32_bf16 v[100:103], a[220:223], v[228:231], v[100:103]\n"
    "v_mfma_f32_16x16x32_bf16 v[132:135], a[220:223], v[232:235], v[132:135]\n"
    "s_waitcnt vmcnt(7) lgkmcnt(0)\n"
    "s_barrier\n"
    "v_mfma_f32_16x16x32_bf16 v[164:167], a[220:223], v[236:239], v[164:167]\n"
    "ds_read_b64_tr_b16 a[216:217], v250 offset:16384\n"
    "s_add_u32 m0, s72, 0xc000\n"
    "s_add_u32 s62, s84, 0x200000\n"
    "v_mfma_f32_16x16x32_bf16 v[8:11], a[224:227], v[216:219], v[8:11]\n"
    "buffer_load_dwordx4 v255, s[44:47], s62 offen lds\n"
    "v_mfma_f32_16x16x32_bf16 v[40:43], a[224:227], v[220:223], v[40:43]\n"
    "ds_read_b64_tr_b16 a[218:219], v250 offset:17408\n"
    "v_mfma_f32_16x16x32_bf16 v[72:75], a[224:227], v[224:227], v[72:75]\n"
    "ds_read_b64_tr_b16 a[220:221], v250 offset:16392\n"
    "s_add_u32 m0, s72, 0xc400\n"
    "v_mfma_f32_16x16x32_bf16 v[104:107], a[224:227], v[228:231], v[104:107]\n"
    "ds_read_b64_tr_b16 a[222:223], v250 offset:17416\n"
    "s_add_u32 s62, s84, 0x208000\n"
    "v_mfma_f32_16x16x32_bf16 v[136:139], a[224:227], v[232:235], v[136:139]\n"
    "buffer_load_dwordx4 v255, s[44:47], s62 offen lds\n"
    "v_mfma_f32_16x16x32_bf16 v[168:171], a[224:227], v[236:239], v[168:171]\n"
    "ds_read_b128 v[192:195], v248 offset:49152\n"
    "v_mfma_f32_16x16x32_bf16 v[12:15], a[228:231], v[216:219], v[12:15]\n"
    "ds_read_b128 v[196:199], v248 offset:51200\n"
    "s_add_u32 m0, s71, 0x0\n"
    "v_mfma_f32_16x16x32_bf16 v[44:47], a[228:231], v[220:223], v[44:47]\n"
    "ds_read_b128 v[200:203], v248 offset:53248\n"
    "s_add_u32 s62, s66, 0x280000\n"
    "v_mfma_f32_16x16x32_bf16 v[76:79], a[228:231], v[224:227], v[76:79]\n"
    "buffer_load_dwordx4 v255, s[44:47], s62 offen lds\n"
    "v_mfma_f32_16x16x32_bf16 v[108:111], a[228:231], v[228:231], v[108:111]\n"
    "ds_read_b128 v[204:207], v248 offset:55296\n"
    "v_mfma_f32_16x16x32_bf16 v[140:143], a[228:231], v[232:235], v[140:143]\n"
    "ds_read_b128 v[208:211], v248 offset:57344\n"
    "s_add_u32 m0, s71, 0x400\n"
    "v_mfma_f32_16x16x32_bf16 v[172:175], a[228:231], v[236:239], v[172:175]\n"
    "ds_read_b128 v[212:215], v248 offset:59392\n"
    "s_add_u32 s62, s66, 0x288000\n"
    "v_mfma_f32_16x16x32_bf16 v[16:19], a[232:235], v[216:219], v[16:19]\n"
    "buffer_load_dwordx4 v255, s[44:47], s62 offen lds\n"
    "v_mfma_f32_16x16x32_bf16 v[48:51], a[232:235], v[220:223], v[48:51]\n"
    "ds_read_b64_tr_b16 a[224:225], v251 offset:16384\n"
    "v_mfma_f32_16x16x32_bf16 v[80:83], a[232:235], v[224:227], v[80:83]\n"
    "ds_read_b64_tr_b16 a[226:227], v251 offset:17408\n"
    "s_add_u32 m0, s74, 0x0\n"
    "v_mfma_f32_16x16x32_bf16 v[112:115], a[232:235], v[228:231], v[112:115]\n"
    "ds_read_b64_tr_b16 a[228:229], v251 offset:16392\n"
    "s_add_u32 s62, s73, 0x200\n"
    "v_mfma_f32_16x16x32_bf16 v[144:147], a[232:235], v[232:235], v[144:147]\n"
    "buffer_load_dwordx4 v254, s[40:43], s62 offen lds\n"
    "v_mfma_f32_16x16x32_bf16 v[176:179], a[232:235], v[236:239], v[176:179]\n"
    "ds_read_b64_tr_b16 a[230:231], v251 offset:17416\n"
    "v_mfma_f32_16x16x32_bf16 v[20:23], a[236:239], v[216:219], v[20:23]\n"
    "ds_read_b64_tr_b16 a[232:233], v252 offset:16384\n"
    "v_mfma_f32_16x16x32_bf16 v[52:55], a[236:239], v[220:223], v[52:55]\n"
    "ds_read_b64_tr_b16 a[234:235], v252 offset:17408\n"
    "s_add_u32 m0, s74, 0x400\n"
    "v_mfma_f32_16x16x32_bf16 v[84:87], a[236:239], v[224:227], v[84:87]\n"
    "s_add_u32 s62, s73, 0x2a00\n"
    "v_mfma_f32_16x16x32_bf16 v[116:119], a[236:239], v[228:231], v[116:119]\n"
    "buffer_load_dwordx4 v254, s[40:43], s62 offen lds\n"
    "v_mfma_f32_16x16x32_bf16 v[148:151], a[236:239], v[232:235], v[148:151]\n"
    "s_add_u32 m0, s74, 0x800\n"
    "v_mfma_f32_16x16x32_bf16 v[180:183], a[236:239], v[236:239], v[180:183]\n"
    "s_add_u32 s62, s73, 0x5200\n"
    "v_mfma_f32_16x16x32_bf16 v[24:27], a[240:243], v[216:219], v[24:27]\n"
    "buffer_load_dwordx4 v254, s[40:43], s62 offen lds\n"
    "v_mfma_f32_16x16x32_bf16 v[56:59], a[240:243], v[220:223], v[56:59]\n"
    "ds_read_b64_tr_b16 a[236:237], v252 offset:16392\n"
    "v_mfma_f32_16x16x32_bf16 v[88:91], a[240:243], v[224:227], v[88:91]\n"
    "ds_read_b64_tr_b16 a[238:239], v252 offset:17416\n"
    "s_add_u32 m0, s65, 0x6000\n"
    "v_mfma_f32_16x16x32_bf16 v[120:123], a[240:243], v[228:231], v[120:123]\n"
    "s_add_u32 s62, s64, 0x280\n"
    "v_mfma_f32_16x16x32_bf16 v[152:155], a[240:243], v[232:235], v[152:155]\n"
    "buffer_load_dwordx4 v254, s[40:43], s62 offen lds\n"
    "v_mfma_f32_16x16x32_bf16 v[184:187], a[240:243], v[236:239], v[184:187]\n"
    "v_mfma_f32_16x16x32_bf16 v[28:31], a[244:247], v[216:219], v[28:31]\n"
    "ds_read_b64_tr_b16 a[240:241], v253 offset:16384\n"
    "v_mfma_f32_16x16x32_bf16 v[60:63], a[244:247], v[220:223], v[60:63]\n"
    "ds_read_b64_tr_b16 a[242:243], v253 offset:17408\n"
    "s_add_u32 m0, s65, 0x6400\n"
    "v_mfma_f32_16x16x32_bf16 v[92:95], a[244:247], v[224:227], v[92:95]\n"
    "s_add_u32 s62, s64, 0x2a80\n"
    "v_mfma_f32_16x16x32_bf16 v[124:127], a[244:247], v[228:231], v[124:127]\n"
    "buffer_load_dwordx4 v254, s[40:43], s62 offen lds\n"
    "v_mfma_f32_16x16x32_bf16 v[156:159], a[244:247], v[232:235], v[156:159]\n"
    "v_mfma_f32_16x16x32_bf16 v[188:191], a[244:247], v[236:239], v[188:191]\n"
    "s_waitcnt lgkmcnt(8)\n"
    "v_mfma_f32_16x16x32_bf16 v[0:3], a[216:219], v[192:195], v[0:3]\n"
    "ds_read_b64_tr_b16 a[244:245], v253 offset:16392\n"
    "v_mfma_f32_16x16x32_bf16 v[32:35], a[216:219], v[196:199], v[32:35]\n"
    "ds_read_b64_tr_b16 a[246:247], v253 offset:17416\n"
    "v_mfma_f32_16x16x32_bf16 v[64:67], a[216:219], v[200:203], v[64:67]\n"
    "s_add_u32 m0, s65, 0x6800\n"
    "v_mfma_f32_16x16x32_bf16 v[96:99], a[216:219], v[204:207], v[96:99]\n"
    "s_add_u32 s62, s64, 0x5280\n"
    "v_mfma_f32_16x16x32_bf16 v[128:131], a[216:219], v[208:211], v[128:131]\n"
    "buffer_load_dwordx4 v254, s[40:43], s62 offen lds\n"
    "v_mfma_f32_16x16x32_bf16 v[160:163], a[216:219], v[212:215], v[160:163]\n"
    "v_mfma_f32_16x16x32_bf16 v[4:7], a[220:223], v[192:195], v[4:7]\n"
    "v_mfma_f32_16x16x32_bf16 v[36:39], a[220:223], v[196:199], v[36:39]\n"
    "v_mfma_f32_16x16x32_bf16 v[68:71], a[220:223], v[200:203], v[68:71]\n"
    "v_mfma_f32_16x16x32_bf16 v[100:103], a[220:223], v[204:207], v[100:103]\n"
    "v_mfma_f32_16x16x32_bf16 v[132:135], a[220:223], v[208:211], v[132:135]\n"
    "s_waitcnt vmcnt(12) lgkmcnt(0)\n"
    "s_barrier\n"
    "v_mfma_f32_16x16x32_bf16 v[164:167], a[220:223], v[212:215], v[164:167]\n"
    "ds_read_b64_tr_b16 a[216:217], v250 offset:32768\n"
    "s_add_u32 m0, s72, 0x10000\n"
    "s_add_u32 s62, s84, 0x240000\n"
    "v_mfma_f32_16x16x32_bf16 v[8:11], a[224:227], v[192:195], v[8:11]\n"
    "buffer_load_dwordx4 v255, s[44:47], s62 offen lds\n"
    "v_mfma_f32_16x16x32_bf16 v[40:43], a[224:227], v[196:199], v[40:43]\n"
    "ds_read_b64_tr_b16 a[218:219], v250 offset:33792\n"
    "v_mfma_f32_16x16x32_bf16 v[72:75], a[224:227], v[200:203], v[72:75]\n"
    "ds_read_b64_tr_b16 a[220:221], v250 offset:32776\n"
    "s_add_u32 m0, s72, 0x10400\n"
    "v_mfma_f32_16x16x32_bf16 v[104:107], a[224:227], v[204:207], v[104:107]\n"
    "ds_read_b64_tr_b16 a[222:223], v250 offset:33800\n"
    "s_add_u32 s62, s84, 0x248000\n"
    "v_mfma_f32_16x16x32_bf16 v[136:139], a[224:227], v[208:211], v[136:139]\n"
    "buffer_load_dwordx4 v255, s[44:47], s62 offen lds\n"
    "v_mfma_f32_16x16x32_bf16 v[168:171], a[224:227], v[212:215], v[168:171]\n"
    "ds_read_b128 v[216:219], v249 offset:49152\n"
    "v_mfma_f32_16x16x32_bf16 v[12:15], a[228:231], v[192:195], v[12:15]\n"
    "ds_read_b128 v[220:223], v249 offset:51200\n"
    "s_add_u32 m0, s71, 0x4000\n"
    "v_mfma_f32_16x16x32_bf16 v[44:47], a[228:231], v[196:199], v[44:47]\n"
    "ds_read_b128 v[224:227], v249 offset:53248\n"
    "s_add_u32 s62, s66, 0x2c0000\n"
    "v_mfma_f32_16x16x32_bf16 v[76:79], a[228:231], v[200:203], v[76:79]\n"
    "buffer_load_dwordx4 v255, s[44:47], s62 offen lds\n"
    "v_mfma_f32_16x16x32_bf16 v[108:111], a[228:231], v[204:207], v[108:111]\n"
    "ds_read_b128 v[228:231], v249 offset:55296\n"
    "v_mfma_f32_16x16x32_bf16 v[140:143], a[228:231], v[208:211], v[140:143]\n"
    "ds_read_b128 v[232:235], v249 offset:57344\n"
    "s_add_u32 m0, s71, 0x4400\n"
    "v_mfma_f32_16x16x32_bf16 v[172:175], a[228:231], v[212:215], v[172:175]\n"
    "ds_read_b128 v[236:239], v249 offset:59392\n"
    "s_add_u32 s62, s66, 0x2c8000\n"
    "v_mfma_f32_16x16x32_bf16 v[16:19], a[232:235], v[192:195], v[16:19]\n"
    "buffer_load_dwordx4 v255, s[44:47], s62 offen lds\n"
    "v_mfma_f32_16x16x32_bf16 v[48:51], a[232:235], v[196:199], v[48:51]\n"
    "ds_read_b64_tr_b16 a[224:225], v251 offset:32768\n"
    "v_mfma_f32_16x16x32_bf16 v[80:83], a[232:235], v[200:203], v[80:83]\n"
    "ds_read_b64_tr_b16 a[226:227], v251 offset:33792\n"
    "v_mfma_f32_16x16x32_bf16 v[112:115], a[232:235], v[204:207], v[112:115]\n"
    "ds_read_b64_tr_b16 a[228:229], v251 offset:32776\n"
    "v_mfma_f32_16x16x32_bf16 v[144:147], a[232:235], v[208:211], v[144:147]\n"
    "v_mfma_f32_16x16x32_bf16 v[176:179], a[232:235], v[212:215], v[176:179]\n"
    "ds_read_b64_tr_b16 a[230:231], v251 offset:33800\n"
    "v_mfma_f32_16x16x32_bf16 v[20:23], a[236:239], v[192:195], v[20:23]\n"
    "ds_read_b64_tr_b16 a[232:233], v252 offset:32768\n"
    "v_mfma_f32_16x16x32_bf16 v[52:55], a[236:239], v[196:199], v[52:55]\n"
    "ds_read_b64_tr_b16 a[234:235], v252 offset:33792\n"
    "v_mfma_f32_16x16x32_bf16 v[84:87], a[236:239], v[200:203], v[84:87]\n"
    "v_mfma_f32_16x16x32_bf16 v[116:119], a[236:239], v[204:207], v[116:119]\n"
    "v_mfma_f32_16x16x32_bf16 v[148:151], a[236:239], v[208:211], v[148:151]\n"
    "v_mfma_f32_16x16x32_bf16 v[180:183], a[236:239], v[212:215], v[180:183]\n"
    "v_mfma_f32_16x16x32_bf16 v[24:27], a[240:243], v[192:195], v[24:27]\n"
    "v_mfma_f32_16x16x32_bf16 v[56:59], a[240:243], v[196:199], v[56:59]\n"
    "ds_read_b64_tr_b16 a[236:237], v252 offset:32776\n"
    "v_mfma_f32_16x16x32_bf16 v[88:91], a[240:243], v[200:203], v[88:91]\n"
    "ds_read_b64_tr_b16 a[238:239], v252 offset:33800\n"
    "v_mfma_f32_16x16x32_bf16 v[120:123], a[240:243], v[204:207], v[120:123]\n"
    "v_mfma_f32_16x16x32_bf16 v[152:155], a[240:243], v[208:211], v[152:155]\n"
    "v_mfma_f32_16x16x32_bf16 v[184:187], a[240:243], v[212:215], v[184:187]\n"
    "v_mfma_f32_16x16x32_bf16 v[28:31], a[244:247], v[192:195], v[28:31]\n"
    "ds_read_b64_tr_b16 a[240:241], v253 offset:32768\n"
    "v_mfma_f32_16x16x32_bf16 v[60:63], a[244:247], v[196:199], v[60:63]\n"
    "ds_read_b64_tr_b16 a[242:243], v253 offset:33792\n"
    "v_mfma_f32_16x16x32_bf16 v[92:95], a[244:247], v[200:203], v[92:95]\n"
    "v_mfma_f32_16x16x32_bf16 v[124:127], a[244:247], v[204:207], v[124:127]\n"
    "v_mfma_f32_16x16x32_bf16 v[156:159], a[244:247], v[208:211], v[156:159]\n"
    "v_mfma_f32_16x16x32_bf16 v[188:191], a[244:247], v[212:215], v[188:191]\n"
    "s_waitcnt lgkmcnt(8)\n"
    "v_mfma_f32_16x16x32_bf16 v[0:3], a[216:219], v[216:219], v[0:3]\n"
    "ds_read_b64_tr_b16 a[244:245], v253 offset:32776\n"
    "v_mfma_f32_16x16x32_bf16 v[32:35], a[216:219], v[220:223], v[32:35]\n"
    "ds_read_b64_tr_b16 a[246:247], v253 offset:33800\n"
    "v_mfma_f32_16x16x32_bf16 v[64:67], a[216:219], v[224:227], v[64:67]\n"
    "v_mfma_f32_16x16x32_bf16 v[96:99], a[216:219], v[228:231], v[96:99]\n"
    "v_mfma_f32_16x16x32_bf16 v[128:131], a[216:219], v[232:235], v[128:131]\n"
    "v_mfma_f32_16x16x32_bf16 v[160:163], a[216:219], v[236:239], v[160:163]\n"
    "v_mfma_f32_16x16x32_bf16 v[4:7], a[220:223], v[216:219], v[4:7]\n"
    "v_mfma_f32_16x16x32_bf16 v[36:39], a[220:223], v[220:223], v[36:39]\n"
    "v_mfma_f32_16x16x32_bf16 v[68:71], a[220:223], v[224:227], v[68:71]\n"
    "v_mfma_f32_16x16x32_bf16 v[100:103], a[220:223], v[228:231], v[100:103]\n"
    "v_mfma_f32_16x16x32_bf16 v[132:135], a[220:223], v[232:235], v[132:135]\n"
    "s_waitcnt vmcnt(7) lgkmcnt(0)\n"
    "s_barrier\n"
    "v_mfma_f32_16x16x32_bf16 v[164:167], a[220:223], v[236:239], v[164:167]\n"
    "ds_read_b64_tr_b16 a[216:217], v250 offset:49152\n"
    "s_add_u32 m0, s72, 0x0\n"
    "s_add_u32 s62, s84, 0x280000\n"
    "v_mfma_f32_16x16x32_bf16 v[8:11], a[224:227], v[216:219], v[8:11]\n"
    "buffer_load_dwordx4 v255, s[44:47], s62 offen lds\n"
    "v_mfma_f32_16x16x32_bf16 v[40:43], a[224:227], v[220:223], v[40:43]\n"
    "ds_read_b64_tr_b16 a[218:219], v250 offset:50176\n"
    "v_mfma_f32_16x16x32_bf16 v[72:75], a[224:227], v[224:227], v[72:75]\n"
    "ds_read_b64_tr_b16 a[220:221], v250 offset:49160\n"
    "s_add_u32 m0, s72, 0x400\n"
    "v_mfma_f32_16x16x32_bf16 v[104:107], a[224:227], v[228:231], v[104:107]\n"
    "ds_read_b64_tr_b16 a[222:223], v250 offset:50184\n"
    "s_add_u32 s62, s84, 0x288000\n"
    "v_mfma_f32_16x16x32_bf16 v[136:139], a[224:227], v[232:235], v[136:139]\n"
    "buffer_load_dwordx4 v255, s[44:47], s62 offen lds\n"
    "v_mfma_f32_16x16x32_bf16 v[168:171], a[224:227], v[236:239], v[168:171]\n"
    "ds_read_b128 v[192:195], v248 offset:0\n"
    "v_mfma_f32_16x16x32_bf16 v[12:15], a[228:231], v[216:219], v[12:15]\n"
    "ds_read_b128 v[196:199], v248 offset:2048\n"
    "s_add_u32 m0, s71, 0x8000\n"
    "v_mfma_f32_16x16x32_bf16 v[44:47], a[228:231], v[220:223], v[44:47]\n"
    "ds_read_b128 v[200:203], v248 offset:4096\n"
    "s_add_u32 s62, s66, 0x300000\n"
    "v_mfma_f32_16x16x32_bf16 v[76:79], a[228:231], v[224:227], v[76:79]\n"
    "buffer_load_dwordx4 v255, s[44:47], s62 offen lds\n"
    "v_mfma_f32_16x16x32_bf16 v[108:111], a[228:231], v[228:231], v[108:111]\n"
    "ds_read_b128 v[204:207], v248 offset:6144\n"
    "v_mfma_f32_16x16x32_bf16 v[140:143], a[228:231], v[232:235], v[140:143]\n"
    "ds_read_b128 v[208:211], v248 offset:8192\n"
    "s_add_u32 m0, s71, 0x8400\n"
    "v_mfma_f32_16x16x32_bf16 v[172:175], a[228:231], v[236:239], v[172:175]\n"
    "ds_read_b128 v[212:215], v248 offset:10240\n"
    "s_add_u32 s62, s66, 0x308000\n"
    "v_mfma_f32_16x16x32_bf16 v[16:19], a[232:235], v[216:219], v[16:19]\n"
    "buffer_load_dwordx4 v255, s[44:47], s62 offen lds\n"
    "v_mfma_f32_16x16x32_bf16 v[48:51], a[232:235], v[220:223], v[48:51]\n"
    "ds_read_b64_tr_b16 a[224:225], v251 offset:49152\n"
    "v_mfma_f32_16x16x32_bf16 v[80:83], a[232:235], v[224:227], v[80:83]\n"
    "ds_read_b64_tr_b16 a[226:227], v251 offset:50176\n"
    "s_add_u32 m0, s74, 0x6000\n"
    "v_mfma_f32_16x16x32_bf16 v[112:115], a[232:235], v[228:231], v[112:115]\n"
    "ds_read_b64_tr_b16 a[228:229], v251 offset:49160\n"
    "s_add_u32 s62, s73, 0x280\n"
    "v_mfma_f32_16x16x32_bf16 v[144:147], a[232:235], v[232:235], v[144:147]\n"
    "buffer_load_dwordx4 v254, s[40:43], s62 offen lds\n"
    "v_mfma_f32_16x16x32_bf16 v[176:179], a[232:235], v[236:239], v[176:179]\n"
    "ds_read_b64_tr_b16 a[230:231], v251 offset:50184\n"
    "v_mfma_f32_16x16x32_bf16 v[20:23], a[236:239], v[216:219], v[20:23]\n"
    "ds_read_b64_tr_b16 a[232:233], v252 offset:49152\n"
    "v_mfma_f32_16x16x32_bf16 v[52:55], a[236:239], v[220:223], v[52:55]\n"
    "ds_read_b64_tr_b16 a[234:235], v252 offset:50176\n"
    "s_add_u32 m0, s74, 0x6400\n"
    "v_mfma_f32_16x16x32_bf16 v[84:87], a[236:239], v[224:227], v[84:87]\n"
    "s_add_u32 s62, s73, 0x2a80\n"
    "v_mfma_f32_16x16x32_bf16 v[116:119], a[236:239], v[228:231], v[116:119]\n"
    "buffer_load_dwordx4 v254, s[40:43], s62 offen lds\n"
    "v_mfma_f32_16x16x32_bf16 v[148:151], a[236:239], v[232:235], v[148:151]\n"
    "s_add_u32 m0, s74, 0x6800\n"
    "v_mfma_f32_16x16x32_bf16 v[180:183], a[236:239], v[236:239], v[180:183]\n"
    "s_add_u32 s62, s73, 0x5280\n"
    "v_mfma_f32_16x16x32_bf16 v[24:27], a[240:243], v[216:219], v[24:27]\n"
    "buffer_load_dwordx4 v254, s[40:43], s62 offen lds\n"
    "v_mfma_f32_16x16x32_bf16 v[56:59], a[240:243], v[220:223], v[56:59]\n"
    "ds_read_b64_tr_b16 a[236:237], v252 offset:49160\n"
    "v_mfma_f32_16x16x32_bf16 v[88:91], a[240:243], v[224:227], v[88:91]\n"
    "ds_read_b64_tr_b16 a[238:239], v252 offset:50184\n"
    "s_add_u32 m0, s65, 0xc000\n"
    "v_mfma_f32_16x16x32_bf16 v[120:123], a[240:243], v[228:231], v[120:123]\n"
    "s_add_u32 s62, s64, 0x300\n"
    "v_mfma_f32_16x16x32_bf16 v[152:155], a[240:243], v[232:235], v[152:155]\n"
    "buffer_load_dwordx4 v254, s[40:43], s62 offen lds\n"
    "v_mfma_f32_16x16x32_bf16 v[184:187], a[240:243], v[236:239], v[184:187]\n"
    "v_mfma_f32_16x16x32_bf16 v[28:31], a[244:247], v[216:219], v[28:31]\n"
    "ds_read_b64_tr_b16 a[240:241], v253 offset:49152\n"
    "v_mfma_f32_16x16x32_bf16 v[60:63], a[244:247], v[220:223], v[60:63]\n"
    "ds_read_b64_tr_b16 a[242:243], v253 offset:50176\n"
    "s_add_u32 m0, s65, 0xc400\n"
    "v_mfma_f32_16x16x32_bf16 v[92:95], a[244:247], v[224:227], v[92:95]\n"
    "s_add_u32 s62, s64, 0x2b00\n"
    "v_mfma_f32_16x16x32_bf16 v[124:127], a[244:247], v[228:231], v[124:127]\n"
    "buffer_load_dwordx4 v254, s[40:43], s62 offen lds\n"
    "v_mfma_f32_16x16x32_bf16 v[156:159], a[244:247], v[232:235], v[156:159]\n"
    "v_mfma_f32_16x16x32_bf16 v[188:191], a[244:247], v[236:239], v[188:191]\n"
    "s_waitcnt lgkmcnt(8)\n"
    "v_mfma_f32_16x16x32_bf16 v[0:3], a[216:219], v[192:195], v[0:3]\n"
    "ds_read_b64_tr_b16 a[244:245], v253 offset:49160\n"
    "v_mfma_f32_16x16x32_bf16 v[32:35], a[216:219], v[196:199], v[32:35]\n"
    "ds_read_b64_tr_b16 a[246:247], v253 offset:50184\n"
    "v_mfma_f32_16x16x32_bf16 v[64:67], a[216:219], v[200:203], v[64:67]\n"
    "s_add_u32 m0, s65, 0xc800\n"
    "v_mfma_f32_16x16x32_bf16 v[96:99], a[216:219], v[204:207], v[96:99]\n"
    "s_add_u32 s62, s64, 0x5300\n"
    "v_mfma_f32_16x16x32_bf16 v[128:131], a[216:219], v[208:211], v[128:131]\n"
    "buffer_load_dwordx4 v254, s[40:43], s62 offen lds\n"
    "v_mfma_f32_16x16x32_bf16 v[160:163], a[216:219], v[212:215], v[160:163]\n"
    "v_add_u32 v250, 0x10000, v250\n"
    "v_add_u32 v251, 0x10000, v251\n"
    "v_mfma_f32_16x16x32_bf16 v[4:7], a[220:223], v[192:195], v[4:7]\n"
    "v_mfma_f32_16x16x32_bf16 v[36:39], a[220:223], v[196:199], v[36:39]\n"
    "v_mfma_f32_16x16x32_bf16 v[68:71], a[220:223], v[200:203], v[68:71]\n"
    "v_mfma_f32_16x16x32_bf16 v[100:103], a[220:223], v[204:207], v[100:103]\n"
    "v_add_u32 v252, 0x10000, v252\n"
    "v_add_u32 v253, 0x10000, v253\n"
    "v_mfma_f32_16x16x32_bf16 v[132:135], a[220:223], v[208:211], v[132:135]\n"
    "s_waitcnt vmcnt(12) lgkmcnt(0)\n"
    "s_barrier\n"
    "v_mfma_f32_16x16x32_bf16 v[164:167], a[220:223], v[212:215], v[164:167]\n"
    "ds_read_b64_tr_b16 a[216:217], v250 offset:0\n"
    "s_add_u32 m0, s72, 0x4000\n"
    "s_add_u32 s62, s84, 0x2c0000\n"
    "v_mfma_f32_16x16x32_bf16 v[8:11], a[224:227], v[192:195], v[8:11]\n"
    "buffer_load_dwordx4 v255, s[44:47], s62 offen lds\n"
    "v_mfma_f32_16x16x32_bf16 v[40:43], a[224:227], v[196:199], v[40:43]\n"
    "ds_read_b64_tr_b16 a[218:219], v250 offset:1024\n"
    "v_mfma_f32_16x16x32_bf16 v[72:75], a[224:227], v[200:203], v[72:75]\n"
    "ds_read_b64_tr_b16 a[220:221], v250 offset:8\n"
    "s_add_u32 m0, s72, 0x4400\n"
    "v_mfma_f32_16x16x32_bf16 v[104:107], a[224:227], v[204:207], v[104:107]\n"
    "ds_read_b64_tr_b16 a[222:223], v250 offset:1032\n"
    "s_add_u32 s62, s84, 0x2c8000\n"
    "v_mfma_f32_16x16x32_bf16 v[136:139], a[224:227], v[208:211], v[136:139]\n"
    "buffer_load_dwordx4 v255, s[44:47], s62 offen lds\n"
    "v_mfma_f32_16x16x32_bf16 v[168:171], a[224:227], v[212:215], v[168:171]\n"
    "ds_read_b128 v[216:219], v249 offset:0\n"
    "v_mfma_f32_16x16x32_bf16 v[12:15], a[228:231], v[192:195], v[12:15]\n"
    "ds_read_b128 v[220:223], v249 offset:2048\n"
    "s_add_u32 m0, s71, 0xc000\n"
    "v_mfma_f32_16x16x32_bf16 v[44:47], a[228:231], v[196:199], v[44:47]\n"
    "ds_read_b128 v[224:227], v249 offset:4096\n"
    "s_add_u32 s62, s66, 0x340000\n"
    "v_mfma_f32_16x16x32_bf16 v[76:79], a[228:231], v[200:203], v[76:79]\n"
    "buffer_load_dwordx4 v255, s[44:47], s62 offen lds\n"
    "v_mfma_f32_16x16x32_bf16 v[108:111], a[228:231], v[204:207], v[108:111]\n"
    "ds_read_b128 v[228:231], v249 offset:6144\n"
    "v_mfma_f32_16x16x32_bf16 v[140:143], a[228:231], v[208:211], v[140:143]\n"
    "ds_read_b128 v[232:235], v249 offset:8192\n"
    "s_add_u32 m0, s71, 0xc400\n"
    "v_mfma_f32_16x16x32_bf16 v[172:175], a[228:231], v[212:215], v[172:175]\n"
    "ds_read_b128 v[236:239], v249 offset:10240\n"
    "s_add_u32 s62, s66, 0x348000\n"
    "v_mfma_f32_16x16x32_bf16 v[16:19], a[232:235], v[192:195], v[16:19]\n"
    "buffer_load_dwordx4 v255, s[44:47], s62 offen lds\n"
    "v_mfma_f32_16x16x32_bf16 v[48:51], a[232:235], v[196:199], v[48:51]\n"
    "ds_read_b64_tr_b16 a[224:225], v251 offset:0\n"
    "v_mfma_f32_16x16x32_bf16 v[80:83], a[232:235], v[200:203], v[80:83]\n"
    "ds_read_b64_tr_b16 a[226:227], v251 offset:1024\n"
    "v_mfma_f32_16x16x32_bf16 v[112:115], a[232:235], v[204:207], v[112:115]\n"
    "ds_read_b64_tr_b16 a[228:229], v251 offset:8\n"
    "v_mfma_f32_16x16x32_bf16 v[144:147], a[232:235], v[208:211], v[144:147]\n"
    "v_mfma_f32_16x16x32_bf16 v[176:179], a[232:235], v[212:215], v[176:179]\n"
    "ds_read_b64_tr_b16 a[230:231], v251 offset:1032\n"
    "v_mfma_f32_16x16x32_bf16 v[20:23], a[236:239], v[192:195], v[20:23]\n"
    "ds_read_b64_tr_b16 a[232:233], v252 offset:0\n"
    "v_mfma_f32_16x16x32_bf16 v[52:55], a[236:239], v[196:199], v[52:55]\n"
    "ds_read_b64_tr_b16 a[234:235], v252 offset:1024\n"
    "v_mfma_f32_16x16x32_bf16 v[84:87], a[236:239], v[200:203], v[84:87]\n"
    "v_mfma_f32_16x16x32_bf16 v[116:119], a[236:239], v[204:207], v[116:119]\n"
    "v_mfma_f32_16x16x32_bf16 v[148:151], a[236:239], v[208:211], v[148:151]\n"
    "v_mfma_f32_16x16x32_bf16 v[180:183], a[236:239], v[212:215], v[180:183]\n"
    "v_mfma_f32_16x16x32_bf16 v[24:27], a[240:243], v[192:195], v[24:27]\n"
    "v_mfma_f32_16x16x32_bf16 v[56:59], a[240:243], v[196:199], v[56:59]\n"
    "ds_read_b64_tr_b16 a[236:237], v252 offset:8\n"
    "v_mfma_f32_16x16x32_bf16 v[88:91], a[240:243], v[200:203], v[88:91]\n"
    "ds_read_b64_tr_b16 a[238:239], v252 offset:1032\n"
    "v_mfma_f32_16x16x32_bf16 v[120:123], a[240:243], v[204:207], v[120:123]\n"
    "v_mfma_f32_16x16x32_bf16 v[152:155], a[240:243], v[208:211], v[152:155]\n"
    "v_mfma_f32_16x16x32_bf16 v[184:187], a[240:243], v[212:215], v[184:187]\n"
    "v_mfma_f32_16x16x32_bf16 v[28:31], a[244:247], v[192:195], v[28:31]\n"
    "ds_read_b64_tr_b16 a[240:241], v253 offset:0\n"
    "v_mfma_f32_16x16x32_bf16 v[60:63], a[244:247], v[196:199], v[60:63]\n"
    "ds_read_b64_tr_b16 a[242:243], v253 offset:1024\n"
    "v_mfma_f32_16x16x32_bf16 v[92:95], a[244:247], v[200:203], v[92:95]\n"
    "v_mfma_f32_16x16x32_bf16 v[124:127], a[244:247], v[204:207], v[124:127]\n"
    "v_mfma_f32_16x16x32_bf16 v[156:159], a[244:247], v[208:211], v[156:159]\n"
    "v_mfma_f32_16x16x32_bf16 v[188:191], a[244:247], v[212:215], v[188:191]\n"
    "s_waitcnt lgkmcnt(8)\n"
    "v_mfma_f32_16x16x32_bf16 v[0:3], a[216:219], v[216:219], v[0:3]\n"
    "ds_read_b64_tr_b16 a[244:245], v253 offset:8\n"
    "v_mfma_f32_16x16x32_bf16 v[32:35], a[216:219], v[220:223], v[32:35]\n"
    "ds_read_b64_tr_b16 a[246:247], v253 offset:1032\n"
    "v_mfma_f32_16x16x32_bf16 v[64:67], a[216:219], v[224:227], v[64:67]\n"
    "v_mfma_f32_16x16x32_bf16 v[96:99], a[216:219], v[228:231], v[96:99]\n"
    "v_mfma_f32_16x16x32_bf16 v[128:131], a[216:219], v[232:235], v[128:131]\n"
    "v_mfma_f32_16x16x32_bf16 v[160:163], a[216:219], v[236:239], v[160:163]\n"
    "v_add_u32 v250, s75, v250\n"
    "v_add_u32 v251, s75, v251\n"
    "v_mfma_f32_16x16x32_bf16 v[4:7], a[220:223], v[216:219], v[4:7]\n"
    "v_mfma_f32_16x16x32_bf16 v[36:39], a[220:223], v[220:223], v[36:39]\n"
    "v_mfma_f32_16x16x32_bf16 v[68:71], a[220:223], v[224:227], v[68:71]\n"
    "v_mfma_f32_16x16x32_bf16 v[100:103], a[220:223], v[228:231], v[100:103]\n"
    "v_add_u32 v252, s75, v252\n"
    "v_add_u32 v253, s75, v253\n"
    "v_mfma_f32_16x16x32_bf16 v[132:135], a[220:223], v[232:235], v[132:135]\n"
    "s_waitcnt vmcnt(7) lgkmcnt(0)\n"
    "s_barrier\n"
    "v_mfma_f32_16x16x32_bf16 v[164:167], a[220:223], v[236:239], v[164:167]\n"
    "ds_read_b64_tr_b16 a[216:217], v250 offset:0\n"
    "s_add_u32 m0, s72, 0x8000\n"
    "s_add_u32 s62, s84, 0x300000\n"
    "v_mfma_f32_16x16x32_bf16 v[8:11], a[224:227], v[216:219], v[8:11]\n"
    "buffer_load_dwordx4 v255, s[44:47], s62 offen lds\n"
    "v_mfma_f32_16x16x32_bf16 v[40:43], a[224:227], v[220:223], v[40:43]\n"
    "ds_read_b64_tr_b16 a[218:219], v250 offset:1024\n"
    "v_mfma_f32_16x16x32_bf16 v[72:75], a[224:227], v[224:227], v[72:75]\n"
    "ds_read_b64_tr_b16 a[220:221], v250 offset:8\n"
    "s_add_u32 m0, s72, 0x8400\n"
    "v_mfma_f32_16x16x32_bf16 v[104:107], a[224:227], v[228:231], v[104:107]\n"
    "ds_read_b64_tr_b16 a[222:223], v250 offset:1032\n"
    "s_add_u32 s62, s84, 0x308000\n"
    "v_mfma_f32_16x16x32_bf16 v[136:139], a[224:227], v[232:235], v[136:139]\n"
    "buffer_load_dwordx4 v255, s[44:47], s62 offen lds\n"
    "v_mfma_f32_16x16x32_bf16 v[168:171], a[224:227], v[236:239], v[168:171]\n"
    "ds_read_b128 v[192:195], v248 offset:24576\n"
    "v_mfma_f32_16x16x32_bf16 v[12:15], a[228:231], v[216:219], v[12:15]\n"
    "ds_read_b128 v[196:199], v248 offset:26624\n"
    "s_add_u32 m0, s71, 0x10000\n"
    "v_mfma_f32_16x16x32_bf16 v[44:47], a[228:231], v[220:223], v[44:47]\n"
    "ds_read_b128 v[200:203], v248 offset:28672\n"
    "s_add_u32 s62, s66, 0x380000\n"
    "v_mfma_f32_16x16x32_bf16 v[76:79], a[228:231], v[224:227], v[76:79]\n"
    "buffer_load_dwordx4 v255, s[44:47], s62 offen lds\n"
    "v_mfma_f32_16x16x32_bf16 v[108:111], a[228:231], v[228:231], v[108:111]\n"
    "ds_read_b128 v[204:207], v248 offset:30720\n"
    "v_mfma_f32_16x16x32_bf16 v[140:143], a[228:231], v[232:235], v[140:143]\n"
    "ds_read_b128 v[208:211], v248 offset:32768\n"
    "s_add_u32 m0, s71, 0x10400\n"
    "v_mfma_f32_16x16x32_bf16 v[172:175], a[228:231], v[236:239], v[172:175]\n"
    "ds_read_b128 v[212:215], v248 offset:34816\n"
    "s_add_u32 s62, s66, 0x388000\n"
    "v_mfma_f32_16x16x32_bf16 v[16:19], a[232:235], v[216:219], v[16:19]\n"
    "buffer_load_dwordx4 v255, s[44:47], s62 offen lds\n"
    "v_mfma_f32_16x16x32_bf16 v[48:51], a[232:235], v[220:223], v[48:51]\n"
    "ds_read_b64_tr_b16 a[224:225], v251 offset:0\n"
    "v_mfma_f32_16x16x32_bf16 v[80:83], a[232:235], v[224:227], v[80:83]\n"
    "ds_read_b64_tr_b16 a[226:227], v251 offset:1024\n"
    "s_add_u32 m0, s74, 0xc000\n"
    "v_mfma_f32_16x16x32_bf16 v[112:115], a[232:235], v[228:231], v[112:115]\n"
    "ds_read_b64_tr_b16 a[228:229], v251 offset:8\n"
    "s_add_u32 s62, s73, 0x300\n"
    "v_mfma_f32_16x16x32_bf16 v[144:147], a[232:235], v[232:235], v[144:147]\n"
    "buffer_load_dwordx4 v254, s[40:43], s62 offen lds\n"
    "v_mfma_f32_16x16x32_bf16 v[176:179], a[232:235], v[236:239], v[176:179]\n"
    "ds_read_b64_tr_b16 a[230:231], v251 offset:1032\n"
    "v_mfma_f32_16x16x32_bf16 v[20:23], a[236:239], v[216:219], v[20:23]\n"
    "ds_read_b64_tr_b16 a[232:233], v252 offset:0\n"
    "v_mfma_f32_16x16x32_bf16 v[52:55], a[236:239], v[220:223], v[52:55]\n"
    "ds_read_b64_tr_b16 a[234:235], v252 offset:1024\n"
    "s_add_u32 m0, s74, 0xc400\n"
    "v_mfma_f32_16x16x32_bf16 v[84:87], a[236:239], v[224:227], v[84:87]\n"
    "s_add_u32 s62, s73, 0x2b00\n"
    "v_mfma_f32_16x16x32_bf16 v[116:119], a[236:239], v[228:231], v[116:119]\n"
    "buffer_load_dwordx4 v254, s[40:43], s62 offen lds\n"
    "v_mfma_f32_16x16x32_bf16 v[148:151], a[236:239], v[232:235], v[148:151]\n"
    "s_add_u32 m0, s74, 0xc800\n"
    "v_mfma_f32_16x16x32_bf16 v[180:183], a[236:239], v[236:239], v[180:183]\n"
    "s_add_u32 s62, s73, 0x5300\n"
    "v_mfma_f32_16x16x32_bf16 v[24:27], a[240:243], v[216:219], v[24:27]\n"
    "buffer_load_dwordx4 v254, s[40:43], s62 offen lds\n"
    "v_mfma_f32_16x16x32_bf16 v[56:59], a[240:243], v[220:223], v[56:59]\n"
    "ds_read_b64_tr_b16 a[236:237], v252 offset:8\n"
    "v_mfma_f32_16x16x32_bf16 v[88:91], a[240:243], v[224:227], v[88:91]\n"
    "ds_read_b64_tr_b16 a[238:239], v252 offset:1032\n"
    "s_add_u32 m0, s65, 0x0\n"
    "v_mfma_f32_16x16x32_bf16 v[120:123], a[240:243], v[228:231], v[120:123]\n"
    "s_add_u32 s62, s64, 0x380\n"
    "v_mfma_f32_16x16x32_bf16 v[152:155], a[240:243], v[232:235], v[152:155]\n"
    "buffer_load_dwordx4 v254, s[40:43], s62 offen lds\n"
    "v_mfma_f32_16x16x32_bf16 v[184:187], a[240:243], v[236:239], v[184:187]\n"
    "v_mfma_f32_16x16x32_bf16 v[28:31], a[244:247], v[216:219], v[28:31]\n"
    "ds_read_b64_tr_b16 a[240:241], v253 offset:0\n"
    "v_mfma_f32_16x16x32_bf16 v[60:63], a[244:247], v[220:223], v[60:63]\n"
    "ds_read_b64_tr_b16 a[242:243], v253 offset:1024\n"
    "s_add_u32 m0, s65, 0x400\n"
    "v_mfma_f32_16x16x32_bf16 v[92:95], a[244:247], v[224:227], v[92:95]\n"
    "s_add_u32 s62, s64, 0x2b80\n"
    "v_mfma_f32_16x16x32_bf16 v[124:127], a[244:247], v[228:231], v[124:127]\n"
    "buffer_load_dwordx4 v254, s[40:43], s62 offen lds\n"
    "v_mfma_f32_16x16x32_bf16 v[156:159], a[244:247], v[232:235], v[156:159]\n"
    "v_mfma_f32_16x16x32_bf16 v[188:191], a[244:247], v[236:239], v[188:191]\n"
    "s_waitcnt lgkmcnt(8)\n"
    "v_mfma_f32_16x16x32_bf16 v[0:3], a[216:219], v[192:195], v[0:3]\n"
    "ds_read_b64_tr_b16 a[244:245], v253 offset:8\n"
    "v_mfma_f32_16x16x32_bf16 v[32:35], a[216:219], v[196:199], v[32:35]\n"
    "ds_read_b64_tr_b16 a[246:247], v253 offset:1032\n"
    "v_mfma_f32_16x16x32_bf16 v[64:67], a[216:219], v[200:203], v[64:67]\n"
    "s_add_u32 m0, s65, 0x800\n"
    "v_mfma_f32_16x16x32_bf16 v[96:99], a[216:219], v[204:207], v[96:99]\n"
    "s_add_u32 s62, s64, 0x5380\n"
    "v_mfma_f32_16x16x32_bf16 v[128:131], a[216:219], v[208:211], v[128:131]\n"
    "buffer_load_dwordx4 v254, s[40:43], s62 offen lds\n"
    "v_mfma_f32_16x16x32_bf16 v[160:163], a[216:219], v[212:215], v[160:163]\n"
    "v_mfma_f32_16x16x32_bf16 v[4:7], a[220:223], v[192:195], v[4:7]\n"
    "v_mfma_f32_16x16x32_bf16 v[36:39], a[220:223], v[196:199], v[36:39]\n"
    "v_mfma_f32_16x16x32_bf16 v[68:71], a[220:223], v[200:203], v[68:71]\n"
    "v_mfma_f32_16x16x32_bf16 v[100:103], a[220:223], v[204:207], v[100:103]\n"
    "v_mfma_f32_16x16x32_bf16 v[132:135], a[220:223], v[208:211], v[132:135]\n"
    "s_waitcnt vmcnt(12) lgkmcnt(0)\n"
    "s_barrier\n"
    "v_mfma_f32_16x16x32_bf16 v[164:167], a[220:223], v[212:215], v[164:167]\n"
    "ds_read_b64_tr_b16 a[216:217], v250 offset:16384\n"
    "s_add_u32 m0, s72, 0xc000\n"
    "s_add_u32 s62, s84, 0x340000\n"
    "v_mfma_f32_16x16x32_bf16 v[8:11], a[224:227], v[192:195], v[8:11]\n"
    "buffer_load_dwordx4 v255, s[44:47], s62 offen lds\n"
    "v_mfma_f32_16x16x32_bf16 v[40:43], a[224:227], v[196:199], v[40:43]\n"
    "ds_read_b64_tr_b16 a[218:219], v250 offset:17408\n"
    "v_mfma_f32_16x16x32_bf16 v[72:75], a[224:227], v[200:203], v[72:75]\n"
    "ds_read_b64_tr_b16 a[220:221], v250 offset:16392\n"
    "s_add_u32 m0, s72, 0xc400\n"
    "v_mfma_f32_16x16x32_bf16 v[104:107], a[224:227], v[204:207], v[104:107]\n"
    "ds_read_b64_tr_b16 a[222:223], v250 offset:17416\n"
    "s_add_u32 s62, s84, 0x348000\n"
    "v_mfma_f32_16x16x32_bf16 v[136:139], a[224:227], v[208:211], v[136:139]\n"
    "buffer_load_dwordx4 v255, s[44:47], s62 offen lds\n"
    "v_mfma_f32_16x16x32_bf16 v[168:171], a[224:227], v[212:215], v[168:171]\n"
    "ds_read_b128 v[216:219], v249 offset:24576\n"
    "v_mfma_f32_16x16x32_bf16 v[12:15], a[228:231], v[192:195], v[12:15]\n"
    "ds_read_b128 v[220:223], v249 offset:26624\n"
    "s_add_u32 m0, s71, 0x0\n"
    "v_mfma_f32_16x16x32_bf16 v[44:47], a[228:231], v[196:199], v[44:47]\n"
    "ds_read_b128 v[224:227], v249 offset:28672\n"
    "s_add_u32 s62, s66, 0x3c0000\n"
    "v_mfma_f32_16x16x32_bf16 v[76:79], a[228:231], v[200:203], v[76:79]\n"
    "buffer_load_dwordx4 v255, s[44:47], s62 offen lds\n"
    "v_mfma_f32_16x16x32_bf16 v[108:111], a[228:231], v[204:207], v[108:111]\n"
    "ds_read_b128 v[228:231], v249 offset:30720\n"
    "v_mfma_f32_16x16x32_bf16 v[140:143], a[228:231], v[208:211], v[140:143]\n"
    "ds_read_b128 v[232:235], v249 offset:32768\n"
    "s_add_u32 m0, s71, 0x400\n"
    "v_mfma_f32_16x16x32_bf16 v[172:175], a[228:231], v[212:215], v[172:175]\n"
    "ds_read_b128 v[236:239], v249 offset:34816\n"
    "s_add_u32 s62, s66, 0x3c8000\n"
    "v_mfma_f32_16x16x32_bf16 v[16:19], a[232:235], v[192:195], v[16:19]\n"
    "buffer_load_dwordx4 v255, s[44:47], s62 offen lds\n"
    "v_mfma_f32_16x16x32_bf16 v[48:51], a[232:235], v[196:199], v[48:51]\n"
    "ds_read_b64_tr_b16 a[224:225], v251 offset:16384\n"
    "v_mfma_f32_16x16x32_bf16 v[80:83], a[232:235], v[200:203], v[80:83]\n"
    "ds_read_b64_tr_b16 a[226:227], v251 offset:17408\n"
    "v_mfma_f32_16x16x32_bf16 v[112:115], a[232:235], v[204:207], v[112:115]\n"
    "ds_read_b64_tr_b16 a[228:229], v251 offset:16392\n"
    "v_mfma_f32_16x16x32_bf16 v[144:147], a[232:235], v[208:211], v[144:147]\n"
    "v_mfma_f32_16x16x32_bf16 v[176:179], a[232:235], v[212:215], v[176:179]\n"
    "ds_read_b64_tr_b16 a[230:231], v251 offset:17416\n"
    "v_mfma_f32_16x16x32_bf16 v[20:23], a[236:239], v[192:195], v[20:23]\n"
    "ds_read_b64_tr_b16 a[232:233], v252 offset:16384\n"
    "v_mfma_f32_16x16x32_bf16 v[52:55], a[236:239], v[196:199], v[52:55]\n"
    "ds_read_b64_tr_b16 a[234:235], v252 offset:17408\n"
    "v_mfma_f32_16x16x32_bf16 v[84:87], a[236:239], v[200:203], v[84:87]\n"
    "v_mfma_f32_16x16x32_bf16 v[116:119], a[236:239], v[204:207], v[116:119]\n"
    "v_mfma_f32_16x16x32_bf16 v[148:151], a[236:239], v[208:211], v[148:151]\n"
    "v_mfma_f32_16x16x32_bf16 v[180:183], a[236:239], v[212:215], v[180:183]\n"
    "v_mfma_f32_16x16x32_bf16 v[24:27], a[240:243], v[192:195], v[24:27]\n"
    "v_mfma_f32_16x16x32_bf16 v[56:59], a[240:243], v[196:199], v[56:59]\n"
    "ds_read_b64_tr_b16 a[236:237], v252 offset:16392\n"
    "v_mfma_f32_16x16x32_bf16 v[88:91], a[240:243], v[200:203], v[88:91]\n"
    "ds_read_b64_tr_b16 a[238:239], v252 offset:17416\n"
    "v_mfma_f32_16x16x32_bf16 v[120:123], a[240:243], v[204:207], v[120:123]\n"
    "v_mfma_f32_16x16x32_bf16 v[152:155], a[240:243], v[208:211], v[152:155]\n"
    "v_mfma_f32_16x16x32_bf16 v[184:187], a[240:243], v[212:215], v[184:187]\n"
    "v_mfma_f32_16x16x32_bf16 v[28:31], a[244:247], v[192:195], v[28:31]\n"
    "ds_read_b64_tr_b16 a[240:241], v253 offset:16384\n"
    "v_mfma_f32_16x16x32_bf16 v[60:63], a[244:247], v[196:199], v[60:63]\n"
    "ds_read_b64_tr_b16 a[242:243], v253 offset:17408\n"
    "v_mfma_f32_16x16x32_bf16 v[92:95], a[244:247], v[200:203], v[92:95]\n"
    "v_mfma_f32_16x16x32_bf16 v[124:127], a[244:247], v[204:207], v[124:127]\n"
    "v_mfma_f32_16x16x32_bf16 v[156:159], a[244:247], v[208:211], v[156:159]\n"
    "v_mfma_f32_16x16x32_bf16 v[188:191], a[244:247], v[212:215], v[188:191]\n"
    "s_waitcnt lgkmcnt(8)\n"
    "v_mfma_f32_16x16x32_bf16 v[0:3], a[216:219], v[216:219], v[0:3]\n"
    "ds_read_b64_tr_b16 a[244:245], v253 offset:16392\n"
    "v_mfma_f32_16x16x32_bf16 v[32:35], a[216:219], v[220:223], v[32:35]\n"
    "ds_read_b64_tr_b16 a[246:247], v253 offset:17416\n"
    "v_mfma_f32_16x16x32_bf16 v[64:67], a[216:219], v[224:227], v[64:67]\n"
    "v_mfma_f32_16x16x32_bf16 v[96:99], a[216:219], v[228:231], v[96:99]\n"
    "v_mfma_f32_16x16x32_bf16 v[128:131], a[216:219], v[232:235], v[128:131]\n"
    "v_mfma_f32_16x16x32_bf16 v[160:163], a[216:219], v[236:239], v[160:163]\n"
    "v_mfma_f32_16x16x32_bf16 v[4:7], a[220:223], v[216:219], v[4:7]\n"
    "v_mfma_f32_16x16x32_bf16 v[36:39], a[220:223], v[220:223], v[36:39]\n"
    "v_mfma_f32_16x16x32_bf16 v[68:71], a[220:223], v[224:227], v[68:71]\n"
    "v_mfma_f32_16x16x32_bf16 v[100:103], a[220:223], v[228:231], v[100:103]\n"
    "v_mfma_f32_16x16x32_bf16 v[132:135], a[220:223], v[232:235], v[132:135]\n"
    "s_waitcnt vmcnt(7) lgkmcnt(0)\n"
    "s_barrier\n"
    "v_mfma_f32_16x16x32_bf16 v[164:167], a[220:223], v[236:239], v[164:167]\n"
    "ds_read_b64_tr_b16 a[216:217], v250 offset:32768\n"
    "s_add_u32 m0, s72, 0x10000\n"
    "s_add_u32 s62, s84, 0x380000\n"
    "v_mfma_f32_16x16x32_bf16 v[8:11], a[224:227], v[216:219], v[8:11]\n"
    "buffer_load_dwordx4 v255, s[44:47], s62 offen lds\n"
    "v_mfma_f32_16x16x32_bf16 v[40:43], a[224:227], v[220:223], v[40:43]\n"
    "ds_read_b64_tr_b16 a[218:219], v250 offset:33792\n"
    "v_mfma_f32_16x16x32_bf16 v[72:75], a[224:227], v[224:227], v[72:75]\n"
    "ds_read_b64_tr_b16 a[220:221], v250 offset:32776\n"
    "s_add_u32 m0, s72, 0x10400\n"
    "v_mfma_f32_16x16x32_bf16 v[104:107], a[224:227], v[228:231], v[104:107]\n"
    "ds_read_b64_tr_b16 a[222:223], v250 offset:33800\n"
    "s_add_u32 s62, s84, 0x388000\n"
    "v_mfma_f32_16x16x32_bf16 v[136:139], a[224:227], v[232:235], v[136:139]\n"
    "buffer_load_dwordx4 v255, s[44:47], s62 offen lds\n"
    "v_mfma_f32_16x16x32_bf16 v[168:171], a[224:227], v[236:239], v[168:171]\n"
    "ds_read_b128 v[192:195], v248 offset:49152\n"
    "v_mfma_f32_16x16x32_bf16 v[12:15], a[228:231], v[216:219], v[12:15]\n"
    "ds_read_b128 v[196:199], v248 offset:51200\n"
    "s_add_u32 m0, s71, 0x4000\n"
    "v_mfma_f32_16x16x32_bf16 v[44:47], a[228:231], v[220:223], v[44:47]\n"
    "ds_read_b128 v[200:203], v248 offset:53248\n"
    "s_add_u32 s62, s66, 0x400000\n"
    "v_mfma_f32_16x16x32_bf16 v[76:79], a[228:231], v[224:227], v[76:79]\n"
    "buffer_load_dwordx4 v255, s[44:47], s62 offen lds\n"
    "v_mfma_f32_16x16x32_bf16 v[108:111], a[228:231], v[228:231], v[108:111]\n"
    "ds_read_b128 v[204:207], v248 offset:55296\n"
    "v_mfma_f32_16x16x32_bf16 v[140:143], a[228:231], v[232:235], v[140:143]\n"
    "ds_read_b128 v[208:211], v248 offset:57344\n"
    "s_add_u32 m0, s71, 0x4400\n"
    "v_mfma_f32_16x16x32_bf16 v[172:175], a[228:231], v[236:239], v[172:175]\n"
    "ds_read_b128 v[212:215], v248 offset:59392\n"
    "s_add_u32 s62, s66, 0x408000\n"
    "v_mfma_f32_16x16x32_bf16 v[16:19], a[232:235], v[216:219], v[16:19]\n"
    "buffer_load_dwordx4 v255, s[44:47], s62 offen lds\n"
    "v_mfma_f32_16x16x32_bf16 v[48:51], a[232:235], v[220:223], v[48:51]\n"
    "ds_read_b64_tr_b16 a[224:225], v251 offset:32768\n"
    "v_mfma_f32_16x16x32_bf16 v[80:83], a[232:235], v[224:227], v[80:83]\n"
    "ds_read_b64_tr_b16 a[226:227], v251 offset:33792\n"
    "s_add_u32 m0, s74, 0x0\n"
    "v_mfma_f32_16x16x32_bf16 v[112:115], a[232:235], v[228:231], v[112:115]\n"
    "ds_read_b64_tr_b16 a[228:229], v251 offset:32776\n"
    "s_add_u32 s62, s73, 0x380\n"
    "v_mfma_f32_16x16x32_bf16 v[144:147], a[232:235], v[232:235], v[144:147]\n"
    "buffer_load_dwordx4 v254, s[40:43], s62 offen lds\n"
    "v_mfma_f32_16x16x32_bf16 v[176:179], a[232:235], v[236:239], v[176:179]\n"
    "ds_read_b64_tr_b16 a[230:231], v251 offset:33800\n"
    "v_mfma_f32_16x16x32_bf16 v[20:23], a[236:239], v[216:219], v[20:23]\n"
    "ds_read_b64_tr_b16 a[232:233], v252 offset:32768\n"
    "v_mfma_f32_16x16x32_bf16 v[52:55], a[236:239], v[220:223], v[52:55]\n"
    "ds_read_b64_tr_b16 a[234:235], v252 offset:33792\n"
    "s_add_u32 m0, s74, 0x400\n"
    "v_mfma_f32_16x16x32_bf16 v[84:87], a[236:239], v[224:227], v[84:87]\n"
    "s_add_u32 s62, s73, 0x2b80\n"
    "v_mfma_f32_16x16x32_bf16 v[116:119], a[236:239], v[228:231], v[116:119]\n"
    "buffer_load_dwordx4 v254, s[40:43], s62 offen lds\n"
    "v_mfma_f32_16x16x32_bf16 v[148:151], a[236:239], v[232:235], v[148:151]\n"
    "s_add_u32 m0, s74, 0x800\n"
    "v_mfma_f32_16x16x32_bf16 v[180:183], a[236:239], v[236:239], v[180:183]\n"
    "s_add_u32 s62, s73, 0x5380\n"
    "v_mfma_f32_16x16x32_bf16 v[24:27], a[240:243], v[216:219], v[24:27]\n"
    "buffer_load_dwordx4 v254, s[40:43], s62 offen lds\n"
    "v_mfma_f32_16x16x32_bf16 v[56:59], a[240:243], v[220:223], v[56:59]\n"
    "ds_read_b64_tr_b16 a[236:237], v252 offset:32776\n"
    "v_mfma_f32_16x16x32_bf16 v[88:91], a[240:243], v[224:227], v[88:91]\n"
    "ds_read_b64_tr_b16 a[238:239], v252 offset:33800\n"
    "s_add_u32 m0, s65, 0x6000\n"
    "v_mfma_f32_16x16x32_bf16 v[120:123], a[240:243], v[228:231], v[120:123]\n"
    "s_add_u32 s62, s64, 0x400\n"
    "v_mfma_f32_16x16x32_bf16 v[152:155], a[240:243], v[232:235], v[152:155]\n"
    "buffer_load_dwordx4 v254, s[40:43], s62 offen lds\n"
    "v_mfma_f32_16x16x32_bf16 v[184:187], a[240:243], v[236:239], v[184:187]\n"
    "v_mfma_f32_16x16x32_bf16 v[28:31], a[244:247], v[216:219], v[28:31]\n"
    "ds_read_b64_tr_b16 a[240:241], v253 offset:32768\n"
    "v_mfma_f32_16x16x32_bf16 v[60:63], a[244:247], v[220:223], v[60:63]\n"
    "ds_read_b64_tr_b16 a[242:243], v253 offset:33792\n"
    "s_add_u32 m0, s65, 0x6400\n"
    "v_mfma_f32_16x16x32_bf16 v[92:95], a[244:247], v[224:227], v[92:95]\n"
    "s_add_u32 s62, s64, 0x2c00\n"
    "v_mfma_f32_16x16x32_bf16 v[124:127], a[244:247], v[228:231], v[124:127]\n"
    "buffer_load_dwordx4 v254, s[40:43], s62 offen lds\n"
    "v_mfma_f32_16x16x32_bf16 v[156:159], a[244:247], v[232:235], v[156:159]\n"
    "v_mfma_f32_16x16x32_bf16 v[188:191], a[244:247], v[236:239], v[188:191]\n"
    "s_waitcnt lgkmcnt(8)\n"
    "v_mfma_f32_16x16x32_bf16 v[0:3], a[216:219], v[192:195], v[0:3]\n"
    "ds_read_b64_tr_b16 a[244:245], v253 offset:32776\n"
    "v_mfma_f32_16x16x32_bf16 v[32:35], a[216:219], v[196:199], v[32:35]\n"
    "ds_read_b64_tr_b16 a[246:247], v253 offset:33800\n"
    "v_mfma_f32_16x16x32_bf16 v[64:67], a[216:219], v[200:203], v[64:67]\n"
    "s_add_u32 m0, s65, 0x6800\n"
    "v_mfma_f32_16x16x32_bf16 v[96:99], a[216:219], v[204:207], v[96:99]\n"
    "s_add_u32 s62, s64, 0x5400\n"
    "v_mfma_f32_16x16x32_bf16 v[128:131], a[216:219], v[208:211], v[128:131]\n"
    "buffer_load_dwordx4 v254, s[40:43], s62 offen lds\n"
    "v_mfma_f32_16x16x32_bf16 v[160:163], a[216:219], v[212:215], v[160:163]\n"
    "v_mfma_f32_16x16x32_bf16 v[4:7], a[220:223], v[192:195], v[4:7]\n"
    "v_mfma_f32_16x16x32_bf16 v[36:39], a[220:223], v[196:199], v[36:39]\n"
    "v_mfma_f32_16x16x32_bf16 v[68:71], a[220:223], v[200:203], v[68:71]\n"
    "v_mfma_f32_16x16x32_bf16 v[100:103], a[220:223], v[204:207], v[100:103]\n"
    "v_mfma_f32_16x16x32_bf16 v[132:135], a[220:223], v[208:211], v[132:135]\n"
    "s_waitcnt vmcnt(12) lgkmcnt(0)\n"
    "s_barrier\n"
    "v_mfma_f32_16x16x32_bf16 v[164:167], a[220:223], v[212:215], v[164:167]\n"
    "ds_read_b64_tr_b16 a[216:217], v250 offset:49152\n"
    "s_add_u32 m0, s72, 0x0\n"
    "s_add_u32 s62, s84, 0x3c0000\n"
    "v_mfma_f32_16x16x32_bf16 v[8:11], a[224:227], v[192:195], v[8:11]\n"
    "buffer_load_dwordx4 v255, s[44:47], s62 offen lds\n"
    "v_mfma_f32_16x16x32_bf16 v[40:43], a[224:227], v[196:199], v[40:43]\n"
    "ds_read_b64_tr_b16 a[218:219], v250 offset:50176\n"
    "v_mfma_f32_16x16x32_bf16 v[72:75], a[224:227], v[200:203], v[72:75]\n"
    "ds_read_b64_tr_b16 a[220:221], v250 offset:49160\n"
    "s_add_u32 m0, s72, 0x400\n"
    "v_mfma_f32_16x16x32_bf16 v[104:107], a[224:227], v[204:207], v[104:107]\n"
    "ds_read_b64_tr_b16 a[222:223], v250 offset:50184\n"
    "s_add_u32 s62, s84, 0x3c8000\n"
    "v_mfma_f32_16x16x32_bf16 v[136:139], a[224:227], v[208:211], v[136:139]\n"
    "buffer_load_dwordx4 v255, s[44:47], s62 offen lds\n"
    "v_mfma_f32_16x16x32_bf16 v[168:171], a[224:227], v[212:215], v[168:171]\n"
    "ds_read_b128 v[216:219], v249 offset:49152\n"
    "v_mfma_f32_16x16x32_bf16 v[12:15], a[228:231], v[192:195], v[12:15]\n"
    "ds_read_b128 v[220:223], v249 offset:51200\n"
    "s_add_u32 m0, s71, 0x8000\n"
    "v_mfma_f32_16x16x32_bf16 v[44:47], a[228:231], v[196:199], v[44:47]\n"
    "ds_read_b128 v[224:227], v249 offset:53248\n"
    "s_add_u32 s62, s66, 0x440000\n"
    "v_mfma_f32_16x16x32_bf16 v[76:79], a[228:231], v[200:203], v[76:79]\n"
    "buffer_load_dwordx4 v255, s[44:47], s62 offen lds\n"
    "v_mfma_f32_16x16x32_bf16 v[108:111], a[228:231], v[204:207], v[108:111]\n"
    "ds_read_b128 v[228:231], v249 offset:55296\n"
    "v_mfma_f32_16x16x32_bf16 v[140:143], a[228:231], v[208:211], v[140:143]\n"
    "ds_read_b128 v[232:235], v249 offset:57344\n"
    "s_add_u32 m0, s71, 0x8400\n"
    "v_mfma_f32_16x16x32_bf16 v[172:175], a[228:231], v[212:215], v[172:175]\n"
    "ds_read_b128 v[236:239], v249 offset:59392\n"
    "s_add_u32 s62, s66, 0x448000\n"
    "v_mfma_f32_16x16x32_bf16 v[16:19], a[232:235], v[192:195], v[16:19]\n"
    "buffer_load_dwordx4 v255, s[44:47], s62 offen lds\n"
    "v_mfma_f32_16x16x32_bf16 v[48:51], a[232:235], v[196:199], v[48:51]\n"
    "ds_read_b64_tr_b16 a[224:225], v251 offset:49152\n"
    "v_mfma_f32_16x16x32_bf16 v[80:83], a[232:235], v[200:203], v[80:83]\n"
    "ds_read_b64_tr_b16 a[226:227], v251 offset:50176\n"
    "v_mfma_f32_16x16x32_bf16 v[112:115], a[232:235], v[204:207], v[112:115]\n"
    "ds_read_b64_tr_b16 a[228:229], v251 offset:49160\n"
    "v_mfma_f32_16x16x32_bf16 v[144:147], a[232:235], v[208:211], v[144:147]\n"
    "v_mfma_f32_16x16x32_bf16 v[176:179], a[232:235], v[212:215], v[176:179]\n"
    "ds_read_b64_tr_b16 a[230:231], v251 offset:50184\n"
    "v_mfma_f32_16x16x32_bf16 v[20:23], a[236:239], v[192:195], v[20:23]\n"
    "ds_read_b64_tr_b16 a[232:233], v252 offset:49152\n"
    "v_mfma_f32_16x16x32_bf16 v[52:55], a[236:239], v[196:199], v[52:55]\n"
    "ds_read_b64_tr_b16 a[234:235], v252 offset:50176\n"
    "v_mfma_f32_16x16x32_bf16 v[84:87], a[236:239], v[200:203], v[84:87]\n"
    "v_mfma_f32_16x16x32_bf16 v[116:119], a[236:239], v[204:207], v[116:119]\n"
    "v_mfma_f32_16x16x32_bf16 v[148:151], a[236:239], v[208:211], v[148:151]\n"
    "v_mfma_f32_16x16x32_bf16 v[180:183], a[236:239], v[212:215], v[180:183]\n"
    "v_mfma_f32_16x16x32_bf16 v[24:27], a[240:243], v[192:195], v[24:27]\n"
    "v_mfma_f32_16x16x32_bf16 v[56:59], a[240:243], v[196:199], v[56:59]\n"
    "ds_read_b64_tr_b16 a[236:237], v252 offset:49160\n"
    "v_mfma_f32_16x16x32_bf16 v[88:91], a[240:243], v[200:203], v[88:91]\n"
    "ds_read_b64_tr_b16 a[238:239], v252 offset:50184\n"
    "v_mfma_f32_16x16x32_bf16 v[120:123], a[240:243], v[204:207], v[120:123]\n"
    "v_mfma_f32_16x16x32_bf16 v[152:155], a[240:243], v[208:211], v[152:155]\n"
    "v_mfma_f32_16x16x32_bf16 v[184:187], a[240:243], v[212:215], v[184:187]\n"
    "v_mfma_f32_16x16x32_bf16 v[28:31], a[244:247], v[192:195], v[28:31]\n"
    "ds_read_b64_tr_b16 a[240:241], v253 offset:49152\n"
    "v_mfma_f32_16x16x32_bf16 v[60:63], a[244:247], v[196:199], v[60:63]\n"
    "ds_read_b64_tr_b16 a[242:243], v253 offset:50176\n"
    "v_mfma_f32_16x16x32_bf16 v[92:95], a[244:247], v[200:203], v[92:95]\n"
    "v_mfma_f32_16x16x32_bf16 v[124:127], a[244:247], v[204:207], v[124:127]\n"
    "v_mfma_f32_16x16x32_bf16 v[156:159], a[244:247], v[208:211], v[156:159]\n"
    "v_mfma_f32_16x16x32_bf16 v[188:191], a[244:247], v[212:215], v[188:191]\n"
    "s_waitcnt lgkmcnt(8)\n"
    "v_mfma_f32_16x16x32_bf16 v[0:3], a[216:219], v[216:219], v[0:3]\n"
    "ds_read_b64_tr_b16 a[244:245], v253 offset:49160\n"
    "v_mfma_f32_16x16x32_bf16 v[32:35], a[216:219], v[220:223], v[32:35]\n"
    "ds_read_b64_tr_b16 a[246:247], v253 offset:50184\n"
    "v_mfma_f32_16x16x32_bf16 v[64:67], a[216:219], v[224:227], v[64:67]\n"
    "v_mfma_f32_16x16x32_bf16 v[96:99], a[216:219], v[228:231], v[96:99]\n"
    "v_mfma_f32_16x16x32_bf16 v[128:131], a[216:219], v[232:235], v[128:131]\n"
    "v_mfma_f32_16x16x32_bf16 v[160:163], a[216:219], v[236:239], v[160:163]\n"
    "v_add_u32 v250, 0x10000, v250\n"
    "v_add_u32 v251, 0x10000, v251\n"
    "v_mfma_f32_16x16x32_bf16 v[4:7], a[220:223], v[216:219], v[4:7]\n"
    "v_mfma_f32_16x16x32_bf16 v[36:39], a[220:223], v[220:223], v[36:39]\n"
    "v_mfma_f32_16x16x32_bf16 v[68:71], a[220:223], v[224:227], v[68:71]\n"
    "v_mfma_f32_16x16x32_bf16 v[100:103], a[220:223], v[228:231], v[100:103]\n"
    "v_add_u32 v252, 0x10000, v252\n"
    "v_add_u32 v253, 0x10000, v253\n"
    "v_mfma_f32_16x16x32_bf16 v[132:135], a[220:223], v[232:235], v[132:135]\n"
    "s_waitcnt vmcnt(7) lgkmcnt(0)\n"
    "s_barrier\n"
    "v_mfma_f32_16x16x32_bf16 v[164:167], a[220:223], v[236:239], v[164:167]\n"
    "ds_read_b64_tr_b16 a[216:217], v250 offset:0\n"
    "s_add_u32 m0, s72, 0x4000\n"
    "s_add_u32 s62, s84, 0x400000\n"
    "v_mfma_f32_16x16x32_bf16 v[8:11], a[224:227], v[216:219], v[8:11]\n"
    "buffer_load_dwordx4 v255, s[44:47], s62 offen lds\n"
    "v_mfma_f32_16x16x32_bf16 v[40:43], a[224:227], v[220:223], v[40:43]\n"
    "ds_read_b64_tr_b16 a[218:219], v250 offset:1024\n"
    "v_mfma_f32_16x16x32_bf16 v[72:75], a[224:227], v[224:227], v[72:75]\n"
    "ds_read_b64_tr_b16 a[220:221], v250 offset:8\n"
    "s_add_u32 m0, s72, 0x4400\n"
    "v_mfma_f32_16x16x32_bf16 v[104:107], a[224:227], v[228:231], v[104:107]\n"
    "ds_read_b64_tr_b16 a[222:223], v250 offset:1032\n"
    "s_add_u32 s62, s84, 0x408000\n"
    "v_mfma_f32_16x16x32_bf16 v[136:139], a[224:227], v[232:235], v[136:139]\n"
    "buffer_load_dwordx4 v255, s[44:47], s62 offen lds\n"
    "v_mfma_f32_16x16x32_bf16 v[168:171], a[224:227], v[236:239], v[168:171]\n"
    "ds_read_b128 v[192:195], v248 offset:0\n"
    "v_mfma_f32_16x16x32_bf16 v[12:15], a[228:231], v[216:219], v[12:15]\n"
    "ds_read_b128 v[196:199], v248 offset:2048\n"
    "s_add_u32 m0, s71, 0xc000\n"
    "v_mfma_f32_16x16x32_bf16 v[44:47], a[228:231], v[220:223], v[44:47]\n"
    "ds_read_b128 v[200:203], v248 offset:4096\n"
    "s_add_u32 s62, s66, 0x480000\n"
    "v_mfma_f32_16x16x32_bf16 v[76:79], a[228:231], v[224:227], v[76:79]\n"
    "buffer_load_dwordx4 v255, s[44:47], s62 offen lds\n"
    "v_mfma_f32_16x16x32_bf16 v[108:111], a[228:231], v[228:231], v[108:111]\n"
    "ds_read_b128 v[204:207], v248 offset:6144\n"
    "v_mfma_f32_16x16x32_bf16 v[140:143], a[228:231], v[232:235], v[140:143]\n"
    "ds_read_b128 v[208:211], v248 offset:8192\n"
    "s_add_u32 m0, s71, 0xc400\n"
    "v_mfma_f32_16x16x32_bf16 v[172:175], a[228:231], v[236:239], v[172:175]\n"
    "ds_read_b128 v[212:215], v248 offset:10240\n"
    "s_add_u32 s62, s66, 0x488000\n"
    "v_mfma_f32_16x16x32_bf16 v[16:19], a[232:235], v[216:219], v[16:19]\n"
    "buffer_load_dwordx4 v255, s[44:47], s62 offen lds\n"
    "v_mfma_f32_16x16x32_bf16 v[48:51], a[232:235], v[220:223], v[48:51]\n"
    "ds_read_b64_tr_b16 a[224:225], v251 offset:0\n"
    "v_mfma_f32_16x16x32_bf16 v[80:83], a[232:235], v[224:227], v[80:83]\n"
    "ds_read_b64_tr_b16 a[226:227], v251 offset:1024\n"
    "s_add_u32 m0, s74, 0x6000\n"
    "v_mfma_f32_16x16x32_bf16 v[112:115], a[232:235], v[228:231], v[112:115]\n"
    "ds_read_b64_tr_b16 a[228:229], v251 offset:8\n"
    "s_add_u32 s62, s73, 0x400\n"
    "v_mfma_f32_16x16x32_bf16 v[144:147], a[232:235], v[232:235], v[144:147]\n"
    "buffer_load_dwordx4 v254, s[40:43], s62 offen lds\n"
    "v_mfma_f32_16x16x32_bf16 v[176:179], a[232:235], v[236:239], v[176:179]\n"
    "ds_read_b64_tr_b16 a[230:231], v251 offset:1032\n"
    "v_mfma_f32_16x16x32_bf16 v[20:23], a[236:239], v[216:219], v[20:23]\n"
    "ds_read_b64_tr_b16 a[232:233], v252 offset:0\n"
    "v_mfma_f32_16x16x32_bf16 v[52:55], a[236:239], v[220:223], v[52:55]\n"
    "ds_read_b64_tr_b16 a[234:235], v252 offset:1024\n"
    "s_add_u32 m0, s74, 0x6400\n"
    "v_mfma_f32_16x16x32_bf16 v[84:87], a[236:239], v[224:227], v[84:87]\n"
    "s_add_u32 s62, s73, 0x2c00\n"
    "v_mfma_f32_16x16x32_bf16 v[116:119], a[236:239], v[228:231], v[116:119]\n"
    "buffer_load_dwordx4 v254, s[40:43], s62 offen lds\n"
    "v_mfma_f32_16x16x32_bf16 v[148:151], a[236:239], v[232:235], v[148:151]\n"
    "s_add_u32 m0, s74, 0x6800\n"
    "v_mfma_f32_16x16x32_bf16 v[180:183], a[236:239], v[236:239], v[180:183]\n"
    "s_add_u32 s62, s73, 0x5400\n"
    "v_mfma_f32_16x16x32_bf16 v[24:27], a[240:243], v[216:219], v[24:27]\n"
    "buffer_load_dwordx4 v254, s[40:43], s62 offen lds\n"
    "v_mfma_f32_16x16x32_bf16 v[56:59], a[240:243], v[220:223], v[56:59]\n"
    "ds_read_b64_tr_b16 a[236:237], v252 offset:8\n"
    "v_mfma_f32_16x16x32_bf16 v[88:91], a[240:243], v[224:227], v[88:91]\n"
    "ds_read_b64_tr_b16 a[238:239], v252 offset:1032\n"
    "s_add_u32 m0, s65, 0xc000\n"
    "v_mfma_f32_16x16x32_bf16 v[120:123], a[240:243], v[228:231], v[120:123]\n"
    "s_add_u32 s62, s64, 0x480\n"
    "v_mfma_f32_16x16x32_bf16 v[152:155], a[240:243], v[232:235], v[152:155]\n"
    "buffer_load_dwordx4 v254, s[40:43], s62 offen lds\n"
    "v_mfma_f32_16x16x32_bf16 v[184:187], a[240:243], v[236:239], v[184:187]\n"
    "v_mfma_f32_16x16x32_bf16 v[28:31], a[244:247], v[216:219], v[28:31]\n"
    "ds_read_b64_tr_b16 a[240:241], v253 offset:0\n"
    "v_mfma_f32_16x16x32_bf16 v[60:63], a[244:247], v[220:223], v[60:63]\n"
    "ds_read_b64_tr_b16 a[242:243], v253 offset:1024\n"
    "s_add_u32 m0, s65, 0xc400\n"
    "v_mfma_f32_16x16x32_bf16 v[92:95], a[244:247], v[224:227], v[92:95]\n"
    "s_add_u32 s62, s64, 0x2c80\n"
    "v_mfma_f32_16x16x32_bf16 v[124:127], a[244:247], v[228:231], v[124:127]\n"
    "buffer_load_dwordx4 v254, s[40:43], s62 offen lds\n"
    "v_mfma_f32_16x16x32_bf16 v[156:159], a[244:247], v[232:235], v[156:159]\n"
    "v_mfma_f32_16x16x32_bf16 v[188:191], a[244:247], v[236:239], v[188:191]\n"
    "s_waitcnt lgkmcnt(8)\n"
    "v_mfma_f32_16x16x32_bf16 v[0:3], a[216:219], v[192:195], v[0:3]\n"
    "ds_read_b64_tr_b16 a[244:245], v253 offset:8\n"
    "v_mfma_f32_16x16x32_bf16 v[32:35], a[216:219], v[196:199], v[32:35]\n"
    "ds_read_b64_tr_b16 a[246:247], v253 offset:1032\n"
    "v_mfma_f32_16x16x32_bf16 v[64:67], a[216:219], v[200:203], v[64:67]\n"
    "s_add_u32 m0, s65, 0xc800\n"
    "v_mfma_f32_16x16x32_bf16 v[96:99], a[216:219], v[204:207], v[96:99]\n"
    "s_add_u32 s62, s64, 0x5480\n"
    "v_mfma_f32_16x16x32_bf16 v[128:131], a[216:219], v[208:211], v[128:131]\n"
    "buffer_load_dwordx4 v254, s[40:43], s62 offen lds\n"
    "v_mfma_f32_16x16x32_bf16 v[160:163], a[216:219], v[212:215], v[160:163]\n"
    "v_add_u32 v250, s75, v250\n"
    "v_add_u32 v251, s75, v251\n"
    "v_mfma_f32_16x16x32_bf16 v[4:7], a[220:223], v[192:195], v[4:7]\n"
    "v_mfma_f32_16x16x32_bf16 v[36:39], a[220:223], v[196:199], v[36:39]\n"
    "v_mfma_f32_16x16x32_bf16 v[68:71], a[220:223], v[200:203], v[68:71]\n"
    "v_mfma_f32_16x16x32_bf16 v[100:103], a[220:223], v[204:207], v[100:103]\n"
    "v_add_u32 v252, s75, v252\n"
    "v_add_u32 v253, s75, v253\n"
    "v_mfma_f32_16x16x32_bf16 v[132:135], a[220:223], v[208:211], v[132:135]\n"
    "s_waitcnt vmcnt(12) lgkmcnt(0)\n"
    "s_barrier\n"
    "v_mfma_f32_16x16x32_bf16 v[164:167], a[220:223], v[212:215], v[164:167]\n"
    "ds_read_b64_tr_b16 a[216:217], v250 offset:0\n"
    "s_add_u32 m0, s72, 0x8000\n"
    "s_add_u32 s62, s84, 0x440000\n"
    "v_mfma_f32_16x16x32_bf16 v[8:11], a[224:227], v[192:195], v[8:11]\n"
    "buffer_load_dwordx4 v255, s[44:47], s62 offen lds\n"
    "v_mfma_f32_16x16x32_bf16 v[40:43], a[224:227], v[196:199], v[40:43]\n"
    "ds_read_b64_tr_b16 a[218:219], v250 offset:1024\n"
    "v_mfma_f32_16x16x32_bf16 v[72:75], a[224:227], v[200:203], v[72:75]\n"
    "ds_read_b64_tr_b16 a[220:221], v250 offset:8\n"
    "s_add_u32 m0, s72, 0x8400\n"
    "v_mfma_f32_16x16x32_bf16 v[104:107], a[224:227], v[204:207], v[104:107]\n"
    "ds_read_b64_tr_b16 a[222:223], v250 offset:1032\n"
    "s_add_u32 s62, s84, 0x448000\n"
    "v_mfma_f32_16x16x32_bf16 v[136:139], a[224:227], v[208:211], v[136:139]\n"
    "buffer_load_dwordx4 v255, s[44:47], s62 offen lds\n"
    "v_mfma_f32_16x16x32_bf16 v[168:171], a[224:227], v[212:215], v[168:171]\n"
    "ds_read_b128 v[216:219], v249 offset:0\n"
    "v_mfma_f32_16x16x32_bf16 v[12:15], a[228:231], v[192:195], v[12:15]\n"
    "ds_read_b128 v[220:223], v249 offset:2048\n"
    "s_add_u32 m0, s71, 0x10000\n"
    "v_mfma_f32_16x16x32_bf16 v[44:47], a[228:231], v[196:199], v[44:47]\n"
    "ds_read_b128 v[224:227], v249 offset:4096\n"
    "s_add_u32 s62, s66, 0x4c0000\n"
    "v_mfma_f32_16x16x32_bf16 v[76:79], a[228:231], v[200:203], v[76:79]\n"
    "buffer_load_dwordx4 v255, s[44:47], s62 offen lds\n"
    "v_mfma_f32_16x16x32_bf16 v[108:111], a[228:231], v[204:207], v[108:111]\n"
    "ds_read_b128 v[228:231], v249 offset:6144\n"
    "v_mfma_f32_16x16x32_bf16 v[140:143], a[228:231], v[208:211], v[140:143]\n"
    "ds_read_b128 v[232:235], v249 offset:8192\n"
    "s_add_u32 m0, s71, 0x10400\n"
    "v_mfma_f32_16x16x32_bf16 v[172:175], a[228:231], v[212:215], v[172:175]\n"
    "ds_read_b128 v[236:239], v249 offset:10240\n"
    "s_add_u32 s62, s66, 0x4c8000\n"
    "v_mfma_f32_16x16x32_bf16 v[16:19], a[232:235], v[192:195], v[16:19]\n"
    "buffer_load_dwordx4 v255, s[44:47], s62 offen lds\n"
    "v_mfma_f32_16x16x32_bf16 v[48:51], a[232:235], v[196:199], v[48:51]\n"
    "ds_read_b64_tr_b16 a[224:225], v251 offset:0\n"
    "v_mfma_f32_16x16x32_bf16 v[80:83], a[232:235], v[200:203], v[80:83]\n"
    "ds_read_b64_tr_b16 a[226:227], v251 offset:1024\n"
    "s_add_u32 m0, s72, 0xc000\n"
    "v_mfma_f32_16x16x32_bf16 v[112:115], a[232:235], v[204:207], v[112:115]\n"
    "ds_read_b64_tr_b16 a[228:229], v251 offset:8\n"
    "s_add_u32 s62, s84, 0x480000\n"
    "v_mfma_f32_16x16x32_bf16 v[144:147], a[232:235], v[208:211], v[144:147]\n"
    "buffer_load_dwordx4 v255, s[44:47], s62 offen lds\n"
    "v_mfma_f32_16x16x32_bf16 v[176:179], a[232:235], v[212:215], v[176:179]\n"
    "ds_read_b64_tr_b16 a[230:231], v251 offset:1032\n"
    "v_mfma_f32_16x16x32_bf16 v[20:23], a[236:239], v[192:195], v[20:23]\n"
    "ds_read_b64_tr_b16 a[232:233], v252 offset:0\n"
    "v_mfma_f32_16x16x32_bf16 v[52:55], a[236:239], v[196:199], v[52:55]\n"
    "ds_read_b64_tr_b16 a[234:235], v252 offset:1024\n"
    "s_add_u32 m0, s72, 0xc400\n"
    "v_mfma_f32_16x16x32_bf16 v[84:87], a[236:239], v[200:203], v[84:87]\n"
    "s_add_u32 s62, s84, 0x488000\n"
    "v_mfma_f32_16x16x32_bf16 v[116:119], a[236:239], v[204:207], v[116:119]\n"
    "buffer_load_dwordx4 v255, s[44:47], s62 offen lds\n"
    "v_mfma_f32_16x16x32_bf16 v[148:151], a[236:239], v[208:211], v[148:151]\n"
    "s_add_u32 m0, s74, 0xc000\n"
    "v_mfma_f32_16x16x32_bf16 v[180:183], a[236:239], v[212:215], v[180:183]\n"
    "s_add_u32 s62, s73, 0x480\n"
    "v_mfma_f32_16x16x32_bf16 v[24:27], a[240:243], v[192:195], v[24:27]\n"
    "buffer_load_dwordx4 v254, s[40:43], s62 offen lds\n"
    "v_mfma_f32_16x16x32_bf16 v[56:59], a[240:243], v[196:199], v[56:59]\n"
    "ds_read_b64_tr_b16 a[236:237], v252 offset:8\n"
    "v_mfma_f32_16x16x32_bf16 v[88:91], a[240:243], v[200:203], v[88:91]\n"
    "ds_read_b64_tr_b16 a[238:239], v252 offset:1032\n"
    "s_add_u32 m0, s74, 0xc400\n"
    "v_mfma_f32_16x16x32_bf16 v[120:123], a[240:243], v[204:207], v[120:123]\n"
    "s_add_u32 s62, s73, 0x2c80\n"
    "v_mfma_f32_16x16x32_bf16 v[152:155], a[240:243], v[208:211], v[152:155]\n"
    "buffer_load_dwordx4 v254, s[40:43], s62 offen lds\n"
    "v_mfma_f32_16x16x32_bf16 v[184:187], a[240:243], v[212:215], v[184:187]\n"
    "v_mfma_f32_16x16x32_bf16 v[28:31], a[244:247], v[192:195], v[28:31]\n"
    "ds_read_b64_tr_b16 a[240:241], v253 offset:0\n"
    "v_mfma_f32_16x16x32_bf16 v[60:63], a[244:247], v[196:199], v[60:63]\n"
    "ds_read_b64_tr_b16 a[242:243], v253 offset:1024\n"
    "s_add_u32 m0, s74, 0xc800\n"
    "v_mfma_f32_16x16x32_bf16 v[92:95], a[244:247], v[200:203], v[92:95]\n"
    "s_add_u32 s62, s73, 0x5480\n"
    "v_mfma_f32_16x16x32_bf16 v[124:127], a[244:247], v[204:207], v[124:127]\n"
    "buffer_load_dwordx4 v254, s[40:43], s62 offen lds\n"
    "v_mfma_f32_16x16x32_bf16 v[156:159], a[244:247], v[208:211], v[156:159]\n"
    "v_mfma_f32_16x16x32_bf16 v[188:191], a[244:247], v[212:215], v[188:191]\n"
    "s_waitcnt lgkmcnt(8)\n"
    "v_mfma_f32_16x16x32_bf16 v[0:3], a[216:219], v[216:219], v[0:3]\n"
    "ds_read_b64_tr_b16 a[244:245], v253 offset:8\n"
    "v_mfma_f32_16x16x32_bf16 v[32:35], a[216:219], v[220:223], v[32:35]\n"
    "ds_read_b64_tr_b16 a[246:247], v253 offset:1032\n"
    "v_mfma_f32_16x16x32_bf16 v[64:67], a[216:219], v[224:227], v[64:67]\n"
    "s_add_u32 m0, s72, 0x10000\n"
    "v_mfma_f32_16x16x32_bf16 v[96:99], a[216:219], v[228:231], v[96:99]\n"
    "s_add_u32 s62, s84, 0x4c0000\n"
    "v_mfma_f32_16x16x32_bf16 v[128:131], a[216:219], v[232:235], v[128:131]\n"
    "buffer_load_dwordx4 v255, s[44:47], s62 offen lds\n"
    "v_mfma_f32_16x16x32_bf16 v[160:163], a[216:219], v[236:239], v[160:163]\n"
    "v_mfma_f32_16x16x32_bf16 v[4:7], a[220:223], v[216:219], v[4:7]\n"
    "s_add_u32 m0, s72, 0x10400\n"
    "v_mfma_f32_16x16x32_bf16 v[36:39], a[220:223], v[220:223], v[36:39]\n"
    "s_add_u32 s62, s84, 0x4c8000\n"
    "v_mfma_f32_16x16x32_bf16 v[68:71], a[220:223], v[224:227], v[68:71]\n"
    "buffer_load_dwordx4 v255, s[44:47], s62 offen lds\n"
    "v_mfma_f32_16x16x32_bf16 v[100:103], a[220:223], v[228:231], v[100:103]\n"
    "s_add_u32 s68, s77, 0x0\n"
    "s_add_u32 m0, s67, 0x0\n"
    "v_mfma_f32_16x16x32_bf16 v[132:135], a[220:223], v[232:235], v[132:135]\n"
    "s_waitcnt vmcnt(14) lgkmcnt(0)\n"
    "s_barrier\n"
    "v_mfma_f32_16x16x32_bf16 v[164:167], a[220:223], v[236:239], v[164:167]\n"
    "ds_read_b64_tr_b16 a[216:217], v250 offset:16384\n"
    "s_and_b32 s68, s68, 0x3ffff\n"
    "v_mfma_f32_16x16x32_bf16 v[8:11], a[224:227], v[216:219], v[8:11]\n"
    "buffer_load_dwordx4 v244, s[48:51], s68 offen nt lds\n"
    "v_mfma_f32_16x16x32_bf16 v[40:43], a[224:227], v[220:223], v[40:43]\n"
    "ds_read_b64_tr_b16 a[218:219], v250 offset:17408\n"
    "v_mfma_f32_16x16x32_bf16 v[72:75], a[224:227], v[224:227], v[72:75]\n"
    "ds_read_b64_tr_b16 a[220:221], v250 offset:16392\n"
    "v_mfma_f32_16x16x32_bf16 v[104:107], a[224:227], v[228:231], v[104:107]\n"
    "ds_read_b64_tr_b16 a[222:223], v250 offset:17416\n"
    "v_mfma_f32_16x16x32_bf16 v[136:139], a[224:227], v[232:235], v[136:139]\n"
    "v_mfma_f32_16x16x32_bf16 v[168:171], a[224:227], v[236:239], v[168:171]\n"
    "ds_read_b128 v[192:195], v248 offset:24576\n"
    "v_mfma_f32_16x16x32_bf16 v[12:15], a[228:231], v[216:219], v[12:15]\n"
    "ds_read_b128 v[196:199], v248 offset:26624\n"
    "v_mfma_f32_16x16x32_bf16 v[44:47], a[228:231], v[220:223], v[44:47]\n"
    "ds_read_b128 v[200:203], v248 offset:28672\n"
    "v_mfma_f32_16x16x32_bf16 v[76:79], a[228:231], v[224:227], v[76:79]\n"
    "v_mfma_f32_16x16x32_bf16 v[108:111], a[228:231], v[228:231], v[108:111]\n"
    "ds_read_b128 v[204:207], v248 offset:30720\n"
    "v_mfma_f32_16x16x32_bf16 v[140:143], a[228:231], v[232:235], v[140:143]\n"
    "ds_read_b128 v[208:211], v248 offset:32768\n"
    "v_mfma_f32_16x16x32_bf16 v[172:175], a[228:231], v[236:239], v[172:175]\n"
    "ds_read_b128 v[212:215], v248 offset:34816\n"
    "v_mfma_f32_16x16x32_bf16 v[16:19], a[232:235], v[216:219], v[16:19]\n"
    "buffer_load_dwordx4 v245, s[48:51], s68 offen offset:1024 nt lds\n"
    "v_mfma_f32_16x16x32_bf16 v[48:51], a[232:235], v[220:223], v[48:51]\n"
    "ds_read_b64_tr_b16 a[224:225], v251 offset:16384\n"
    "v_mfma_f32_16x16x32_bf16 v[80:83], a[232:235], v[224:227], v[80:83]\n"
    "ds_read_b64_tr_b16 a[226:227], v251 offset:17408\n"
    "v_mfma_f32_16x16x32_bf16 v[112:115], a[232:235], v[228:231], v[112:115]\n"
    "ds_read_b64_tr_b16 a[228:229], v251 offset:16392\n"
    "v_mfma_f32_16x16x32_bf16 v[144:147], a[232:235], v[232:235], v[144:147]\n"
    "v_mfma_f32_16x16x32_bf16 v[176:179], a[232:235], v[236:239], v[176:179]\n"
    "ds_read_b64_tr_b16 a[230:231], v251 offset:17416\n"
    "v_mfma_f32_16x16x32_bf16 v[20:23], a[236:239], v[216:219], v[20:23]\n"
    "ds_read_b64_tr_b16 a[232:233], v252 offset:16384\n"
    "v_mfma_f32_16x16x32_bf16 v[52:55], a[236:239], v[220:223], v[52:55]\n"
    "ds_read_b64_tr_b16 a[234:235], v252 offset:17408\n"
    "v_mfma_f32_16x16x32_bf16 v[84:87], a[236:239], v[224:227], v[84:87]\n"
    "v_mfma_f32_16x16x32_bf16 v[116:119], a[236:239], v[228:231], v[116:119]\n"
    "v_mfma_f32_16x16x32_bf16 v[148:151], a[236:239], v[232:235], v[148:151]\n"
    "v_mfma_f32_16x16x32_bf16 v[180:183], a[236:239], v[236:239], v[180:183]\n"
    "v_mfma_f32_16x16x32_bf16 v[24:27], a[240:243], v[216:219], v[24:27]\n"
    "buffer_load_dwordx4 v244, s[48:51], s68 offen offset:2048 nt lds\n"
    "v_mfma_f32_16x16x32_bf16 v[56:59], a[240:243], v[220:223], v[56:59]\n"
    "ds_read_b64_tr_b16 a[236:237], v252 offset:16392\n"
    "v_mfma_f32_16x16x32_bf16 v[88:91], a[240:243], v[224:227], v[88:91]\n"
    "ds_read_b64_tr_b16 a[238:239], v252 offset:17416\n"
    "v_mfma_f32_16x16x32_bf16 v[120:123], a[240:243], v[228:231], v[120:123]\n"
    "v_mfma_f32_16x16x32_bf16 v[152:155], a[240:243], v[232:235], v[152:155]\n"
    "v_mfma_f32_16x16x32_bf16 v[184:187], a[240:243], v[236:239], v[184:187]\n"
    "v_mfma_f32_16x16x32_bf16 v[28:31], a[244:247], v[216:219], v[28:31]\n"
    "ds_read_b64_tr_b16 a[240:241], v253 offset:16384\n"
    "v_mfma_f32_16x16x32_bf16 v[60:63], a[244:247], v[220:223], v[60:63]\n"
    "ds_read_b64_tr_b16 a[242:243], v253 offset:17408\n"
    "v_mfma_f32_16x16x32_bf16 v[92:95], a[244:247], v[224:227], v[92:95]\n"
    "v_mfma_f32_16x16x32_bf16 v[124:127], a[244:247], v[228:231], v[124:127]\n"
    "buffer_load_dwordx4 v245, s[48:51], s68 offen offset:3072 nt lds\n"
    "v_mfma_f32_16x16x32_bf16 v[156:159], a[244:247], v[232:235], v[156:159]\n"
    "v_mfma_f32_16x16x32_bf16 v[188:191], a[244:247], v[236:239], v[188:191]\n"
    "s_waitcnt lgkmcnt(8)\n"
    "v_mfma_f32_16x16x32_bf16 v[0:3], a[216:219], v[192:195], v[0:3]\n"
    "ds_read_b64_tr_b16 a[244:245], v253 offset:16392\n"
    "v_mfma_f32_16x16x32_bf16 v[32:35], a[216:219], v[196:199], v[32:35]\n"
    "ds_read_b64_tr_b16 a[246:247], v253 offset:17416\n"
    "v_mfma_f32_16x16x32_bf16 v[64:67], a[216:219], v[200:203], v[64:67]\n"
    "v_mfma_f32_16x16x32_bf16 v[96:99], a[216:219], v[204:207], v[96:99]\n"
    "v_mfma_f32_16x16x32_bf16 v[128:131], a[216:219], v[208:211], v[128:131]\n"
    "v_mfma_f32_16x16x32_bf16 v[160:163], a[216:219], v[212:215], v[160:163]\n"
    "v_mfma_f32_16x16x32_bf16 v[4:7], a[220:223], v[192:195], v[4:7]\n"
    "v_mfma_f32_16x16x32_bf16 v[36:39], a[220:223], v[196:199], v[36:39]\n"
    "v_mfma_f32_16x16x32_bf16 v[68:71], a[220:223], v[200:203], v[68:71]\n"
    "v_mfma_f32_16x16x32_bf16 v[100:103], a[220:223], v[204:207], v[100:103]\n"
    "s_add_u32 s69, s77, 0x0\n"
    "s_add_u32 m0, s67, 0x14000\n"
    "v_mfma_f32_16x16x32_bf16 v[132:135], a[220:223], v[208:211], v[132:135]\n"
    "s_waitcnt vmcnt(13) lgkmcnt(0)\n"
    "s_barrier\n"
    "v_mfma_f32_16x16x32_bf16 v[164:167], a[220:223], v[212:215], v[164:167]\n"
    "ds_read_b64_tr_b16 a[216:217], v250 offset:32768\n"
    "s_and_b32 s69, s69, 0x3ffff\n"
    "v_mfma_f32_16x16x32_bf16 v[8:11], a[224:227], v[192:195], v[8:11]\n"
    "buffer_load_dwordx4 v246, s[52:55], s69 offen lds\n"
    "v_mfma_f32_16x16x32_bf16 v[40:43], a[224:227], v[196:199], v[40:43]\n"
    "ds_read_b64_tr_b16 a[218:219], v250 offset:33792\n"
    "v_mfma_f32_16x16x32_bf16 v[72:75], a[224:227], v[200:203], v[72:75]\n"
    "ds_read_b64_tr_b16 a[220:221], v250 offset:32776\n"
    "v_mfma_f32_16x16x32_bf16 v[104:107], a[224:227], v[204:207], v[104:107]\n"
    "ds_read_b64_tr_b16 a[222:223], v250 offset:33800\n"
    "v_mfma_f32_16x16x32_bf16 v[136:139], a[224:227], v[208:211], v[136:139]\n"
    "v_mfma_f32_16x16x32_bf16 v[168:171], a[224:227], v[212:215], v[168:171]\n"
    "ds_read_b128 v[216:219], v249 offset:24576\n"
    "v_mfma_f32_16x16x32_bf16 v[12:15], a[228:231], v[192:195], v[12:15]\n"
    "ds_read_b128 v[220:223], v249 offset:26624\n"
    "v_mfma_f32_16x16x32_bf16 v[44:47], a[228:231], v[196:199], v[44:47]\n"
    "ds_read_b128 v[224:227], v249 offset:28672\n"
    "v_mfma_f32_16x16x32_bf16 v[76:79], a[228:231], v[200:203], v[76:79]\n"
    "v_mfma_f32_16x16x32_bf16 v[108:111], a[228:231], v[204:207], v[108:111]\n"
    "ds_read_b128 v[228:231], v249 offset:30720\n"
    "v_mfma_f32_16x16x32_bf16 v[140:143], a[228:231], v[208:211], v[140:143]\n"
    "ds_read_b128 v[232:235], v249 offset:32768\n"
    "v_mfma_f32_16x16x32_bf16 v[172:175], a[228:231], v[212:215], v[172:175]\n"
    "ds_read_b128 v[236:239], v249 offset:34816\n"
    "v_mfma_f32_16x16x32_bf16 v[16:19], a[232:235], v[192:195], v[16:19]\n"
    "buffer_load_dwordx4 v247, s[52:55], s69 offen offset:1024 lds\n"
    "v_mfma_f32_16x16x32_bf16 v[48:51], a[232:235], v[196:199], v[48:51]\n"
    "ds_read_b64_tr_b16 a[224:225], v251 offset:32768\n"
    "v_mfma_f32_16x16x32_bf16 v[80:83], a[232:235], v[200:203], v[80:83]\n"
    "ds_read_b64_tr_b16 a[226:227], v251 offset:33792\n"
    "v_mfma_f32_16x16x32_bf16 v[112:115], a[232:235], v[204:207], v[112:115]\n"
    "ds_read_b64_tr_b16 a[228:229], v251 offset:32776\n"
    "v_mfma_f32_16x16x32_bf16 v[144:147], a[232:235], v[208:211], v[144:147]\n"
    "v_mfma_f32_16x16x32_bf16 v[176:179], a[232:235], v[212:215], v[176:179]\n"
    "ds_read_b64_tr_b16 a[230:231], v251 offset:33800\n"
    "v_mfma_f32_16x16x32_bf16 v[20:23], a[236:239], v[192:195], v[20:23]\n"
    "ds_read_b64_tr_b16 a[232:233], v252 offset:32768\n"
    "v_mfma_f32_16x16x32_bf16 v[52:55], a[236:239], v[196:199], v[52:55]\n"
    "ds_read_b64_tr_b16 a[234:235], v252 offset:33792\n"
    "v_mfma_f32_16x16x32_bf16 v[84:87], a[236:239], v[200:203], v[84:87]\n"
    "v_mfma_f32_16x16x32_bf16 v[116:119], a[236:239], v[204:207], v[116:119]\n"
    "v_mfma_f32_16x16x32_bf16 v[148:151], a[236:239], v[208:211], v[148:151]\n"
    "v_mfma_f32_16x16x32_bf16 v[180:183], a[236:239], v[212:215], v[180:183]\n"
    "v_mfma_f32_16x16x32_bf16 v[24:27], a[240:243], v[192:195], v[24:27]\n"
    "buffer_load_dwordx4 v246, s[52:55], s69 offen offset:2048 lds\n"
    "v_mfma_f32_16x16x32_bf16 v[56:59], a[240:243], v[196:199], v[56:59]\n"
    "ds_read_b64_tr_b16 a[236:237], v252 offset:32776\n"
    "v_mfma_f32_16x16x32_bf16 v[88:91], a[240:243], v[200:203], v[88:91]\n"
    "ds_read_b64_tr_b16 a[238:239], v252 offset:33800\n"
    "v_mfma_f32_16x16x32_bf16 v[120:123], a[240:243], v[204:207], v[120:123]\n"
    "v_mfma_f32_16x16x32_bf16 v[152:155], a[240:243], v[208:211], v[152:155]\n"
    "v_mfma_f32_16x16x32_bf16 v[184:187], a[240:243], v[212:215], v[184:187]\n"
    "v_mfma_f32_16x16x32_bf16 v[28:31], a[244:247], v[192:195], v[28:31]\n"
    "ds_read_b64_tr_b16 a[240:241], v253 offset:32768\n"
    "v_mfma_f32_16x16x32_bf16 v[60:63], a[244:247], v[196:199], v[60:63]\n"
    "ds_read_b64_tr_b16 a[242:243], v253 offset:33792\n"
    "v_mfma_f32_16x16x32_bf16 v[92:95], a[244:247], v[200:203], v[92:95]\n"
    "v_mfma_f32_16x16x32_bf16 v[124:127], a[244:247], v[204:207], v[124:127]\n"
    "buffer_load_dwordx4 v247, s[52:55], s69 offen offset:3072 lds\n"
    "v_mfma_f32_16x16x32_bf16 v[156:159], a[244:247], v[208:211], v[156:159]\n"
    "v_mfma_f32_16x16x32_bf16 v[188:191], a[244:247], v[212:215], v[188:191]\n"
    "s_waitcnt lgkmcnt(8)\n"
    "v_mfma_f32_16x16x32_bf16 v[0:3], a[216:219], v[216:219], v[0:3]\n"
    "ds_read_b64_tr_b16 a[244:245], v253 offset:32776\n"
    "v_mfma_f32_16x16x32_bf16 v[32:35], a[216:219], v[220:223], v[32:35]\n"
    "ds_read_b64_tr_b16 a[246:247], v253 offset:33800\n"
    "v_mfma_f32_16x16x32_bf16 v[64:67], a[216:219], v[224:227], v[64:67]\n"
    "v_mfma_f32_16x16x32_bf16 v[96:99], a[216:219], v[228:231], v[96:99]\n"
    "v_mfma_f32_16x16x32_bf16 v[128:131], a[216:219], v[232:235], v[128:131]\n"
    "v_mfma_f32_16x16x32_bf16 v[160:163], a[216:219], v[236:239], v[160:163]\n"
    "v_mfma_f32_16x16x32_bf16 v[4:7], a[220:223], v[216:219], v[4:7]\n"
    "v_mfma_f32_16x16x32_bf16 v[36:39], a[220:223], v[220:223], v[36:39]\n"
    "v_mfma_f32_16x16x32_bf16 v[68:71], a[220:223], v[224:227], v[68:71]\n"
    "v_mfma_f32_16x16x32_bf16 v[100:103], a[220:223], v[228:231], v[100:103]\n"
    "s_add_u32 s68, s77, 0x2000\n"
    "s_add_u32 m0, s67, 0x4000\n"
    "v_mfma_f32_16x16x32_bf16 v[132:135], a[220:223], v[232:235], v[132:135]\n"
    "s_waitcnt vmcnt(10) lgkmcnt(0)\n"
    "s_barrier\n"
    "v_mfma_f32_16x16x32_bf16 v[164:167], a[220:223], v[236:239], v[164:167]\n"
    "ds_read_b64_tr_b16 a[216:217], v250 offset:49152\n"
    "s_and_b32 s68, s68, 0x3ffff\n"
    "v_mfma_f32_16x16x32_bf16 v[8:11], a[224:227], v[216:219], v[8:11]\n"
    "buffer_load_dwordx4 v244, s[48:51], s68 offen nt lds\n"
    "v_mfma_f32_16x16x32_bf16 v[40:43], a[224:227], v[220:223], v[40:43]\n"
    "ds_read_b64_tr_b16 a[218:219], v250 offset:50176\n"
    "v_mfma_f32_16x16x32_bf16 v[72:75], a[224:227], v[224:227], v[72:75]\n"
    "ds_read_b64_tr_b16 a[220:221], v250 offset:49160\n"
    "v_mfma_f32_16x16x32_bf16 v[104:107], a[224:227], v[228:231], v[104:107]\n"
    "ds_read_b64_tr_b16 a[222:223], v250 offset:50184\n"
    "v_mfma_f32_16x16x32_bf16 v[136:139], a[224:227], v[232:235], v[136:139]\n"
    "v_mfma_f32_16x16x32_bf16 v[168:171], a[224:227], v[236:239], v[168:171]\n"
    "ds_read_b128 v[192:195], v248 offset:49152\n"
    "v_mfma_f32_16x16x32_bf16 v[12:15], a[228:231], v[216:219], v[12:15]\n"
    "ds_read_b128 v[196:199], v248 offset:51200\n"
    "v_mfma_f32_16x16x32_bf16 v[44:47], a[228:231], v[220:223], v[44:47]\n"
    "ds_read_b128 v[200:203], v248 offset:53248\n"
    "v_mfma_f32_16x16x32_bf16 v[76:79], a[228:231], v[224:227], v[76:79]\n"
    "v_mfma_f32_16x16x32_bf16 v[108:111], a[228:231], v[228:231], v[108:111]\n"
    "ds_read_b128 v[204:207], v248 offset:55296\n"
    "v_mfma_f32_16x16x32_bf16 v[140:143], a[228:231], v[232:235], v[140:143]\n"
    "ds_read_b128 v[208:211], v248 offset:57344\n"
    "v_mfma_f32_16x16x32_bf16 v[172:175], a[228:231], v[236:239], v[172:175]\n"
    "ds_read_b128 v[212:215], v248 offset:59392\n"
    "v_mfma_f32_16x16x32_bf16 v[16:19], a[232:235], v[216:219], v[16:19]\n"
    "buffer_load_dwordx4 v245, s[48:51], s68 offen offset:1024 nt lds\n"
    "v_mfma_f32_16x16x32_bf16 v[48:51], a[232:235], v[220:223], v[48:51]\n"
    "ds_read_b64_tr_b16 a[224:225], v251 offset:49152\n"
    "v_mfma_f32_16x16x32_bf16 v[80:83], a[232:235], v[224:227], v[80:83]\n"
    "ds_read_b64_tr_b16 a[226:227], v251 offset:50176\n"
    "v_mfma_f32_16x16x32_bf16 v[112:115], a[232:235], v[228:231], v[112:115]\n"
    "ds_read_b64_tr_b16 a[228:229], v251 offset:49160\n"
    "v_mfma_f32_16x16x32_bf16 v[144:147], a[232:235], v[232:235], v[144:147]\n"
    "v_mfma_f32_16x16x32_bf16 v[176:179], a[232:235], v[236:239], v[176:179]\n"
    "ds_read_b64_tr_b16 a[230:231], v251 offset:50184\n"
    "v_mfma_f32_16x16x32_bf16 v[20:23], a[236:239], v[216:219], v[20:23]\n"
    "ds_read_b64_tr_b16 a[232:233], v252 offset:49152\n"
    "v_mfma_f32_16x16x32_bf16 v[52:55], a[236:239], v[220:223], v[52:55]\n"
    "ds_read_b64_tr_b16 a[234:235], v252 offset:50176\n"
    "v_mfma_f32_16x16x32_bf16 v[84:87], a[236:239], v[224:227], v[84:87]\n"
    "v_mfma_f32_16x16x32_bf16 v[116:119], a[236:239], v[228:231], v[116:119]\n"
    "v_mfma_f32_16x16x32_bf16 v[148:151], a[236:239], v[232:235], v[148:151]\n"
    "v_mfma_f32_16x16x32_bf16 v[180:183], a[236:239], v[236:239], v[180:183]\n"
    "v_mfma_f32_16x16x32_bf16 v[24:27], a[240:243], v[216:219], v[24:27]\n"
    "buffer_load_dwordx4 v244, s[48:51], s68 offen offset:2048 nt lds\n"
    "v_mfma_f32_16x16x32_bf16 v[56:59], a[240:243], v[220:223], v[56:59]\n"
    "ds_read_b64_tr_b16 a[236:237], v252 offset:49160\n"
    "v_mfma_f32_16x16x32_bf16 v[88:91], a[240:243], v[224:227], v[88:91]\n"
    "ds_read_b64_tr_b16 a[238:239], v252 offset:50184\n"
    "v_mfma_f32_16x16x32_bf16 v[120:123], a[240:243], v[228:231], v[120:123]\n"
    "v_mfma_f32_16x16x32_bf16 v[152:155], a[240:243], v[232:235], v[152:155]\n"
    "v_mfma_f32_16x16x32_bf16 v[184:187], a[240:243], v[236:239], v[184:187]\n"
    "v_mfma_f32_16x16x32_bf16 v[28:31], a[244:247], v[216:219], v[28:31]\n"
    "ds_read_b64_tr_b16 a[240:241], v253 offset:49152\n"
    "v_mfma_f32_16x16x32_bf16 v[60:63], a[244:247], v[220:223], v[60:63]\n"
    "ds_read_b64_tr_b16 a[242:243], v253 offset:50176\n"
    "v_mfma_f32_16x16x32_bf16 v[92:95], a[244:247], v[224:227], v[92:95]\n"
    "v_mfma_f32_16x16x32_bf16 v[124:127], a[244:247], v[228:231], v[124:127]\n"
    "buffer_load_dwordx4 v245, s[48:51], s68 offen offset:3072 nt lds\n"
    "v_mfma_f32_16x16x32_bf16 v[156:159], a[244:247], v[232:235], v[156:159]\n"
    "v_mfma_f32_16x16x32_bf16 v[188:191], a[244:247], v[236:239], v[188:191]\n"
    "s_waitcnt lgkmcnt(8)\n"
    "v_mfma_f32_16x16x32_bf16 v[0:3], a[216:219], v[192:195], v[0:3]\n"
    "ds_read_b64_tr_b16 a[244:245], v253 offset:49160\n"
    "v_mfma_f32_16x16x32_bf16 v[32:35], a[216:219], v[196:199], v[32:35]\n"
    "ds_read_b64_tr_b16 a[246:247], v253 offset:50184\n"
    "v_mfma_f32_16x16x32_bf16 v[64:67], a[216:219], v[200:203], v[64:67]\n"
    "v_mfma_f32_16x16x32_bf16 v[96:99], a[216:219], v[204:207], v[96:99]\n"
    "v_mfma_f32_16x16x32_bf16 v[128:131], a[216:219], v[208:211], v[128:131]\n"
    "v_mfma_f32_16x16x32_bf16 v[160:163], a[216:219], v[212:215], v[160:163]\n"
    "v_add_u32 v250, 0x10000, v250\n"
    "v_add_u32 v251, 0x10000, v251\n"
    "v_mfma_f32_16x16x32_bf16 v[4:7], a[220:223], v[192:195], v[4:7]\n"
    "v_mfma_f32_16x16x32_bf16 v[36:39], a[220:223], v[196:199], v[36:39]\n"
    "v_mfma_f32_16x16x32_bf16 v[68:71], a[220:223], v[200:203], v[68:71]\n"
    "v_mfma_f32_16x16x32_bf16 v[100:103], a[220:223], v[204:207], v[100:103]\n"
    "v_add_u32 v252, 0x10000, v252\n"
    "v_add_u32 v253, 0x10000, v253\n"
    "s_add_u32 s69, s77, 0x2000\n"
    "s_add_u32 m0, s67, 0x18000\n"
    "v_mfma_f32_16x16x32_bf16 v[132:135], a[220:223], v[208:211], v[132:135]\n"
    "s_waitcnt vmcnt(12) lgkmcnt(0)\n"
    "s_barrier\n"
    "v_mfma_f32_16x16x32_bf16 v[164:167], a[220:223], v[212:215], v[164:167]\n"
    "ds_read_b64_tr_b16 a[216:217], v250 offset:0\n"
    "s_and_b32 s69, s69, 0x3ffff\n"
    "v_mfma_f32_16x16x32_bf16 v[8:11], a[224:227], v[192:195], v[8:11]\n"
    "buffer_load_dwordx4 v246, s[52:55], s69 offen lds\n"
    "v_mfma_f32_16x16x32_bf16 v[40:43], a[224:227], v[196:199], v[40:43]\n"
    "ds_read_b64_tr_b16 a[218:219], v250 offset:1024\n"
    "v_mfma_f32_16x16x32_bf16 v[72:75], a[224:227], v[200:203], v[72:75]\n"
    "ds_read_b64_tr_b16 a[220:221], v250 offset:8\n"
    "v_mfma_f32_16x16x32_bf16 v[104:107], a[224:227], v[204:207], v[104:107]\n"
    "ds_read_b64_tr_b16 a[222:223], v250 offset:1032\n"
    "v_mfma_f32_16x16x32_bf16 v[136:139], a[224:227], v[208:211], v[136:139]\n"
    "v_mfma_f32_16x16x32_bf16 v[168:171], a[224:227], v[212:215], v[168:171]\n"
    "ds_read_b128 v[216:219], v249 offset:49152\n"
    "v_mfma_f32_16x16x32_bf16 v[12:15], a[228:231], v[192:195], v[12:15]\n"
    "ds_read_b128 v[220:223], v249 offset:51200\n"
    "v_mfma_f32_16x16x32_bf16 v[44:47], a[228:231], v[196:199], v[44:47]\n"
    "ds_read_b128 v[224:227], v249 offset:53248\n"
    "v_mfma_f32_16x16x32_bf16 v[76:79], a[228:231], v[200:203], v[76:79]\n"
    "v_mfma_f32_16x16x32_bf16 v[108:111], a[228:231], v[204:207], v[108:111]\n"
    "ds_read_b128 v[228:231], v249 offset:55296\n"
    "v_mfma_f32_16x16x32_bf16 v[140:143], a[228:231], v[208:211], v[140:143]\n"
    "ds_read_b128 v[232:235], v249 offset:57344\n"
    "v_mfma_f32_16x16x32_bf16 v[172:175], a[228:231], v[212:215], v[172:175]\n"
    "ds_read_b128 v[236:239], v249 offset:59392\n"
    "v_mfma_f32_16x16x32_bf16 v[16:19], a[232:235], v[192:195], v[16:19]\n"
    "buffer_load_dwordx4 v247, s[52:55], s69 offen offset:1024 lds\n"
    "v_mfma_f32_16x16x32_bf16 v[48:51], a[232:235], v[196:199], v[48:51]\n"
    "ds_read_b64_tr_b16 a[224:225], v251 offset:0\n"
    "v_mfma_f32_16x16x32_bf16 v[80:83], a[232:235], v[200:203], v[80:83]\n"
    "ds_read_b64_tr_b16 a[226:227], v251 offset:1024\n"
    "v_mfma_f32_16x16x32_bf16 v[112:115], a[232:235], v[204:207], v[112:115]\n"
    "ds_read_b64_tr_b16 a[228:229], v251 offset:8\n"
    "v_mfma_f32_16x16x32_bf16 v[144:147], a[232:235], v[208:211], v[144:147]\n"
    "v_mfma_f32_16x16x32_bf16 v[176:179], a[232:235], v[212:215], v[176:179]\n"
    "ds_read_b64_tr_b16 a[230:231], v251 offset:1032\n"
    "v_mfma_f32_16x16x32_bf16 v[20:23], a[236:239], v[192:195], v[20:23]\n"
    "ds_read_b64_tr_b16 a[232:233], v252 offset:0\n"
    "v_mfma_f32_16x16x32_bf16 v[52:55], a[236:239], v[196:199], v[52:55]\n"
    "ds_read_b64_tr_b16 a[234:235], v252 offset:1024\n"
    "v_mfma_f32_16x16x32_bf16 v[84:87], a[236:239], v[200:203], v[84:87]\n"
    "v_mfma_f32_16x16x32_bf16 v[116:119], a[236:239], v[204:207], v[116:119]\n"
    "v_mfma_f32_16x16x32_bf16 v[148:151], a[236:239], v[208:211], v[148:151]\n"
    "v_mfma_f32_16x16x32_bf16 v[180:183], a[236:239], v[212:215], v[180:183]\n"
    "v_mfma_f32_16x16x32_bf16 v[24:27], a[240:243], v[192:195], v[24:27]\n"
    "buffer_load_dwordx4 v246, s[52:55], s69 offen offset:2048 lds\n"
    "v_mfma_f32_16x16x32_bf16 v[56:59], a[240:243], v[196:199], v[56:59]\n"
    "ds_read_b64_tr_b16 a[236:237], v252 offset:8\n"
    "v_mfma_f32_16x16x32_bf16 v[88:91], a[240:243], v[200:203], v[88:91]\n"
    "ds_read_b64_tr_b16 a[238:239], v252 offset:1032\n"
    "v_mfma_f32_16x16x32_bf16 v[120:123], a[240:243], v[204:207], v[120:123]\n"
    "v_mfma_f32_16x16x32_bf16 v[152:155], a[240:243], v[208:211], v[152:155]\n"
    "v_mfma_f32_16x16x32_bf16 v[184:187], a[240:243], v[212:215], v[184:187]\n"
    "v_mfma_f32_16x16x32_bf16 v[28:31], a[244:247], v[192:195], v[28:31]\n"
    "ds_read_b64_tr_b16 a[240:241], v253 offset:0\n"
    "v_mfma_f32_16x16x32_bf16 v[60:63], a[244:247], v[196:199], v[60:63]\n"
    "ds_read_b64_tr_b16 a[242:243], v253 offset:1024\n"
    "v_mfma_f32_16x16x32_bf16 v[92:95], a[244:247], v[200:203], v[92:95]\n"
    "v_mfma_f32_16x16x32_bf16 v[124:127], a[244:247], v[204:207], v[124:127]\n"
    "buffer_load_dwordx4 v247, s[52:55], s69 offen offset:3072 lds\n"
    "v_mfma_f32_16x16x32_bf16 v[156:159], a[244:247], v[208:211], v[156:159]\n"
    "v_mfma_f32_16x16x32_bf16 v[188:191], a[244:247], v[212:215], v[188:191]\n"
    "s_waitcnt lgkmcnt(8)\n"
    "v_mfma_f32_16x16x32_bf16 v[0:3], a[216:219], v[216:219], v[0:3]\n"
    "ds_read_b64_tr_b16 a[244:245], v253 offset:8\n"
    "v_mfma_f32_16x16x32_bf16 v[32:35], a[216:219], v[220:223], v[32:35]\n"
    "ds_read_b64_tr_b16 a[246:247], v253 offset:1032\n"
    "v_mfma_f32_16x16x32_bf16 v[64:67], a[216:219], v[224:227], v[64:67]\n"
    "v_mfma_f32_16x16x32_bf16 v[96:99], a[216:219], v[228:231], v[96:99]\n"
    "v_mfma_f32_16x16x32_bf16 v[128:131], a[216:219], v[232:235], v[128:131]\n"
    "v_mfma_f32_16x16x32_bf16 v[160:163], a[216:219], v[236:239], v[160:163]\n"
    "v_mfma_f32_16x16x32_bf16 v[4:7], a[220:223], v[216:219], v[4:7]\n"
    "v_mfma_f32_16x16x32_bf16 v[36:39], a[220:223], v[220:223], v[36:39]\n"
    "v_mfma_f32_16x16x32_bf16 v[68:71], a[220:223], v[224:227], v[68:71]\n"
    "v_mfma_f32_16x16x32_bf16 v[100:103], a[220:223], v[228:231], v[100:103]\n"
    "s_add_u32 s68, s77, 0x4000\n"
    "s_add_u32 m0, s67, 0x8000\n"
    "v_mfma_f32_16x16x32_bf16 v[132:135], a[220:223], v[232:235], v[132:135]\n"
    "s_waitcnt vmcnt(12) lgkmcnt(0)\n"
    "s_barrier\n"
    "v_mfma_f32_16x16x32_bf16 v[164:167], a[220:223], v[236:239], v[164:167]\n"
    "s_and_b32 s68, s68, 0x3ffff\n"
    "v_mfma_f32_16x16x32_bf16 v[8:11], a[224:227], v[216:219], v[8:11]\n"
    "buffer_load_dwordx4 v244, s[48:51], s68 offen nt lds\n"
    "v_mfma_f32_16x16x32_bf16 v[40:43], a[224:227], v[220:223], v[40:43]\n"
    "v_mfma_f32_16x16x32_bf16 v[72:75], a[224:227], v[224:227], v[72:75]\n"
    "v_mfma_f32_16x16x32_bf16 v[104:107], a[224:227], v[228:231], v[104:107]\n"
    "v_mfma_f32_16x16x32_bf16 v[136:139], a[224:227], v[232:235], v[136:139]\n"
    "v_mfma_f32_16x16x32_bf16 v[168:171], a[224:227], v[236:239], v[168:171]\n"
    "v_mfma_f32_16x16x32_bf16 v[12:15], a[228:231], v[216:219], v[12:15]\n"
    "v_mfma_f32_16x16x32_bf16 v[44:47], a[228:231], v[220:223], v[44:47]\n"
    "v_mfma_f32_16x16x32_bf16 v[76:79], a[228:231], v[224:227], v[76:79]\n"
    "v_mfma_f32_16x16x32_bf16 v[108:111], a[228:231], v[228:231], v[108:111]\n"
    "v_mfma_f32_16x16x32_bf16 v[140:143], a[228:231], v[232:235], v[140:143]\n"
    "v_mfma_f32_16x16x32_bf16 v[172:175], a[228:231], v[236:239], v[172:175]\n"
    "v_mfma_f32_16x16x32_bf16 v[16:19], a[232:235], v[216:219], v[16:19]\n"
    "buffer_load_dwordx4 v245, s[48:51], s68 offen offset:1024 nt lds\n"
    "v_mfma_f32_16x16x32_bf16 v[48:51], a[232:235], v[220:223], v[48:51]\n"
    "ds_read_b128 a[216:219], v240 offset:0\n"
    "v_mfma_f32_16x16x32_bf16 v[80:83], a[232:235], v[224:227], v[80:83]\n"
    "v_mfma_f32_16x16x32_bf16 v[112:115], a[232:235], v[228:231], v[112:115]\n"
    "v_mfma_f32_16x16x32_bf16 v[144:147], a[232:235], v[232:235], v[144:147]\n"
    "v_mfma_f32_16x16x32_bf16 v[176:179], a[232:235], v[236:239], v[176:179]\n"
    "v_mfma_f32_16x16x32_bf16 v[20:23], a[236:239], v[216:219], v[20:23]\n"
    "v_mfma_f32_16x16x32_bf16 v[52:55], a[236:239], v[220:223], v[52:55]\n"
    "ds_read_b128 a[220:223], v241 offset:0\n"
    "v_mfma_f32_16x16x32_bf16 v[84:87], a[236:239], v[224:227], v[84:87]\n"
    "v_mfma_f32_16x16x32_bf16 v[116:119], a[236:239], v[228:231], v[116:119]\n"
    "v_mfma_f32_16x16x32_bf16 v[148:151], a[236:239], v[232:235], v[148:151]\n"
    "v_mfma_f32_16x16x32_bf16 v[180:183], a[236:239], v[236:239], v[180:183]\n"
    "v_mfma_f32_16x16x32_bf16 v[24:27], a[240:243], v[216:219], v[24:27]\n"
    "buffer_load_dwordx4 v244, s[48:51], s68 offen offset:2048 nt lds\n"
    "v_mfma_f32_16x16x32_bf16 v[56:59], a[240:243], v[220:223], v[56:59]\n"
    "ds_read_b128 a[224:227], v240 offset:1024\n"
    "v_mfma_f32_16x16x32_bf16 v[88:91], a[240:243], v[224:227], v[88:91]\n"
    "v_mfma_f32_16x16x32_bf16 v[120:123], a[240:243], v[228:231], v[120:123]\n"
    "v_mfma_f32_16x16x32_bf16 v[152:155], a[240:243], v[232:235], v[152:155]\n"
    "v_mfma_f32_16x16x32_bf16 v[184:187], a[240:243], v[236:239], v[184:187]\n"
    "v_mfma_f32_16x16x32_bf16 v[28:31], a[244:247], v[216:219], v[28:31]\n"
    "v_mfma_f32_16x16x32_bf16 v[60:63], a[244:247], v[220:223], v[60:63]\n"
    "ds_read_b128 a[228:231], v241 offset:1024\n"
    "v_mfma_f32_16x16x32_bf16 v[92:95], a[244:247], v[224:227], v[92:95]\n"
    "v_mfma_f32_16x16x32_bf16 v[124:127], a[244:247], v[228:231], v[124:127]\n"
    "buffer_load_dwordx4 v245, s[48:51], s68 offen offset:3072 nt lds\n"
    "v_mfma_f32_16x16x32_bf16 v[156:159], a[244:247], v[232:235], v[156:159]\n"
    "v_mfma_f32_16x16x32_bf16 v[188:191], a[244:247], v[236:239], v[188:191]\n"
    "v_accvgpr_read_b32 v192, a252\n"
    "v_accvgpr_read_b32 v193, a253\n"
    "s_nop 0\n"
    "v_readfirstlane_b32 s85, v192\n"
    "v_readfirstlane_b32 s25, v193\n"
    "v_pk_mul_f32 v[0:1], v[0:1], s[60:61]\n"
    "v_pk_mul_f32 v[2:3], v[2:3], s[60:61]\n"
    "v_pk_mul_f32 v[4:5], v[4:5], s[60:61]\n"
    "v_pk_mul_f32 v[6:7], v[6:7], s[60:61]\n"
    "s_add_u32 s69, s77, 0x4000\n"
    "s_add_u32 m0, s67, 0x1c000\n"
    "s_and_b32 s69, s69, 0x3ffff\n"
    "v_cvt_pk_bf16_f32 v0, v0, v1\n"
    "v_cvt_pk_bf16_f32 v1, v2, v3\n"
    "v_cvt_pk_bf16_f32 v2, v4, v5\n"
    "v_cvt_pk_bf16_f32 v3, v6, v7\n"
    "buffer_load_dwordx4 v246, s[52:55], s69 offen lds\n"
    "v_pk_mul_f32 v[8:9], v[8:9], s[60:61]\n"
    "v_pk_mul_f32 v[10:11], v[10:11], s[60:61]\n"
    "v_pk_mul_f32 v[12:13], v[12:13], s[60:61]\n"
    "v_pk_mul_f32 v[14:15], v[14:15], s[60:61]\n"
    "v_cvt_pk_bf16_f32 v4, v8, v9\n"
    "v_cvt_pk_bf16_f32 v5, v10, v11\n"
    "v_cvt_pk_bf16_f32 v6, v12, v13\n"
    "v_cvt_pk_bf16_f32 v7, v14, v15\n"
    "v_pk_mul_f32 v[16:17], v[16:17], s[60:61]\n"
    "v_pk_mul_f32 v[18:19], v[18:19], s[60:61]\n"
    "v_pk_mul_f32 v[20:21], v[20:21], s[60:61]\n"
    "v_pk_mul_f32 v[22:23], v[22:23], s[60:61]\n"
    "v_cvt_pk_bf16_f32 v8, v16, v17\n"
    "v_cvt_pk_bf16_f32 v9, v18, v19\n"
    "v_cvt_pk_bf16_f32 v10, v20, v21\n"
    "v_cvt_pk_bf16_f32 v11, v22, v23\n"
    "v_pk_mul_f32 v[24:25], v[24:25], s[60:61]\n"
    "v_pk_mul_f32 v[26:27], v[26:27], s[60:61]\n"
    "v_pk_mul_f32 v[28:29], v[28:29], s[60:61]\n"
    "v_pk_mul_f32 v[30:31], v[30:31], s[60:61]\n"
    "v_cvt_pk_bf16_f32 v12, v24, v25\n"
    "v_cvt_pk_bf16_f32 v13, v26, v27\n"
    "v_cvt_pk_bf16_f32 v14, v28, v29\n"
    "v_cvt_pk_bf16_f32 v15, v30, v31\n"
    "v_pk_mul_f32 v[32:33], v[32:33], s[60:61]\n"
    "v_pk_mul_f32 v[34:35], v[34:35], s[60:61]\n"
    "v_pk_mul_f32 v[36:37], v[36:37], s[60:61]\n"
    "v_pk_mul_f32 v[38:39], v[38:39], s[60:61]\n"
    "v_cvt_pk_bf16_f32 v16, v32, v33\n"
    "v_cvt_pk_bf16_f32 v17, v34, v35\n"
    "v_cvt_pk_bf16_f32 v18, v36, v37\n"
    "v_cvt_pk_bf16_f32 v19, v38, v39\n"
    "v_pk_mul_f32 v[40:41], v[40:41], s[60:61]\n"
    "v_pk_mul_f32 v[42:43], v[42:43], s[60:61]\n"
    "v_pk_mul_f32 v[44:45], v[44:45], s[60:61]\n"
    "v_pk_mul_f32 v[46:47], v[46:47], s[60:61]\n"
    "v_cvt_pk_bf16_f32 v20, v40, v41\n"
    "v_cvt_pk_bf16_f32 v21, v42, v43\n"
    "v_cvt_pk_bf16_f32 v22, v44, v45\n"
    "v_cvt_pk_bf16_f32 v23, v46, v47\n"
    "v_pk_mul_f32 v[48:49], v[48:49], s[60:61]\n"
    "v_pk_mul_f32 v[50:51], v[50:51], s[60:61]\n"
    "v_pk_mul_f32 v[52:53], v[52:53], s[60:61]\n"
    "v_pk_mul_f32 v[54:55], v[54:55], s[60:61]\n"
    "v_cvt_pk_bf16_f32 v24, v48, v49\n"
    "v_cvt_pk_bf16_f32 v25, v50, v51\n"
    "v_cvt_pk_bf16_f32 v26, v52, v53\n"
    "v_cvt_pk_bf16_f32 v27, v54, v55\n"
    "v_pk_mul_f32 v[56:57], v[56:57], s[60:61]\n"
    "v_pk_mul_f32 v[58:59], v[58:59], s[60:61]\n"
    "v_pk_mul_f32 v[60:61], v[60:61], s[60:61]\n"
    "v_pk_mul_f32 v[62:63], v[62:63], s[60:61]\n"
    "v_cvt_pk_bf16_f32 v28, v56, v57\n"
    "v_cvt_pk_bf16_f32 v29, v58, v59\n"
    "v_cvt_pk_bf16_f32 v30, v60, v61\n"
    "v_cvt_pk_bf16_f32 v31, v62, v63\n"
    "v_pk_mul_f32 v[64:65], v[64:65], s[60:61]\n"
    "v_pk_mul_f32 v[66:67], v[66:67], s[60:61]\n"
    "v_pk_mul_f32 v[68:69], v[68:69], s[60:61]\n"
    "v_pk_mul_f32 v[70:71], v[70:71], s[60:61]\n"
    "v_cvt_pk_bf16_f32 v32, v64, v65\n"
    "v_cvt_pk_bf16_f32 v33, v66, v67\n"
    "v_cvt_pk_bf16_f32 v34, v68, v69\n"
    "v_cvt_pk_bf16_f32 v35, v70, v71\n"
    "v_pk_mul_f32 v[72:73], v[72:73], s[60:61]\n"
    "v_pk_mul_f32 v[74:75], v[74:75], s[60:61]\n"
    "v_pk_mul_f32 v[76:77], v[76:77], s[60:61]\n"
    "v_pk_mul_f32 v[78:79], v[78:79], s[60:61]\n"
    "v_cvt_pk_bf16_f32 v36, v72, v73\n"
    "v_cvt_pk_bf16_f32 v37, v74, v75\n"
    "v_cvt_pk_bf16_f32 v38, v76, v77\n"
    "v_cvt_pk_bf16_f32 v39, v78, v79\n"
    "v_pk_mul_f32 v[80:81], v[80:81], s[60:61]\n"
    "v_pk_mul_f32 v[82:83], v[82:83], s[60:61]\n"
    "v_pk_mul_f32 v[84:85], v[84:85], s[60:61]\n"
    "v_pk_mul_f32 v[86:87], v[86:87], s[60:61]\n"
    "v_cvt_pk_bf16_f32 v40, v80, v81\n"
    "v_cvt_pk_bf16_f32 v41, v82, v83\n"
    "v_cvt_pk_bf16_f32 v42, v84, v85\n"
    "v_cvt_pk_bf16_f32 v43, v86, v87\n"
    "v_pk_mul_f32 v[88:89], v[88:89], s[60:61]\n"
    "v_pk_mul_f32 v[90:91], v[90:91], s[60:61]\n"
    "v_pk_mul_f32 v[92:93], v[92:93], s[60:61]\n"
    "v_pk_mul_f32 v[94:95], v[94:95], s[60:61]\n"
    "v_cvt_pk_bf16_f32 v44, v88, v89\n"
    "v_cvt_pk_bf16_f32 v45, v90, v91\n"
    "v_cvt_pk_bf16_f32 v46, v92, v93\n"
    "v_cvt_pk_bf16_f32 v47, v94, v95\n"
    "v_pk_mul_f32 v[96:97], v[96:97], s[60:61]\n"
    "v_pk_mul_f32 v[98:99], v[98:99], s[60:61]\n"
    "v_pk_mul_f32 v[100:101], v[100:101], s[60:61]\n"
    "v_pk_mul_f32 v[102:103], v[102:103], s[60:61]\n"
    "v_cvt_pk_bf16_f32 v48, v96, v97\n"
    "v_cvt_pk_bf16_f32 v49, v98, v99\n"
    "v_cvt_pk_bf16_f32 v50, v100, v101\n"
    "v_cvt_pk_bf16_f32 v51, v102, v103\n"
    "buffer_load_dwordx4 v247, s[52:55], s69 offen offset:1024 lds\n"
    "v_pk_mul_f32 v[104:105], v[104:105], s[60:61]\n"
    "v_pk_mul_f32 v[106:107], v[106:107], s[60:61]\n"
    "v_pk_mul_f32 v[108:109], v[108:109], s[60:61]\n"
    "v_pk_mul_f32 v[110:111], v[110:111], s[60:61]\n"
    "v_cvt_pk_bf16_f32 v52, v104, v105\n"
    "v_cvt_pk_bf16_f32 v53, v106, v107\n"
    "v_cvt_pk_bf16_f32 v54, v108, v109\n"
    "v_cvt_pk_bf16_f32 v55, v110, v111\n"
    "v_pk_mul_f32 v[112:113], v[112:113], s[60:61]\n"
    "v_pk_mul_f32 v[114:115], v[114:115], s[60:61]\n"
    "v_pk_mul_f32 v[116:117], v[116:117], s[60:61]\n"
    "v_pk_mul_f32 v[118:119], v[118:119], s[60:61]\n"
    "v_cvt_pk_bf16_f32 v56, v112, v113\n"
    "v_cvt_pk_bf16_f32 v57, v114, v115\n"
    "v_cvt_pk_bf16_f32 v58, v116, v117\n"
    "v_cvt_pk_bf16_f32 v59, v118, v119\n"
    "v_pk_mul_f32 v[120:121], v[120:121], s[60:61]\n"
    "v_pk_mul_f32 v[122:123], v[122:123], s[60:61]\n"
    "v_pk_mul_f32 v[124:125], v[124:125], s[60:61]\n"
    "v_pk_mul_f32 v[126:127], v[126:127], s[60:61]\n"
    "v_cvt_pk_bf16_f32 v60, v120, v121\n"
    "v_cvt_pk_bf16_f32 v61, v122, v123\n"
    "v_cvt_pk_bf16_f32 v62, v124, v125\n"
    "v_cvt_pk_bf16_f32 v63, v126, v127\n"
    "v_pk_mul_f32 v[128:129], v[128:129], s[60:61]\n"
    "v_pk_mul_f32 v[130:131], v[130:131], s[60:61]\n"
    "v_pk_mul_f32 v[132:133], v[132:133], s[60:61]\n"
    "v_pk_mul_f32 v[134:135], v[134:135], s[60:61]\n"
    "v_cvt_pk_bf16_f32 v64, v128, v129\n"
    "v_cvt_pk_bf16_f32 v65, v130, v131\n"
    "v_cvt_pk_bf16_f32 v66, v132, v133\n"
    "v_cvt_pk_bf16_f32 v67, v134, v135\n"
    "v_pk_mul_f32 v[136:137], v[136:137], s[60:61]\n"
    "v_pk_mul_f32 v[138:139], v[138:139], s[60:61]\n"
    "v_pk_mul_f32 v[140:141], v[140:141], s[60:61]\n"
    "v_pk_mul_f32 v[142:143], v[142:143], s[60:61]\n"
    "v_cvt_pk_bf16_f32 v68, v136, v137\n"
    "v_cvt_pk_bf16_f32 v69, v138, v139\n"
    "v_cvt_pk_bf16_f32 v70, v140, v141\n"
    "v_cvt_pk_bf16_f32 v71, v142, v143\n"
    "v_pk_mul_f32 v[144:145], v[144:145], s[60:61]\n"
    "v_pk_mul_f32 v[146:147], v[146:147], s[60:61]\n"
    "v_pk_mul_f32 v[148:149], v[148:149], s[60:61]\n"
    "v_pk_mul_f32 v[150:151], v[150:151], s[60:61]\n"
    "v_cvt_pk_bf16_f32 v72, v144, v145\n"
    "v_cvt_pk_bf16_f32 v73, v146, v147\n"
    "v_cvt_pk_bf16_f32 v74, v148, v149\n"
    "v_cvt_pk_bf16_f32 v75, v150, v151\n"
    "v_pk_mul_f32 v[152:153], v[152:153], s[60:61]\n"
    "v_pk_mul_f32 v[154:155], v[154:155], s[60:61]\n"
    "v_pk_mul_f32 v[156:157], v[156:157], s[60:61]\n"
    "v_pk_mul_f32 v[158:159], v[158:159], s[60:61]\n"
    "v_cvt_pk_bf16_f32 v76, v152, v153\n"
    "v_cvt_pk_bf16_f32 v77, v154, v155\n"
    "v_cvt_pk_bf16_f32 v78, v156, v157\n"
    "v_cvt_pk_bf16_f32 v79, v158, v159\n"
    "v_pk_mul_f32 v[160:161], v[160:161], s[60:61]\n"
    "v_pk_mul_f32 v[162:163], v[162:163], s[60:61]\n"
    "v_pk_mul_f32 v[164:165], v[164:165], s[60:61]\n"
    "v_pk_mul_f32 v[166:167], v[166:167], s[60:61]\n"
    "v_cvt_pk_bf16_f32 v80, v160, v161\n"
    "v_cvt_pk_bf16_f32 v81, v162, v163\n"
    "v_cvt_pk_bf16_f32 v82, v164, v165\n"
    "v_cvt_pk_bf16_f32 v83, v166, v167\n"
    "v_pk_mul_f32 v[168:169], v[168:169], s[60:61]\n"
    "v_pk_mul_f32 v[170:171], v[170:171], s[60:61]\n"
    "v_pk_mul_f32 v[172:173], v[172:173], s[60:61]\n"
    "v_pk_mul_f32 v[174:175], v[174:175], s[60:61]\n"
    "v_cvt_pk_bf16_f32 v84, v168, v169\n"
    "v_cvt_pk_bf16_f32 v85, v170, v171\n"
    "v_cvt_pk_bf16_f32 v86, v172, v173\n"
    "v_cvt_pk_bf16_f32 v87, v174, v175\n"
    "v_pk_mul_f32 v[176:177], v[176:177], s[60:61]\n"
    "v_pk_mul_f32 v[178:179], v[178:179], s[60:61]\n"
    "v_pk_mul_f32 v[180:181], v[180:181], s[60:61]\n"
    "v_pk_mul_f32 v[182:183], v[182:183], s[60:61]\n"
    "v_cvt_pk_bf16_f32 v88, v176, v177\n"
    "v_cvt_pk_bf16_f32 v89, v178, v179\n"
    "v_cvt_pk_bf16_f32 v90, v180, v181\n"
    "v_cvt_pk_bf16_f32 v91, v182, v183\n"
    "v_pk_mul_f32 v[184:185], v[184:185], s[60:61]\n"
    "v_pk_mul_f32 v[186:187], v[186:187], s[60:61]\n"
    "v_pk_mul_f32 v[188:189], v[188:189], s[60:61]\n"
    "v_pk_mul_f32 v[190:191], v[190:191], s[60:61]\n"
    "v_cvt_pk_bf16_f32 v92, v184, v185\n"
    "v_cvt_pk_bf16_f32 v93, v186, v187\n"
    "v_cvt_pk_bf16_f32 v94, v188, v189\n"
    "v_cvt_pk_bf16_f32 v95, v190, v191\n"
    "s_waitcnt lgkmcnt(3)\n"
    "v_mfma_f32_16x16x32_bf16 v[96:99], a[216:219], v[0:3], 0\n"
    "v_mbcnt_lo_u32_b32 v234, -1, 0\n"
    "v_mbcnt_hi_u32_b32 v234, -1, v234\n"
    "v_mfma_f32_16x16x32_bf16 v[100:103], a[216:219], v[16:19], 0\n"
    "v_add_u32 v235, 0, v234\n"
    "v_mul_u32_u24 v236, 0xccd, v235\n"
    "v_mfma_f32_16x16x32_bf16 v[104:107], a[216:219], v[32:35], 0\n"
    "v_lshrrev_b32 v236, 16, v236\n"
    "v_mfma_f32_16x16x32_bf16 v[108:111], a[216:219], v[48:51], 0\n"
    "buffer_load_dwordx4 v246, s[52:55], s69 offen offset:2048 lds\n"
    "v_mul_u32_u24 v237, 20, v236\n"
    "v_mfma_f32_16x16x32_bf16 v[112:115], a[216:219], v[64:67], 0\n"
    "v_sub_u32 v235, v235, v237\n"
    "v_mul_u32_u24 v228, 0x500, v236\n"
    "v_mfma_f32_16x16x32_bf16 v[116:119], a[216:219], v[80:83], 0\n"
    "v_bfe_u32 v236, v236, 2, 1\n"
    "v_lshrrev_b32 v237, 1, v235\n"
    "s_waitcnt lgkmcnt(2)\n"
    "v_mfma_f32_16x16x32_bf16 v[96:99], a[220:223], v[4:7], v[96:99]\n"
    "v_xor_b32 v237, v237, v236\n"
    "v_add_u32 v236, s26, v237\n"
    "v_mfma_f32_16x16x32_bf16 v[100:103], a[220:223], v[20:23], v[100:103]\n"
    "ds_read_b128 a[216:219], v240 offset:4096\n"
    "v_and_b32 v236, 7, v236\n"
    "v_mfma_f32_16x16x32_bf16 v[104:107], a[220:223], v[36:39], v[104:107]\n"
    "v_cmp_gt_u32 vcc, 8, v237\n"
    "v_and_b32 v235, 1, v235\n"
    "s_nop 0\n"
    "v_cndmask_b32 v237, v237, v236, vcc\n"
    "v_mfma_f32_16x16x32_bf16 v[108:111], a[220:223], v[52:55], v[108:111]\n"
    "v_lshl_add_u32 v228, v237, 5, v228\n"
    "v_mfma_f32_16x16x32_bf16 v[112:115], a[220:223], v[68:71], v[112:115]\n"
    "buffer_load_dwordx4 v247, s[52:55], s69 offen offset:3072 lds\n"
    "v_lshl_add_u32 v228, v235, 4, v228\n"
    "v_mfma_f32_16x16x32_bf16 v[116:119], a[220:223], v[84:87], v[116:119]\n"
    "v_add_u32 v228, 0x1000, v228\n"
    "v_add_u32 v235, 64, v234\n"
    "s_waitcnt lgkmcnt(2)\n"
    "v_mfma_f32_16x16x32_bf16 v[96:99], a[224:227], v[8:11], v[96:99]\n"
    "v_mul_u32_u24 v236, 0xccd, v235\n"
    "v_lshrrev_b32 v236, 16, v236\n"
    "v_mfma_f32_16x16x32_bf16 v[100:103], a[224:227], v[24:27], v[100:103]\n"
    "ds_read_b128 a[220:223], v241 offset:4096\n"
    "v_mul_u32_u24 v237, 20, v236\n"
    "v_mfma_f32_16x16x32_bf16 v[104:107], a[224:227], v[40:43], v[104:107]\n"
    "v_sub_u32 v235, v235, v237\n"
    "v_mul_u32_u24 v229, 0x500, v236\n"
    "v_mfma_f32_16x16x32_bf16 v[108:111], a[224:227], v[56:59], v[108:111]\n"
    "s_add_u32 s68, s77, 0x6000\n"
    "s_add_u32 m0, s67, 0xc000\n"
    "s_and_b32 s68, s68, 0x3ffff\n"
    "v_bfe_u32 v236, v236, 2, 1\n"
    "v_mfma_f32_16x16x32_bf16 v[112:115], a[224:227], v[72:75], v[112:115]\n"
    "buffer_load_dwordx4 v244, s[48:51], s68 offen nt lds\n"
    "v_lshrrev_b32 v237, 1, v235\n"
    "v_mfma_f32_16x16x32_bf16 v[116:119], a[224:227], v[88:91], v[116:119]\n"
    "v_xor_b32 v237, v237, v236\n"
    "v_add_u32 v236, s26, v237\n"
    "s_waitcnt lgkmcnt(2)\n"
    "v_mfma_f32_16x16x32_bf16 v[96:99], a[228:231], v[12:15], v[96:99]\n"
    "v_and_b32 v236, 7, v236\n"
    "v_cmp_gt_u32 vcc, 8, v237\n"
    "v_and_b32 v235, 1, v235\n"
    "s_nop 0\n"
    "v_cndmask_b32 v237, v237, v236, vcc\n"
    "v_mfma_f32_16x16x32_bf16 v[100:103], a[228:231], v[28:31], v[100:103]\n"
    "ds_read_b128 a[224:227], v240 offset:5120\n"
    "v_lshl_add_u32 v229, v237, 5, v229\n"
    "v_mfma_f32_16x16x32_bf16 v[104:107], a[228:231], v[44:47], v[104:107]\n"
    "v_lshl_add_u32 v229, v235, 4, v229\n"
    "v_add_u32 v229, 0xc00, v229\n"
    "v_mfma_f32_16x16x32_bf16 v[108:111], a[228:231], v[60:63], v[108:111]\n"
    "v_add_u32 v235, 0x80, v234\n"
    "v_mfma_f32_16x16x32_bf16 v[112:115], a[228:231], v[76:79], v[112:115]\n"
    "buffer_load_dwordx4 v245, s[48:51], s68 offen offset:1024 nt lds\n"
    "v_mul_u32_u24 v236, 0xccd, v235\n"
    "v_mfma_f32_16x16x32_bf16 v[116:119], a[228:231], v[92:95], v[116:119]\n"
    "ds_read_b128 a[228:231], v241 offset:5120\n"
    "v_lshrrev_b32 v236, 16, v236\n"
    "s_mov_b32 s72, 0x4000\n"
    "s_add_u32 s70, s67, 0x10000\n"
    "s_mov_b32 s63, 27\n"
    "s_add_u32 s64, s67, 0xc000\n"
    "s_mov_b32 s84, s75\n"
    "v_max3_f32 v216, v96, v97, v98\n"
    "v_max3_f32 v217, v100, v101, v102\n"
    "v_max3_f32 v218, v104, v105, v106\n"
    "v_max3_f32 v219, v108, v109, v110\n"
    "v_max3_f32 v220, v112, v113, v114\n"
    "v_max3_f32 v221, v116, v117, v118\n"
    "v_max_f32 v216, v216, v99\n"
    "v_max_f32 v217, v217, v103\n"
    "v_max_f32 v218, v218, v107\n"
    "v_max_f32 v219, v219, v111\n"
    "v_max_f32 v220, v220, v115\n"
    "v_max_f32 v221, v221, v119\n"
    "v_mov_b32 v222, v216\n"
    "v_mov_b32 v223, v217\n"
    "v_mov_b32 v224, v218\n"
    "v_mov_b32 v225, v219\n"
    "v_mov_b32 v226, v220\n"
    "v_mov_b32 v227, v221\n"
    "s_nop 0\n"
    "v_permlane16_swap_b32 v216, v222\n"
    "v_permlane16_swap_b32 v217, v223\n"
    "v_permlane16_swap_b32 v218, v224\n"
    "v_permlane16_swap_b32 v219, v225\n"
    "v_permlane16_swap_b32 v220, v226\n"
    "v_permlane16_swap_b32 v221, v227\n"
    "v_max_f32 v216, v216, v222\n"
    "v_max_f32 v217, v217, v223\n"
    "v_max_f32 v218, v218, v224\n"
    "v_max_f32 v219, v219, v225\n"
    "v_max_f32 v220, v220, v226\n"
    "v_max_f32 v221, v221, v227\n"
    "v_mov_b32 v222, v216\n"
    "v_mov_b32 v223, v217\n"
    "v_mov_b32 v224, v218\n"
    "v_mov_b32 v225, v219\n"
    "v_mov_b32 v226, v220\n"
    "v_mov_b32 v227, v221\n"
    "s_nop 0\n"
    "v_permlane32_swap_b32 v216, v222\n"
    "v_permlane32_swap_b32 v217, v223\n"
    "v_permlane32_swap_b32 v218, v224\n"
    "v_permlane32_swap_b32 v219, v225\n"
    "v_permlane32_swap_b32 v220, v226\n"
    "v_permlane32_swap_b32 v221, v227\n"
    "v_max_f32 v216, v216, v222\n"
    "v_max_f32 v217, v217, v223\n"
    "v_max_f32 v218, v218, v224\n"
    "v_max_f32 v219, v219, v225\n"
    "v_max_f32 v220, v220, v226\n"
    "v_max_f32 v221, v221, v227\n"
    "v_sub_f32 v192, 0, v216\n"
    "v_sub_f32 v193, 0, v216\n"
    "v_sub_f32 v194, 0, v216\n"
    "v_sub_f32 v195, 0, v216\n"
    "v_sub_f32 v196, 0, v217\n"
    "v_sub_f32 v197, 0, v217\n"
    "v_sub_f32 v198, 0, v217\n"
    "v_sub_f32 v199, 0, v217\n"
    "v_sub_f32 v200, 0, v218\n"
    "v_sub_f32 v201, 0, v218\n"
    "v_sub_f32 v202, 0, v218\n"
    "v_sub_f32 v203, 0, v218\n"
    "v_sub_f32 v204, 0, v219\n"
    "v_sub_f32 v205, 0, v219\n"
    "v_sub_f32 v206, 0, v219\n"
    "v_sub_f32 v207, 0, v219\n"
    "v_sub_f32 v208, 0, v220\n"
    "v_sub_f32 v209, 0, v220\n"
    "v_sub_f32 v210, 0, v220\n"
    "v_sub_f32 v211, 0, v220\n"
    "v_sub_f32 v212, 0, v221\n"
    "v_sub_f32 v213, 0, v221\n"
    "v_sub_f32 v214, 0, v221\n"
    "v_sub_f32 v215, 0, v221\n"
    "v_add_u32 v240, s72, v240\n"
    "v_add_u32 v241, s72, v241\n"
    "s_waitcnt vmcnt(14) lgkmcnt(0)\n"
    "s_barrier\n"
    "v_mfma_f32_16x16x32_bf16 v[120:123], a[216:219], v[0:3], v[192:195]\n"
    "v_sub_f32 v96, v96, v216\n"
    "v_sub_f32 v97, v97, v216\n"
    "v_mfma_f32_16x16x32_bf16 v[124:127], a[216:219], v[16:19], v[196:199]\n"
    "ds_read_b64_tr_b16 a[232:233], v242 offset:0\n"
    "v_mfma_f32_16x16x32_bf16 v[128:131], a[216:219], v[32:35], v[200:203]\n"
    "v_sub_f32 v98, v98, v216\n"
    "v_sub_f32 v99, v99, v216\n"
    "v_mfma_f32_16x16x32_bf16 v[132:135], a[216:219], v[48:51], v[204:207]\n"
    "ds_read_b64_tr_b16 a[234:235], v242 offset:4096\n"
    "v_mfma_f32_16x16x32_bf16 v[136:139], a[216:219], v[64:67], v[208:211]\n"
    "v_sub_f32 v100, v100, v217\n"
    "v_sub_f32 v101, v101, v217\n"
    "v_mfma_f32_16x16x32_bf16 v[140:143], a[216:219], v[80:83], v[212:215]\n"
    "ds_read_b128 a[216:219], v240 offset:0\n"
    "v_mfma_f32_16x16x32_bf16 v[120:123], a[220:223], v[4:7], v[120:123]\n"
    "v_sub_f32 v102, v102, v217\n"
    "v_sub_f32 v103, v103, v217\n"
    "v_mfma_f32_16x16x32_bf16 v[124:127], a[220:223], v[20:23], v[124:127]\n"
    "ds_read_b64_tr_b16 a[236:237], v243 offset:0\n"
    "v_mfma_f32_16x16x32_bf16 v[128:131], a[220:223], v[36:39], v[128:131]\n"
    "v_sub_f32 v104, v104, v218\n"
    "v_sub_f32 v105, v105, v218\n"
    "v_mfma_f32_16x16x32_bf16 v[132:135], a[220:223], v[52:55], v[132:135]\n"
    "ds_read_b64_tr_b16 a[238:239], v243 offset:4096\n"
    "v_mfma_f32_16x16x32_bf16 v[136:139], a[220:223], v[68:71], v[136:139]\n"
    "v_sub_f32 v106, v106, v218\n"
    "v_sub_f32 v107, v107, v218\n"
    "v_mfma_f32_16x16x32_bf16 v[140:143], a[220:223], v[84:87], v[140:143]\n"
    "ds_read_b128 a[220:223], v241 offset:0\n"
    "v_mfma_f32_16x16x32_bf16 v[120:123], a[224:227], v[8:11], v[120:123]\n"
    "v_sub_f32 v108, v108, v219\n"
    "v_sub_f32 v109, v109, v219\n"
    "v_mfma_f32_16x16x32_bf16 v[124:127], a[224:227], v[24:27], v[124:127]\n"
    "ds_read_b64_tr_b16 a[240:241], v242 offset:512\n"
    "v_mfma_f32_16x16x32_bf16 v[128:131], a[224:227], v[40:43], v[128:131]\n"
    "v_sub_f32 v110, v110, v219\n"
    "v_sub_f32 v111, v111, v219\n"
    "v_mfma_f32_16x16x32_bf16 v[132:135], a[224:227], v[56:59], v[132:135]\n"
    "ds_read_b64_tr_b16 a[242:243], v242 offset:4608\n"
    "v_mfma_f32_16x16x32_bf16 v[136:139], a[224:227], v[72:75], v[136:139]\n"
    "v_sub_f32 v112, v112, v220\n"
    "v_sub_f32 v113, v113, v220\n"
    "v_mfma_f32_16x16x32_bf16 v[140:143], a[224:227], v[88:91], v[140:143]\n"
    "ds_read_b128 a[224:227], v240 offset:1024\n"
    "v_mfma_f32_16x16x32_bf16 v[120:123], a[228:231], v[12:15], v[120:123]\n"
    "v_sub_f32 v114, v114, v220\n"
    "v_sub_f32 v115, v115, v220\n"
    "v_mfma_f32_16x16x32_bf16 v[124:127], a[228:231], v[28:31], v[124:127]\n"
    "ds_read_b64_tr_b16 a[244:245], v243 offset:512\n"
    "v_mfma_f32_16x16x32_bf16 v[128:131], a[228:231], v[44:47], v[128:131]\n"
    "v_sub_f32 v116, v116, v221\n"
    "v_sub_f32 v117, v117, v221\n"
    "v_mfma_f32_16x16x32_bf16 v[132:135], a[228:231], v[60:63], v[132:135]\n"
    "ds_read_b64_tr_b16 a[246:247], v243 offset:4608\n"
    "v_mfma_f32_16x16x32_bf16 v[136:139], a[228:231], v[76:79], v[136:139]\n"
    "v_sub_f32 v118, v118, v221\n"
    "v_sub_f32 v119, v119, v221\n"
    "v_mfma_f32_16x16x32_bf16 v[140:143], a[228:231], v[92:95], v[140:143]\n"
    "ds_read_b128 a[228:231], v241 offset:1024\n"
    "v_mul_u32_u24 v237, 20, v236\n"
    "v_sub_u32 v235, v235, v237\n"
    "v_mul_u32_u24 v230, 0x500, v236\n"
    "v_bfe_u32 v236, v236, 2, 1\n"
    "v_lshrrev_b32 v237, 1, v235\n"
    "v_xor_b32 v237, v237, v236\n"
    "v_add_u32 v236, s26, v237\n"
    "v_and_b32 v236, 7, v236\n"
    "v_cmp_gt_u32 vcc, 8, v237\n"
    "v_and_b32 v235, 1, v235\n"
    "s_nop 0\n"
    "v_cndmask_b32 v237, v237, v236, vcc\n"
    "v_lshl_add_u32 v230, v237, 5, v230\n"
    "v_lshl_add_u32 v230, v235, 4, v230\n"
    "v_add_u32 v230, 0x800, v230\n"
    "v_add_u32 v235, 0xc0, v234\n"
    "v_mul_u32_u24 v236, 0xccd, v235\n"
    "v_lshrrev_b32 v236, 16, v236\n"
    "v_mul_u32_u24 v237, 20, v236\n"
    "v_sub_u32 v235, v235, v237\n"
    "v_mul_u32_u24 v231, 0x500, v236\n"
    "v_bfe_u32 v236, v236, 2, 1\n"
    "v_lshrrev_b32 v237, 1, v235\n"
    "v_xor_b32 v237, v237, v236\n"
    "v_add_u32 v236, s26, v237\n"
    "v_and_b32 v236, 7, v236\n"
    "v_cmp_gt_u32 vcc, 8, v237\n"
    "v_and_b32 v235, 1, v235\n"
    "s_nop 0\n"
    "v_cndmask_b32 v237, v237, v236, vcc\n"
    "v_lshl_add_u32 v231, v237, 5, v231\n"
    "v_lshl_add_u32 v231, v235, 4, v231\n"
    "v_add_u32 v231, 0x400, v231\n"
    "v_add_u32 v235, 0x100, v234\n"
    "v_mul_u32_u24 v236, 0xccd, v235\n"
    "v_lshrrev_b32 v236, 16, v236\n"
    "v_mul_u32_u24 v237, 20, v236\n"
    "v_sub_u32 v235, v235, v237\n"
    "v_mul_u32_u24 v232, 0x500, v236\n"
    "v_bfe_u32 v236, v236, 2, 1\n"
    "v_lshrrev_b32 v237, 1, v235\n"
    "v_xor_b32 v237, v237, v236\n"
    "v_add_u32 v236, s26, v237\n"
    "v_and_b32 v236, 7, v236\n"
    "v_cmp_gt_u32 vcc, 8, v237\n"
    "v_and_b32 v235, 1, v235\n"
    "s_nop 0\n"
    "v_cndmask_b32 v237, v237, v236, vcc\n"
    "v_lshl_add_u32 v232, v237, 5, v232\n"
    "v_lshl_add_u32 v232, v235, 4, v232\n"
    "v_add_u32 v232, 0x1000, v232\n"
    "v_mul_u32_u24 v233, 0x500, v234\n"
    "v_min_u32 v239, 6, v234\n"
    "v_lshl_add_u32 v239, v239, 7, s6\n"
    "v_lshlrev_b32 v236, 4, v234\n"
    "v_add_u32 v236, s35, v236\n"
    "v_lshrrev_b32 v235, 2, v234\n"
    "v_mul_u32_u24 v235, 0x140, v235\n"
    "v_and_b32 v237, 3, v234\n"
    "v_lshl_add_u32 v235, v237, 3, v235\n"
    "v_and_b32 v237, 16, v234\n"
    "v_lshl_add_u32 v234, v237, 1, v235\n"
    "v_xor_b32 v235, 32, v234\n"
    "v_exp_f32 v96, v96\n"
    "v_exp_f32 v97, v97\n"
    "buffer_load_dwordx4 v244, s[48:51], s68 offen offset:2048 nt lds\n"
    "v_exp_f32 v98, v98\n"
    "v_cvt_pk_bf16_f32 v144, v96, v97\n"
    "v_exp_f32 v99, v99\n"
    "buffer_load_dwordx4 v245, s[48:51], s68 offen offset:3072 nt lds\n"
    "v_exp_f32 v100, v100\n"
    "v_cvt_pk_bf16_f32 v145, v98, v99\n"
    "v_exp_f32 v101, v101\n"
    "v_exp_f32 v102, v102\n"
    "v_cvt_pk_bf16_f32 v148, v100, v101\n"
    "v_exp_f32 v103, v103\n"
    "v_exp_f32 v104, v104\n"
    "v_cvt_pk_bf16_f32 v149, v102, v103\n"
    "v_exp_f32 v105, v105\n"
    "v_exp_f32 v106, v106\n"
    "v_cvt_pk_bf16_f32 v152, v104, v105\n"
    "v_exp_f32 v107, v107\n"
    "v_exp_f32 v108, v108\n"
    "v_cvt_pk_bf16_f32 v153, v106, v107\n"
    "v_exp_f32 v109, v109\n"
    "v_exp_f32 v110, v110\n"
    "v_cvt_pk_bf16_f32 v156, v108, v109\n"
    "v_exp_f32 v111, v111\n"
    "v_exp_f32 v112, v112\n"
    "v_cvt_pk_bf16_f32 v157, v110, v111\n"
    "v_exp_f32 v113, v113\n"
    "v_exp_f32 v114, v114\n"
    "v_cvt_pk_bf16_f32 v160, v112, v113\n"
    "v_exp_f32 v115, v115\n"
    "v_exp_f32 v116, v116\n"
    "v_cvt_pk_bf16_f32 v161, v114, v115\n"
    "v_exp_f32 v117, v117\n"
    "v_exp_f32 v118, v118\n"
    "v_cvt_pk_bf16_f32 v164, v116, v117\n"
    "v_exp_f32 v119, v119\n"
    "v_exp_f32 v120, v120\n"
    "v_cvt_pk_bf16_f32 v165, v118, v119\n"
    "v_exp_f32 v121, v121\n"
    "v_exp_f32 v122, v122\n"
    "v_cvt_pk_bf16_f32 v146, v120, v121\n"
    "v_exp_f32 v123, v123\n"
    "v_exp_f32 v124, v124\n"
    "v_cvt_pk_bf16_f32 v147, v122, v123\n"
    "v_exp_f32 v125, v125\n"
    "v_exp_f32 v126, v126\n"
    "v_cvt_pk_bf16_f32 v150, v124, v125\n"
    "v_exp_f32 v127, v127\n"
    "v_exp_f32 v128, v128\n"
    "v_cvt_pk_bf16_f32 v151, v126, v127\n"
    "v_exp_f32 v129, v129\n"
    "v_exp_f32 v130, v130\n"
    "v_cvt_pk_bf16_f32 v154, v128, v129\n"
    "v_exp_f32 v131, v131\n"
    "v_exp_f32 v132, v132\n"
    "v_cvt_pk_bf16_f32 v155, v130, v131\n"
    "v_exp_f32 v133, v133\n"
    "v_exp_f32 v134, v134\n"
    "v_cvt_pk_bf16_f32 v158, v132, v133\n"
    "s_waitcnt lgkmcnt(0)\n"
    "v_mfma_f32_16x16x32_bf16 v[96:99], a[216:219], v[0:3], v[192:195]\n"
    "s_add_u32 s68, s77, 0x8000\n"
    "v_exp_f32 v135, v135\n"
    "v_mfma_f32_16x16x32_bf16 v[100:103], a[216:219], v[16:19], v[196:199]\n"
    "v_cvt_pk_bf16_f32 v159, v134, v135\n"
    "v_mfma_f32_16x16x32_bf16 v[104:107], a[216:219], v[32:35], v[200:203]\n"
    "s_mov_b32 m0, s70\n"
    "v_exp_f32 v136, v136\n"
    "v_mfma_f32_16x16x32_bf16 v[108:111], a[216:219], v[48:51], v[204:207]\n"
    "s_and_b32 s68, s68, 0x3ffff\n"
    "buffer_load_dwordx4 v244, s[48:51], s68 offen nt lds\n"
    "v_mfma_f32_16x16x32_bf16 v[112:115], a[216:219], v[64:67], v[208:211]\n"
    "v_exp_f32 v137, v137\n"
    "v_mfma_f32_16x16x32_bf16 v[116:119], a[216:219], v[80:83], v[212:215]\n"
    "v_cvt_pk_bf16_f32 v162, v136, v137\n"
    "v_mfma_f32_16x16x32_bf16 v[96:99], a[220:223], v[4:7], v[96:99]\n"
    "v_exp_f32 v138, v138\n"
    "v_mfma_f32_16x16x32_bf16 v[100:103], a[220:223], v[20:23], v[100:103]\n"
    "ds_read_b128 a[216:219], v240 offset:4096\n"
    "v_mfma_f32_16x16x32_bf16 v[104:107], a[220:223], v[36:39], v[104:107]\n"
    "v_exp_f32 v139, v139\n"
    "v_mfma_f32_16x16x32_bf16 v[108:111], a[220:223], v[52:55], v[108:111]\n"
    "buffer_load_dwordx4 v245, s[48:51], s68 offen offset:1024 nt lds\n"
    "v_cvt_pk_bf16_f32 v163, v138, v139\n"
    "v_mfma_f32_16x16x32_bf16 v[112:115], a[220:223], v[68:71], v[112:115]\n"
    "v_exp_f32 v140, v140\n"
    "v_mfma_f32_16x16x32_bf16 v[116:119], a[220:223], v[84:87], v[116:119]\n"
    "buffer_load_dwordx4 v244, s[48:51], s68 offen offset:2048 nt lds\n"
    "v_mfma_f32_16x16x32_bf16 v[96:99], a[224:227], v[8:11], v[96:99]\n"
    "v_exp_f32 v141, v141\n"
    "v_mfma_f32_16x16x32_bf16 v[100:103], a[224:227], v[24:27], v[100:103]\n"
    "ds_read_b128 a[220:223], v241 offset:4096\n"
    "v_cvt_pk_bf16_f32 v166, v140, v141\n"
    "v_mfma_f32_16x16x32_bf16 v[104:107], a[224:227], v[40:43], v[104:107]\n"
    "s_add_u32 s69, s77, 0x6000\n"
    "v_exp_f32 v142, v142\n"
    "v_mfma_f32_16x16x32_bf16 v[108:111], a[224:227], v[56:59], v[108:111]\n"
    "buffer_load_dwordx4 v245, s[48:51], s68 offen offset:3072 nt lds\n"
    "v_mfma_f32_16x16x32_bf16 v[112:115], a[224:227], v[72:75], v[112:115]\n"
    "s_add_u32 m0, s67, 0x20000\n"
    "v_exp_f32 v143, v143\n"
    "v_mfma_f32_16x16x32_bf16 v[116:119], a[224:227], v[88:91], v[116:119]\n"
    "s_and_b32 s69, s69, 0x3ffff\n"
    "buffer_load_dwordx4 v246, s[52:55], s69 offen lds\n"
    "v_cvt_pk_bf16_f32 v167, v142, v143\n"
    "v_mfma_f32_16x16x32_bf16 v[96:99], a[228:231], v[12:15], v[96:99]\n"
    "v_mfma_f32_16x16x32_bf16 v[100:103], a[228:231], v[28:31], v[100:103]\n"
    "ds_read_b128 a[224:227], v240 offset:5120\n"
    "v_mfma_f32_16x16x32_bf16 v[104:107], a[228:231], v[44:47], v[104:107]\n"
    "v_mfma_f32_16x16x32_bf16 v[108:111], a[228:231], v[60:63], v[108:111]\n"
    "buffer_load_dwordx4 v247, s[52:55], s69 offen offset:1024 lds\n"
    "v_mfma_f32_16x16x32_bf16 v[112:115], a[228:231], v[76:79], v[112:115]\n"
    "s_waitcnt lgkmcnt(3)\n"
    "v_mfma_f32_16x16x32_bf16 v[116:119], a[228:231], v[92:95], v[116:119]\n"
    "buffer_load_dwordx4 v246, s[52:55], s69 offen offset:2048 lds\n"
    "v_mfma_f32_16x16x32_bf16 a[0:3], a[232:235], v[144:147], 0\n"
    "v_exp_f32 v96, v96\n"
    "v_mfma_f32_16x16x32_bf16 a[4:7], a[232:235], v[148:151], 0\n"
    "ds_read_b128 a[228:231], v241 offset:5120\n"
    "v_mfma_f32_16x16x32_bf16 a[8:11], a[232:235], v[152:155], 0\n"
    "v_exp_f32 v97, v97\n"
    "v_mfma_f32_16x16x32_bf16 a[12:15], a[232:235], v[156:159], 0\n"
    "v_exp_f32 v98, v98\n"
    "v_mfma_f32_16x16x32_bf16 a[16:19], a[232:235], v[160:163], 0\n"
    "v_exp_f32 v99, v99\n"
    "v_mfma_f32_16x16x32_bf16 a[20:23], a[232:235], v[164:167], 0\n"
    "v_cvt_pk_bf16_f32 v168, v96, v97\n"
    "v_cvt_pk_bf16_f32 v169, v98, v99\n"
    "v_mfma_f32_16x16x32_bf16 a[24:27], a[236:239], v[144:147], 0\n"
    "v_exp_f32 v100, v100\n"
    "v_mfma_f32_16x16x32_bf16 a[28:31], a[236:239], v[148:151], 0\n"
    "ds_read_b64_tr_b16 a[232:233], v242 offset:1024\n"
    "v_mfma_f32_16x16x32_bf16 a[32:35], a[236:239], v[152:155], 0\n"
    "v_exp_f32 v101, v101\n"
    "v_mfma_f32_16x16x32_bf16 a[36:39], a[236:239], v[156:159], 0\n"
    "ds_read_b64_tr_b16 a[234:235], v242 offset:5120\n"
    "v_cvt_pk_bf16_f32 v172, v100, v101\n"
    "v_mfma_f32_16x16x32_bf16 a[40:43], a[236:239], v[160:163], 0\n"
    "v_exp_f32 v102, v102\n"
    "v_mfma_f32_16x16x32_bf16 a[44:47], a[236:239], v[164:167], 0\n"
    "buffer_load_dwordx4 v247, s[52:55], s69 offen offset:3072 lds\n"
    "v_mfma_f32_16x16x32_bf16 a[48:51], a[240:243], v[144:147], 0\n"
    "s_mov_b32 s69, s68\n"
    "v_exp_f32 v103, v103\n"
    "v_mfma_f32_16x16x32_bf16 a[52:55], a[240:243], v[148:151], 0\n"
    "ds_read_b64_tr_b16 a[236:237], v243 offset:1024\n"
    "v_cvt_pk_bf16_f32 v173, v102, v103\n"
    "v_mfma_f32_16x16x32_bf16 a[56:59], a[240:243], v[152:155], 0\n"
    "s_add_u32 s68, s68, 0x2000\n"
    "v_exp_f32 v104, v104\n"
    "v_mfma_f32_16x16x32_bf16 a[60:63], a[240:243], v[156:159], 0\n"
    "ds_read_b64_tr_b16 a[238:239], v243 offset:5120\n"
    "v_mfma_f32_16x16x32_bf16 a[64:67], a[240:243], v[160:163], 0\n"
    "s_add_u32 s71, s70, 0x14000\n"
    "v_exp_f32 v105, v105\n"
    "v_mfma_f32_16x16x32_bf16 a[68:71], a[240:243], v[164:167], 0\n"
    "s_add_u32 s70, s70, s84\n"
    "s_and_b32 s68, s68, 0x3ffff\n"
    "v_cvt_pk_bf16_f32 v176, v104, v105\n"
    "v_mfma_f32_16x16x32_bf16 a[72:75], a[244:247], v[144:147], 0\n"
    "s_mov_b32 s54, s50\n"
    "v_exp_f32 v106, v106\n"
    "v_mfma_f32_16x16x32_bf16 a[76:79], a[244:247], v[148:151], 0\n"
    "ds_read_b64_tr_b16 a[240:241], v242 offset:1536\n"
    "v_mfma_f32_16x16x32_bf16 a[80:83], a[244:247], v[152:155], 0\n"
    "s_cmp_le_i32 s63, 0\n"
    "s_cselect_b32 s50, 0, s50\n"
    "v_exp_f32 v107, v107\n"
    "v_mfma_f32_16x16x32_bf16 a[84:87], a[244:247], v[156:159], 0\n"
    "ds_read_b64_tr_b16 a[242:243], v242 offset:5632\n"
    "v_cvt_pk_bf16_f32 v177, v106, v107\n"
    "v_mfma_f32_16x16x32_bf16 a[88:91], a[244:247], v[160:163], 0\n"
    "s_waitcnt lgkmcnt(6)\n"
    "v_exp_f32 v108, v108\n"
    "v_mfma_f32_16x16x32_bf16 a[92:95], a[244:247], v[164:167], 0\n"
    "s_sub_u32 s63, s63, 1\n"
    "v_mfma_f32_16x16x32_bf16 v[120:123], a[216:219], v[0:3], v[192:195]\n"
    "s_cmp_ge_u32 s70, s76\n"
    "v_exp_f32 v109, v109\n"
    "v_mfma_f32_16x16x32_bf16 v[124:127], a[216:219], v[16:19], v[196:199]\n"
    "ds_read_b64_tr_b16 a[244:245], v243 offset:1536\n"
    "v_cvt_pk_bf16_f32 v180, v108, v109\n"
    "v_mfma_f32_16x16x32_bf16 v[128:131], a[216:219], v[32:35], v[200:203]\n"
    "s_cselect_b32 s84, s75, 0x4000\n"
    "v_exp_f32 v110, v110\n"
    "v_mfma_f32_16x16x32_bf16 v[132:135], a[216:219], v[48:51], v[204:207]\n"
    "ds_read_b64_tr_b16 a[246:247], v243 offset:5632\n"
    "v_mfma_f32_16x16x32_bf16 v[136:139], a[216:219], v[64:67], v[208:211]\n"
    "s_mov_b32 s73, s72\n"
    "v_exp_f32 v111, v111\n"
    "v_mfma_f32_16x16x32_bf16 v[140:143], a[216:219], v[80:83], v[212:215]\n"
    "v_cvt_pk_bf16_f32 v181, v110, v111\n"
    "v_mfma_f32_16x16x32_bf16 v[120:123], a[220:223], v[4:7], v[120:123]\n"
    "s_cmp_eq_u32 s70, s64\n"
    "v_exp_f32 v112, v112\n"
    "v_mfma_f32_16x16x32_bf16 v[124:127], a[220:223], v[20:23], v[124:127]\n"
    "v_add_u32 v242, s73, v242\n"
    "v_add_u32 v243, s73, v243\n"
    "v_mfma_f32_16x16x32_bf16 v[128:131], a[220:223], v[36:39], v[128:131]\n"
    "s_cselect_b32 s72, s75, 0x4000\n"
    "v_exp_f32 v113, v113\n"
    "v_mfma_f32_16x16x32_bf16 v[132:135], a[220:223], v[52:55], v[132:135]\n"
    "v_cvt_pk_bf16_f32 v184, v112, v113\n"
    "v_mfma_f32_16x16x32_bf16 v[136:139], a[220:223], v[68:71], v[136:139]\n"
    "v_exp_f32 v114, v114\n"
    "v_mfma_f32_16x16x32_bf16 v[140:143], a[220:223], v[84:87], v[140:143]\n"
    "v_add_u32 v240, s72, v240\n"
    "v_add_u32 v241, s72, v241\n"
    "v_mfma_f32_16x16x32_bf16 v[120:123], a[224:227], v[8:11], v[120:123]\n"
    "s_waitcnt vmcnt(16) lgkmcnt(0)\n"
    "s_barrier\n"
    "s_mov_b32 m0, s70\n"
    "v_mfma_f32_16x16x32_bf16 v[124:127], a[224:227], v[24:27], v[124:127]\n"
    "buffer_load_dwordx4 v244, s[48:51], s68 offen nt lds\n"
    "v_mfma_f32_16x16x32_bf16 v[128:131], a[224:227], v[40:43], v[128:131]\n"
    "v_exp_f32 v115, v115\n"
    "v_mfma_f32_16x16x32_bf16 v[132:135], a[224:227], v[56:59], v[132:135]\n"
    "ds_read_b128 a[216:219], v240 offset:0\n"
    "v_cvt_pk_bf16_f32 v185, v114, v115\n"
    "v_mfma_f32_16x16x32_bf16 v[136:139], a[224:227], v[72:75], v[136:139]\n"
    "v_exp_f32 v116, v116\n"
    "v_mfma_f32_16x16x32_bf16 v[140:143], a[224:227], v[88:91], v[140:143]\n"
    "buffer_load_dwordx4 v245, s[48:51], s68 offen offset:1024 nt lds\n"
    "v_mfma_f32_16x16x32_bf16 v[120:123], a[228:231], v[12:15], v[120:123]\n"
    "v_exp_f32 v117, v117\n"
    "v_mfma_f32_16x16x32_bf16 v[124:127], a[228:231], v[28:31], v[124:127]\n"
    "ds_read_b128 a[220:223], v241 offset:0\n"
    "v_cvt_pk_bf16_f32 v188, v116, v117\n"
    "v_mfma_f32_16x16x32_bf16 v[128:131], a[228:231], v[44:47], v[128:131]\n"
    "v_exp_f32 v118, v118\n"
    "v_mfma_f32_16x16x32_bf16 v[132:135], a[228:231], v[60:63], v[132:135]\n"
    "buffer_load_dwordx4 v244, s[48:51], s68 offen offset:2048 nt lds\n"
    "v_mfma_f32_16x16x32_bf16 v[136:139], a[228:231], v[76:79], v[136:139]\n"
    "v_exp_f32 v119, v119\n"
    "v_mfma_f32_16x16x32_bf16 v[140:143], a[228:231], v[92:95], v[140:143]\n"
    "ds_read_b128 a[224:227], v240 offset:1024\n"
    "v_cvt_pk_bf16_f32 v189, v118, v119\n"
    "v_mfma_f32_16x16x32_bf16 a[96:99], a[232:235], v[144:147], 0\n"
    "v_exp_f32 v120, v120\n"
    "v_mfma_f32_16x16x32_bf16 a[100:103], a[232:235], v[148:151], 0\n"
    "buffer_load_dwordx4 v245, s[48:51], s68 offen offset:3072 nt lds\n"
    "v_mfma_f32_16x16x32_bf16 a[104:107], a[232:235], v[152:155], 0\n"
    "s_mov_b32 m0, s71\n"
    "v_exp_f32 v121, v121\n"
    "v_mfma_f32_16x16x32_bf16 a[108:111], a[232:235], v[156:159], 0\n"
    "ds_read_b128 a[228:231], v241 offset:1024\n"
    "v_cvt_pk_bf16_f32 v170, v120, v121\n"
    "v_mfma_f32_16x16x32_bf16 a[112:115], a[232:235], v[160:163], 0\n"
    "v_exp_f32 v122, v122\n"
    "v_mfma_f32_16x16x32_bf16 a[116:119], a[232:235], v[164:167], 0\n"
    "buffer_load_dwordx4 v246, s[52:55], s69 offen lds\n"
    "v_mfma_f32_16x16x32_bf16 a[120:123], a[236:239], v[144:147], 0\n"
    "v_exp_f32 v123, v123\n"
    "v_mfma_f32_16x16x32_bf16 a[124:127], a[236:239], v[148:151], 0\n"
    "ds_read_b64_tr_b16 a[232:233], v242 offset:0\n"
    "v_cvt_pk_bf16_f32 v171, v122, v123\n"
    "v_mfma_f32_16x16x32_bf16 a[128:131], a[236:239], v[152:155], 0\n"
    "v_exp_f32 v124, v124\n"
    "v_mfma_f32_16x16x32_bf16 a[132:135], a[236:239], v[156:159], 0\n"
    "ds_read_b64_tr_b16 a[234:235], v242 offset:4096\n"
    "v_mfma_f32_16x16x32_bf16 a[136:139], a[236:239], v[160:163], 0\n"
    "v_exp_f32 v125, v125\n"
    "v_mfma_f32_16x16x32_bf16 a[140:143], a[236:239], v[164:167], 0\n"
    "buffer_load_dwordx4 v247, s[52:55], s69 offen offset:1024 lds\n"
    "v_mfma_f32_16x16x32_bf16 a[144:147], a[240:243], v[144:147], 0\n"
    "v_exp_f32 v126, v126\n"
    "v_mfma_f32_16x16x32_bf16 a[148:151], a[240:243], v[148:151], 0\n"
    "ds_read_b64_tr_b16 a[236:237], v243 offset:0\n"
    "v_cvt_pk_bf16_f32 v174, v124, v125\n"
    "v_mfma_f32_16x16x32_bf16 a[152:155], a[240:243], v[152:155], 0\n"
    "v_exp_f32 v127, v127\n"
    "v_mfma_f32_16x16x32_bf16 a[156:159], a[240:243], v[156:159], 0\n"
    "ds_read_b64_tr_b16 a[238:239], v243 offset:4096\n"
    "v_cvt_pk_bf16_f32 v175, v126, v127\n"
    "v_mfma_f32_16x16x32_bf16 a[160:163], a[240:243], v[160:163], 0\n"
    "v_exp_f32 v128, v128\n"
    "v_mfma_f32_16x16x32_bf16 a[164:167], a[240:243], v[164:167], 0\n"
    "buffer_load_dwordx4 v246, s[52:55], s69 offen offset:2048 lds\n"
    "v_mfma_f32_16x16x32_bf16 a[168:171], a[244:247], v[144:147], 0\n"
    "v_exp_f32 v129, v129\n"
    "v_mfma_f32_16x16x32_bf16 a[172:175], a[244:247], v[148:151], 0\n"
    "ds_read_b64_tr_b16 a[240:241], v242 offset:512\n"
    "v_cvt_pk_bf16_f32 v178, v128, v129\n"
    "v_mfma_f32_16x16x32_bf16 a[176:179], a[244:247], v[152:155], 0\n"
    "v_exp_f32 v130, v130\n"
    "v_mfma_f32_16x16x32_bf16 a[180:183], a[244:247], v[156:159], 0\n"
    "ds_read_b64_tr_b16 a[242:243], v242 offset:4608\n"
    "v_mfma_f32_16x16x32_bf16 a[184:187], a[244:247], v[160:163], 0\n"
    "v_exp_f32 v131, v131\n"
    "v_mfma_f32_16x16x32_bf16 a[188:191], a[244:247], v[164:167], 0\n"
    "buffer_load_dwordx4 v247, s[52:55], s69 offen offset:3072 lds\n"
    "v_mfma_f32_16x16x32_bf16 a[192:195], a[248:251], v[144:147], 0\n"
    "v_exp_f32 v132, v132\n"
    "v_mfma_f32_16x16x32_bf16 a[196:199], a[248:251], v[148:151], 0\n"
    "ds_read_b64_tr_b16 a[244:245], v243 offset:512\n"
    "v_cvt_pk_bf16_f32 v179, v130, v131\n"
    "v_mfma_f32_16x16x32_bf16 a[200:203], a[248:251], v[152:155], 0\n"
    "v_exp_f32 v133, v133\n"
    "v_mfma_f32_16x16x32_bf16 a[204:207], a[248:251], v[156:159], 0\n"
    "ds_read_b64_tr_b16 a[246:247], v243 offset:4608\n"
    "v_cvt_pk_bf16_f32 v182, v132, v133\n"
    "v_mfma_f32_16x16x32_bf16 a[208:211], a[248:251], v[160:163], 0\n"
    "s_waitcnt lgkmcnt(8)\n"
    "v_exp_f32 v134, v134\n"
    "v_mfma_f32_16x16x32_bf16 a[212:215], a[248:251], v[164:167], 0\n"
    "s_mov_b32 s74, 15\n"
    "s_cmp_lg_u32 s18, 0\n"
    "s_cbranch_scc1 LH%=\n"
    "LC%=:\n"
    "v_mfma_f32_16x16x32_bf16 v[96:99], a[216:219], v[0:3], v[192:195]\n"
    "s_mov_b32 s69, s68\n"
    "v_exp_f32 v135, v135\n"
    "s_nop 3\n"
    "v_mfma_f32_16x16x32_bf16 v[100:103], a[216:219], v[16:19], v[196:199]\n"
    "v_cvt_pk_bf16_f32 v183, v134, v135\n"
    "v_mfma_f32_16x16x32_bf16 v[104:107], a[216:219], v[32:35], v[200:203]\n"
    "s_add_u32 s68, s68, 0x2000\n"
    "v_exp_f32 v136, v136\n"
    "v_mfma_f32_16x16x32_bf16 v[108:111], a[216:219], v[48:51], v[204:207]\n"
    "s_nop 3\n"
    "v_mfma_f32_16x16x32_bf16 v[112:115], a[216:219], v[64:67], v[208:211]\n"
    "s_add_u32 s71, s70, 0x14000\n"
    "v_exp_f32 v137, v137\n"
    "v_mfma_f32_16x16x32_bf16 v[116:119], a[216:219], v[80:83], v[212:215]\n"
    "v_cvt_pk_bf16_f32 v186, v136, v137\n"
    "v_mfma_f32_16x16x32_bf16 v[96:99], a[220:223], v[4:7], v[96:99]\n"
    "s_add_u32 s70, s70, s84\n"
    "v_exp_f32 v138, v138\n"
    "v_mfma_f32_16x16x32_bf16 v[100:103], a[220:223], v[20:23], v[100:103]\n"
    "ds_read_b128 a[216:219], v240 offset:4096\n"
    "s_nop 3\n"
    "v_mfma_f32_16x16x32_bf16 v[104:107], a[220:223], v[36:39], v[104:107]\n"
    "s_and_b32 s68, s68, 0x3ffff\n"
    "v_exp_f32 v139, v139\n"
    "v_mfma_f32_16x16x32_bf16 v[108:111], a[220:223], v[52:55], v[108:111]\n"
    "v_cvt_pk_bf16_f32 v187, v138, v139\n"
    "v_mfma_f32_16x16x32_bf16 v[112:115], a[220:223], v[68:71], v[112:115]\n"
    "s_mov_b32 s54, s50\n"
    "v_exp_f32 v140, v140\n"
    "s_nop 3\n"
    "v_mfma_f32_16x16x32_bf16 v[116:119], a[220:223], v[84:87], v[116:119]\n"
    "v_mfma_f32_16x16x32_bf16 v[96:99], a[224:227], v[8:11], v[96:99]\n"
    "s_cmp_le_i32 s63, 0\n"
    "v_exp_f32 v141, v141\n"
    "v_mfma_f32_16x16x32_bf16 v[100:103], a[224:227], v[24:27], v[100:103]\n"
    "ds_read_b128 a[220:223], v241 offset:4096\n"
    "v_cvt_pk_bf16_f32 v190, v140, v141\n"
    "v_mfma_f32_16x16x32_bf16 v[104:107], a[224:227], v[40:43], v[104:107]\n"
    "s_cselect_b32 s50, 0, s50\n"
    "v_exp_f32 v142, v142\n"
    "s_nop 3\n"
    "v_mfma_f32_16x16x32_bf16 v[108:111], a[224:227], v[56:59], v[108:111]\n"
    "v_mfma_f32_16x16x32_bf16 v[112:115], a[224:227], v[72:75], v[112:115]\n"
    "s_sub_u32 s63, s63, 1\n"
    "v_exp_f32 v143, v143\n"
    "v_mfma_f32_16x16x32_bf16 v[116:119], a[224:227], v[88:91], v[116:119]\n"
    "v_cvt_pk_bf16_f32 v191, v142, v143\n"
    "v_mfma_f32_16x16x32_bf16 v[96:99], a[228:231], v[12:15], v[96:99]\n"
    "s_nop 3\n"
    "v_mfma_f32_16x16x32_bf16 v[100:103], a[228:231], v[28:31], v[100:103]\n"
    "ds_read_b128 a[224:227], v240 offset:5120\n"
    "v_mfma_f32_16x16x32_bf16 v[104:107], a[228:231], v[44:47], v[104:107]\n"
    "v_mfma_f32_16x16x32_bf16 v[108:111], a[228:231], v[60:63], v[108:111]\n"
    "s_nop 3\n"
    "v_mfma_f32_16x16x32_bf16 v[112:115], a[228:231], v[76:79], v[112:115]\n"
    "s_waitcnt lgkmcnt(3)\n"
    "v_mfma_f32_16x16x32_bf16 v[116:119], a[228:231], v[92:95], v[116:119]\n"
    "v_mfma_f32_16x16x32_bf16 a[0:3], a[232:235], v[168:171], a[0:3]\n"
    "v_exp_f32 v96, v96\n"
    "v_mfma_f32_16x16x32_bf16 a[4:7], a[232:235], v[172:175], a[4:7]\n"
    "ds_read_b128 a[228:231], v241 offset:5120\n"
    "s_nop 3\n"
    "v_mfma_f32_16x16x32_bf16 a[8:11], a[232:235], v[176:179], a[8:11]\n"
    "v_exp_f32 v97, v97\n"
    "v_mfma_f32_16x16x32_bf16 a[12:15], a[232:235], v[180:183], a[12:15]\n"
    "v_exp_f32 v98, v98\n"
    "v_mfma_f32_16x16x32_bf16 a[16:19], a[232:235], v[184:187], a[16:19]\n"
    "v_exp_f32 v99, v99\n"
    "s_nop 3\n"
    "v_mfma_f32_16x16x32_bf16 a[20:23], a[232:235], v[188:191], a[20:23]\n"
    "v_cvt_pk_bf16_f32 v144, v96, v97\n"
    "v_cvt_pk_bf16_f32 v145, v98, v99\n"
    "v_mfma_f32_16x16x32_bf16 a[24:27], a[236:239], v[168:171], a[24:27]\n"
    "v_exp_f32 v100, v100\n"
    "v_mfma_f32_16x16x32_bf16 a[28:31], a[236:239], v[172:175], a[28:31]\n"
    "ds_read_b64_tr_b16 a[232:233], v242 offset:1024\n"
    "v_mfma_f32_16x16x32_bf16 a[32:35], a[236:239], v[176:179], a[32:35]\n"
    "v_exp_f32 v101, v101\n"
    "s_nop 3\n"
    "v_mfma_f32_16x16x32_bf16 a[36:39], a[236:239], v[180:183], a[36:39]\n"
    "ds_read_b64_tr_b16 a[234:235], v242 offset:5120\n"
    "v_cvt_pk_bf16_f32 v148, v100, v101\n"
    "v_mfma_f32_16x16x32_bf16 a[40:43], a[236:239], v[184:187], a[40:43]\n"
    "v_exp_f32 v102, v102\n"
    "v_mfma_f32_16x16x32_bf16 a[44:47], a[236:239], v[188:191], a[44:47]\n"
    "v_mfma_f32_16x16x32_bf16 a[48:51], a[240:243], v[168:171], a[48:51]\n"
    "v_exp_f32 v103, v103\n"
    "s_nop 3\n"
    "v_mfma_f32_16x16x32_bf16 a[52:55], a[240:243], v[172:175], a[52:55]\n"
    "ds_read_b64_tr_b16 a[236:237], v243 offset:1024\n"
    "v_cvt_pk_bf16_f32 v149, v102, v103\n"
    "v_mfma_f32_16x16x32_bf16 a[56:59], a[240:243], v[176:179], a[56:59]\n"
    "v_exp_f32 v104, v104\n"
    "v_mfma_f32_16x16x32_bf16 a[60:63], a[240:243], v[180:183], a[60:63]\n"
    "ds_read_b64_tr_b16 a[238:239], v243 offset:5120\n"
    "s_nop 3\n"
    "v_mfma_f32_16x16x32_bf16 a[64:67], a[240:243], v[184:187], a[64:67]\n"
    "v_exp_f32 v105, v105\n"
    "v_mfma_f32_16x16x32_bf16 a[68:71], a[240:243], v[188:191], a[68:71]\n"
    "v_cvt_pk_bf16_f32 v152, v104, v105\n"
    "v_mfma_f32_16x16x32_bf16 a[72:75], a[244:247], v[168:171], a[72:75]\n"
    "v_exp_f32 v106, v106\n"
    "v_mfma_f32_16x16x32_bf16 a[76:79], a[244:247], v[172:175], a[76:79]\n"
    "ds_read_b64_tr_b16 a[240:241], v242 offset:1536\n"
    "s_nop 3\n"
    "v_mfma_f32_16x16x32_bf16 a[80:83], a[244:247], v[176:179], a[80:83]\n"
    "v_exp_f32 v107, v107\n"
    "v_mfma_f32_16x16x32_bf16 a[84:87], a[244:247], v[180:183], a[84:87]\n"
    "ds_read_b64_tr_b16 a[242:243], v242 offset:5632\n"
    "v_cvt_pk_bf16_f32 v153, v106, v107\n"
    "v_mfma_f32_16x16x32_bf16 a[88:91], a[244:247], v[184:187], a[88:91]\n"
    "s_waitcnt lgkmcnt(6)\n"
    "v_exp_f32 v108, v108\n"
    "s_nop 3\n"
    "v_mfma_f32_16x16x32_bf16 a[92:95], a[244:247], v[188:191], a[92:95]\n"
    "v_mfma_f32_16x16x32_bf16 v[120:123], a[216:219], v[0:3], v[192:195]\n"
    "s_cmp_ge_u32 s70, s76\n"
    "v_exp_f32 v109, v109\n"
    "v_mfma_f32_16x16x32_bf16 v[124:127], a[216:219], v[16:19], v[196:199]\n"
    "ds_read_b64_tr_b16 a[244:245], v243 offset:1536\n"
    "v_cvt_pk_bf16_f32 v156, v108, v109\n"
    "v_mfma_f32_16x16x32_bf16 v[128:131], a[216:219], v[32:35], v[200:203]\n"
    "s_cselect_b32 s84, s75, 0x4000\n"
    "v_exp_f32 v110, v110\n"
    "s_nop 3\n"
    "v_mfma_f32_16x16x32_bf16 v[132:135], a[216:219], v[48:51], v[204:207]\n"
    "ds_read_b64_tr_b16 a[246:247], v243 offset:5632\n"
    "v_mfma_f32_16x16x32_bf16 v[136:139], a[216:219], v[64:67], v[208:211]\n"
    "s_mov_b32 s73, s72\n"
    "v_exp_f32 v111, v111\n"
    "v_mfma_f32_16x16x32_bf16 v[140:143], a[216:219], v[80:83], v[212:215]\n"
    "v_cvt_pk_bf16_f32 v157, v110, v111\n"
    "v_mfma_f32_16x16x32_bf16 v[120:123], a[220:223], v[4:7], v[120:123]\n"
    "s_cmp_eq_u32 s70, s64\n"
    "v_exp_f32 v112, v112\n"
    "s_nop 3\n"
    "v_mfma_f32_16x16x32_bf16 v[124:127], a[220:223], v[20:23], v[124:127]\n"
    "v_add_u32 v242, s73, v242\n"
    "v_add_u32 v243, s73, v243\n"
    "v_mfma_f32_16x16x32_bf16 v[128:131], a[220:223], v[36:39], v[128:131]\n"
    "s_cselect_b32 s72, s75, 0x4000\n"
    "v_exp_f32 v113, v113\n"
    "v_mfma_f32_16x16x32_bf16 v[132:135], a[220:223], v[52:55], v[132:135]\n"
    "v_cvt_pk_bf16_f32 v160, v112, v113\n"
    "s_nop 3\n"
    "v_mfma_f32_16x16x32_bf16 v[136:139], a[220:223], v[68:71], v[136:139]\n"
    "v_exp_f32 v114, v114\n"
    "v_mfma_f32_16x16x32_bf16 v[140:143], a[220:223], v[84:87], v[140:143]\n"
    "v_add_u32 v240, s72, v240\n"
    "v_add_u32 v241, s72, v241\n"
    "v_mfma_f32_16x16x32_bf16 v[120:123], a[224:227], v[8:11], v[120:123]\n"
    "s_waitcnt vmcnt(16) lgkmcnt(0)\n"
    "s_barrier\n"
    "s_mov_b32 m0, s70\n"
    "v_mfma_f32_16x16x32_bf16 v[124:127], a[224:227], v[24:27], v[124:127]\n"
    "buffer_load_dwordx4 v244, s[48:51], s68 offen nt lds\n"
    "s_nop 3\n"
    "v_mfma_f32_16x16x32_bf16 v[128:131], a[224:227], v[40:43], v[128:131]\n"
    "v_exp_f32 v115, v115\n"
    "v_mfma_f32_16x16x32_bf16 v[132:135], a[224:227], v[56:59], v[132:135]\n"
    "ds_read_b128 a[216:219], v240 offset:0\n"
    "v_cvt_pk_bf16_f32 v161, v114, v115\n"
    "v_mfma_f32_16x16x32_bf16 v[136:139], a[224:227], v[72:75], v[136:139]\n"
    "v_exp_f32 v116, v116\n"
    "s_nop 3\n"
    "v_mfma_f32_16x16x32_bf16 v[140:143], a[224:227], v[88:91], v[140:143]\n"
    "buffer_load_dwordx4 v245, s[48:51], s68 offen offset:1024 nt lds\n"
    "v_mfma_f32_16x16x32_bf16 v[120:123], a[228:231], v[12:15], v[120:123]\n"
    "v_exp_f32 v117, v117\n"
    "v_mfma_f32_16x16x32_bf16 v[124:127], a[228:231], v[28:31], v[124:127]\n"
    "ds_read_b128 a[220:223], v241 offset:0\n"
    "v_cvt_pk_bf16_f32 v164, v116, v117\n"
    "v_mfma_f32_16x16x32_bf16 v[128:131], a[228:231], v[44:47], v[128:131]\n"
    "v_exp_f32 v118, v118\n"
    "s_nop 3\n"
    "v_mfma_f32_16x16x32_bf16 v[132:135], a[228:231], v[60:63], v[132:135]\n"
    "buffer_load_dwordx4 v244, s[48:51], s68 offen offset:2048 nt lds\n"
    "v_mfma_f32_16x16x32_bf16 v[136:139], a[228:231], v[76:79], v[136:139]\n"
    "v_exp_f32 v119, v119\n"
    "v_mfma_f32_16x16x32_bf16 v[140:143], a[228:231], v[92:95], v[140:143]\n"
    "ds_read_b128 a[224:227], v240 offset:1024\n"
    "v_cvt_pk_bf16_f32 v165, v118, v119\n"
    "v_mfma_f32_16x16x32_bf16 a[96:99], a[232:235], v[168:171], a[96:99]\n"
    "v_exp_f32 v120, v120\n"
    "s_nop 3\n"
    "v_mfma_f32_16x16x32_bf16 a[100:103], a[232:235], v[172:175], a[100:103]\n"
    "buffer_load_dwordx4 v245, s[48:51], s68 offen offset:3072 nt lds\n"
    "v_mfma_f32_16x16x32_bf16 a[104:107], a[232:235], v[176:179], a[104:107]\n"
    "s_mov_b32 m0, s71\n"
    "v_exp_f32 v121, v121\n"
    "v_mfma_f32_16x16x32_bf16 a[108:111], a[232:235], v[180:183], a[108:111]\n"
    "ds_read_b128 a[228:231], v241 offset:1024\n"
    "v_cvt_pk_bf16_f32 v146, v120, v121\n"
    "s_nop 3\n"
    "v_mfma_f32_16x16x32_bf16 a[112:115], a[232:235], v[184:187], a[112:115]\n"
    "v_exp_f32 v122, v122\n"
    "v_mfma_f32_16x16x32_bf16 a[116:119], a[232:235], v[188:191], a[116:119]\n"
    "buffer_load_dwordx4 v246, s[52:55], s69 offen lds\n"
    "v_mfma_f32_16x16x32_bf16 a[120:123], a[236:239], v[168:171], a[120:123]\n"
    "v_exp_f32 v123, v123\n"
    "v_mfma_f32_16x16x32_bf16 a[124:127], a[236:239], v[172:175], a[124:127]\n"
    "ds_read_b64_tr_b16 a[232:233], v242 offset:0\n"
    "v_cvt_pk_bf16_f32 v147, v122, v123\n"
    "s_nop 3\n"
    "v_mfma_f32_16x16x32_bf16 a[128:131], a[236:239], v[176:179], a[128:131]\n"
    "v_exp_f32 v124, v124\n"
    "v_mfma_f32_16x16x32_bf16 a[132:135], a[236:239], v[180:183], a[132:135]\n"
    "ds_read_b64_tr_b16 a[234:235], v242 offset:4096\n"
    "v_mfma_f32_16x16x32_bf16 a[136:139], a[236:239], v[184:187], a[136:139]\n"
    "v_exp_f32 v125, v125\n"
    "s_nop 3\n"
    "v_mfma_f32_16x16x32_bf16 a[140:143], a[236:239], v[188:191], a[140:143]\n"
    "buffer_load_dwordx4 v247, s[52:55], s69 offen offset:1024 lds\n"
    "v_mfma_f32_16x16x32_bf16 a[144:147], a[240:243], v[168:171], a[144:147]\n"
    "v_exp_f32 v126, v126\n"
    "v_mfma_f32_16x16x32_bf16 a[148:151], a[240:243], v[172:175], a[148:151]\n"
    "ds_read_b64_tr_b16 a[236:237], v243 offset:0\n"
    "v_cvt_pk_bf16_f32 v150, v124, v125\n"
    "v_mfma_f32_16x16x32_bf16 a[152:155], a[240:243], v[176:179], a[152:155]\n"
    "v_exp_f32 v127, v127\n"
    "s_nop 3\n"
    "v_mfma_f32_16x16x32_bf16 a[156:159], a[240:243], v[180:183], a[156:159]\n"
    "ds_read_b64_tr_b16 a[238:239], v243 offset:4096\n"
    "v_cvt_pk_bf16_f32 v151, v126, v127\n"
    "v_mfma_f32_16x16x32_bf16 a[160:163], a[240:243], v[184:187], a[160:163]\n"
    "v_exp_f32 v128, v128\n"
    "v_mfma_f32_16x16x32_bf16 a[164:167], a[240:243], v[188:191], a[164:167]\n"
    "buffer_load_dwordx4 v246, s[52:55], s69 offen offset:2048 lds\n"
    "v_mfma_f32_16x16x32_bf16 a[168:171], a[244:247], v[168:171], a[168:171]\n"
    "v_exp_f32 v129, v129\n"
    "s_nop 3\n"
    "v_mfma_f32_16x16x32_bf16 a[172:175], a[244:247], v[172:175], a[172:175]\n"
    "ds_read_b64_tr_b16 a[240:241], v242 offset:512\n"
    "v_cvt_pk_bf16_f32 v154, v128, v129\n"
    "v_mfma_f32_16x16x32_bf16 a[176:179], a[244:247], v[176:179], a[176:179]\n"
    "v_exp_f32 v130, v130\n"
    "v_mfma_f32_16x16x32_bf16 a[180:183], a[244:247], v[180:183], a[180:183]\n"
    "ds_read_b64_tr_b16 a[242:243], v242 offset:4608\n"
    "s_nop 3\n"
    "v_mfma_f32_16x16x32_bf16 a[184:187], a[244:247], v[184:187], a[184:187]\n"
    "v_exp_f32 v131, v131\n"
    "v_mfma_f32_16x16x32_bf16 a[188:191], a[244:247], v[188:191], a[188:191]\n"
    "buffer_load_dwordx4 v247, s[52:55], s69 offen offset:3072 lds\n"
    "v_mfma_f32_16x16x32_bf16 a[192:195], a[248:251], v[168:171], a[192:195]\n"
    "v_exp_f32 v132, v132\n"
    "v_mfma_f32_16x16x32_bf16 a[196:199], a[248:251], v[172:175], a[196:199]\n"
    "ds_read_b64_tr_b16 a[244:245], v243 offset:512\n"
    "v_cvt_pk_bf16_f32 v155, v130, v131\n"
    "s_nop 3\n"
    "v_mfma_f32_16x16x32_bf16 a[200:203], a[248:251], v[176:179], a[200:203]\n"
    "v_exp_f32 v133, v133\n"
    "v_mfma_f32_16x16x32_bf16 a[204:207], a[248:251], v[180:183], a[204:207]\n"
    "ds_read_b64_tr_b16 a[246:247], v243 offset:4608\n"
    "v_cvt_pk_bf16_f32 v158, v132, v133\n"
    "v_mfma_f32_16x16x32_bf16 a[208:211], a[248:251], v[184:187], a[208:211]\n"
    "s_waitcnt lgkmcnt(8)\n"
    "v_exp_f32 v134, v134\n"
    "v_mfma_f32_16x16x32_bf16 a[212:215], a[248:251], v[188:191], a[212:215]\n"
    "v_mfma_f32_16x16x32_bf16 v[96:99], a[216:219], v[0:3], v[192:195]\n"
    "s_mov_b32 s69, s68\n"
    "v_exp_f32 v135, v135\n"
    "s_nop 3\n"
    "v_mfma_f32_16x16x32_bf16 v[100:103], a[216:219], v[16:19], v[196:199]\n"
    "v_cvt_pk_bf16_f32 v159, v134, v135\n"
    "v_mfma_f32_16x16x32_bf16 v[104:107], a[216:219], v[32:35], v[200:203]\n"
    "s_add_u32 s68, s68, 0x2000\n"
    "v_exp_f32 v136, v136\n"
    "v_mfma_f32_16x16x32_bf16 v[108:111], a[216:219], v[48:51], v[204:207]\n"
    "s_nop 3\n"
    "v_mfma_f32_16x16x32_bf16 v[112:115], a[216:219], v[64:67], v[208:211]\n"
    "s_add_u32 s71, s70, 0x14000\n"
    "v_exp_f32 v137, v137\n"
    "v_mfma_f32_16x16x32_bf16 v[116:119], a[216:219], v[80:83], v[212:215]\n"
    "v_cvt_pk_bf16_f32 v162, v136, v137\n"
    "v_mfma_f32_16x16x32_bf16 v[96:99], a[220:223], v[4:7], v[96:99]\n"
    "s_add_u32 s70, s70, s84\n"
    "v_exp_f32 v138, v138\n"
    "v_mfma_f32_16x16x32_bf16 v[100:103], a[220:223], v[20:23], v[100:103]\n"
    "ds_read_b128 a[216:219], v240 offset:4096\n"
    "s_nop 3\n"
    "v_mfma_f32_16x16x32_bf16 v[104:107], a[220:223], v[36:39], v[104:107]\n"
    "s_and_b32 s68, s68, 0x3ffff\n"
    "v_exp_f32 v139, v139\n"
    "v_mfma_f32_16x16x32_bf16 v[108:111], a[220:223], v[52:55], v[108:111]\n"
    "v_cvt_pk_bf16_f32 v163, v138, v139\n"
    "v_mfma_f32_16x16x32_bf16 v[112:115], a[220:223], v[68:71], v[112:115]\n"
    "s_mov_b32 s54, s50\n"
    "v_exp_f32 v140, v140\n"
    "s_nop 3\n"
    "v_mfma_f32_16x16x32_bf16 v[116:119], a[220:223], v[84:87], v[116:119]\n"
    "v_mfma_f32_16x16x32_bf16 v[96:99], a[224:227], v[8:11], v[96:99]\n"
    "s_cmp_le_i32 s63, 0\n"
    "v_exp_f32 v141, v141\n"
    "v_mfma_f32_16x16x32_bf16 v[100:103], a[224:227], v[24:27], v[100:103]\n"
    "ds_read_b128 a[220:223], v241 offset:4096\n"
    "v_cvt_pk_bf16_f32 v166, v140, v141\n"
    "v_mfma_f32_16x16x32_bf16 v[104:107], a[224:227], v[40:43], v[104:107]\n"
    "s_cselect_b32 s50, 0, s50\n"
    "v_exp_f32 v142, v142\n"
    "s_nop 3\n"
    "v_mfma_f32_16x16x32_bf16 v[108:111], a[224:227], v[56:59], v[108:111]\n"
    "v_mfma_f32_16x16x32_bf16 v[112:115], a[224:227], v[72:75], v[112:115]\n"
    "s_sub_u32 s63, s63, 1\n"
    "v_exp_f32 v143, v143\n"
    "v_mfma_f32_16x16x32_bf16 v[116:119], a[224:227], v[88:91], v[116:119]\n"
    "v_cvt_pk_bf16_f32 v167, v142, v143\n"
    "v_mfma_f32_16x16x32_bf16 v[96:99], a[228:231], v[12:15], v[96:99]\n"
    "s_nop 3\n"
    "v_mfma_f32_16x16x32_bf16 v[100:103], a[228:231], v[28:31], v[100:103]\n"
    "ds_read_b128 a[224:227], v240 offset:5120\n"
    "v_mfma_f32_16x16x32_bf16 v[104:107], a[228:231], v[44:47], v[104:107]\n"
    "v_mfma_f32_16x16x32_bf16 v[108:111], a[228:231], v[60:63], v[108:111]\n"
    "s_nop 3\n"
    "v_mfma_f32_16x16x32_bf16 v[112:115], a[228:231], v[76:79], v[112:115]\n"
    "s_waitcnt lgkmcnt(3)\n"
    "v_mfma_f32_16x16x32_bf16 v[116:119], a[228:231], v[92:95], v[116:119]\n"
    "v_mfma_f32_16x16x32_bf16 a[0:3], a[232:235], v[144:147], a[0:3]\n"
    "v_exp_f32 v96, v96\n"
    "v_mfma_f32_16x16x32_bf16 a[4:7], a[232:235], v[148:151], a[4:7]\n"
    "ds_read_b128 a[228:231], v241 offset:5120\n"
    "s_nop 3\n"
    "v_mfma_f32_16x16x32_bf16 a[8:11], a[232:235], v[152:155], a[8:11]\n"
    "v_exp_f32 v97, v97\n"
    "v_mfma_f32_16x16x32_bf16 a[12:15], a[232:235], v[156:159], a[12:15]\n"
    "v_exp_f32 v98, v98\n"
    "v_mfma_f32_16x16x32_bf16 a[16:19], a[232:235], v[160:163], a[16:19]\n"
    "v_exp_f32 v99, v99\n"
    "s_nop 3\n"
    "v_mfma_f32_16x16x32_bf16 a[20:23], a[232:235], v[164:167], a[20:23]\n"
    "v_cvt_pk_bf16_f32 v168, v96, v97\n"
    "v_cvt_pk_bf16_f32 v169, v98, v99\n"
    "v_mfma_f32_16x16x32_bf16 a[24:27], a[236:239], v[144:147], a[24:27]\n"
    "v_exp_f32 v100, v100\n"
    "v_mfma_f32_16x16x32_bf16 a[28:31], a[236:239], v[148:151], a[28:31]\n"
    "ds_read_b64_tr_b16 a[232:233], v242 offset:1024\n"
    "v_mfma_f32_16x16x32_bf16 a[32:35], a[236:239], v[152:155], a[32:35]\n"
    "v_exp_f32 v101, v101\n"
    "s_nop 3\n"
    "v_mfma_f32_16x16x32_bf16 a[36:39], a[236:239], v[156:159], a[36:39]\n"
    "ds_read_b64_tr_b16 a[234:235], v242 offset:5120\n"
    "v_cvt_pk_bf16_f32 v172, v100, v101\n"
    "v_mfma_f32_16x16x32_bf16 a[40:43], a[236:239], v[160:163], a[40:43]\n"
    "v_exp_f32 v102, v102\n"
    "v_mfma_f32_16x16x32_bf16 a[44:47], a[236:239], v[164:167], a[44:47]\n"
    "v_mfma_f32_16x16x32_bf16 a[48:51], a[240:243], v[144:147], a[48:51]\n"
    "v_exp_f32 v103, v103\n"
    "s_nop 3\n"
    "v_mfma_f32_16x16x32_bf16 a[52:55], a[240:243], v[148:151], a[52:55]\n"
    "ds_read_b64_tr_b16 a[236:237], v243 offset:1024\n"
    "v_cvt_pk_bf16_f32 v173, v102, v103\n"
    "v_mfma_f32_16x16x32_bf16 a[56:59], a[240:243], v[152:155], a[56:59]\n"
    "v_exp_f32 v104, v104\n"
    "v_mfma_f32_16x16x32_bf16 a[60:63], a[240:243], v[156:159], a[60:63]\n"
    "ds_read_b64_tr_b16 a[238:239], v243 offset:5120\n"
    "s_nop 3\n"
    "v_mfma_f32_16x16x32_bf16 a[64:67], a[240:243], v[160:163], a[64:67]\n"
    "v_exp_f32 v105, v105\n"
    "v_mfma_f32_16x16x32_bf16 a[68:71], a[240:243], v[164:167], a[68:71]\n"
    "v_cvt_pk_bf16_f32 v176, v104, v105\n"
    "v_mfma_f32_16x16x32_bf16 a[72:75], a[244:247], v[144:147], a[72:75]\n"
    "v_exp_f32 v106, v106\n"
    "v_mfma_f32_16x16x32_bf16 a[76:79], a[244:247], v[148:151], a[76:79]\n"
    "ds_read_b64_tr_b16 a[240:241], v242 offset:1536\n"
    "s_nop 3\n"
    "v_mfma_f32_16x16x32_bf16 a[80:83], a[244:247], v[152:155], a[80:83]\n"
    "v_exp_f32 v107, v107\n"
    "v_mfma_f32_16x16x32_bf16 a[84:87], a[244:247], v[156:159], a[84:87]\n"
    "ds_read_b64_tr_b16 a[242:243], v242 offset:5632\n"
    "v_cvt_pk_bf16_f32 v177, v106, v107\n"
    "v_mfma_f32_16x16x32_bf16 a[88:91], a[244:247], v[160:163], a[88:91]\n"
    "s_waitcnt lgkmcnt(6)\n"
    "v_exp_f32 v108, v108\n"
    "s_nop 3\n"
    "v_mfma_f32_16x16x32_bf16 a[92:95], a[244:247], v[164:167], a[92:95]\n"
    "v_mfma_f32_16x16x32_bf16 v[120:123], a[216:219], v[0:3], v[192:195]\n"
    "s_cmp_ge_u32 s70, s76\n"
    "v_exp_f32 v109, v109\n"
    "v_mfma_f32_16x16x32_bf16 v[124:127], a[216:219], v[16:19], v[196:199]\n"
    "ds_read_b64_tr_b16 a[244:245], v243 offset:1536\n"
    "v_cvt_pk_bf16_f32 v180, v108, v109\n"
    "v_mfma_f32_16x16x32_bf16 v[128:131], a[216:219], v[32:35], v[200:203]\n"
    "s_cselect_b32 s84, s75, 0x4000\n"
    "v_exp_f32 v110, v110\n"
    "s_nop 3\n"
    "v_mfma_f32_16x16x32_bf16 v[132:135], a[216:219], v[48:51], v[204:207]\n"
    "ds_read_b64_tr_b16 a[246:247], v243 offset:5632\n"
    "v_mfma_f32_16x16x32_bf16 v[136:139], a[216:219], v[64:67], v[208:211]\n"
    "s_mov_b32 s73, s72\n"
    "v_exp_f32 v111, v111\n"
    "v_mfma_f32_16x16x32_bf16 v[140:143], a[216:219], v[80:83], v[212:215]\n"
    "v_cvt_pk_bf16_f32 v181, v110, v111\n"
    "v_mfma_f32_16x16x32_bf16 v[120:123], a[220:223], v[4:7], v[120:123]\n"
    "s_cmp_eq_u32 s70, s64\n"
    "v_exp_f32 v112, v112\n"
    "s_nop 3\n"
    "v_mfma_f32_16x16x32_bf16 v[124:127], a[220:223], v[20:23], v[124:127]\n"
    "v_add_u32 v242, s73, v242\n"
    "v_add_u32 v243, s73, v243\n"
    "v_mfma_f32_16x16x32_bf16 v[128:131], a[220:223], v[36:39], v[128:131]\n"
    "s_cselect_b32 s72, s75, 0x4000\n"
    "v_exp_f32 v113, v113\n"
    "v_mfma_f32_16x16x32_bf16 v[132:135], a[220:223], v[52:55], v[132:135]\n"
    "v_cvt_pk_bf16_f32 v184, v112, v113\n"
    "s_nop 3\n"
    "v_mfma_f32_16x16x32_bf16 v[136:139], a[220:223], v[68:71], v[136:139]\n"
    "v_exp_f32 v114, v114\n"
    "v_mfma_f32_16x16x32_bf16 v[140:143], a[220:223], v[84:87], v[140:143]\n"
    "v_add_u32 v240, s72, v240\n"
    "v_add_u32 v241, s72, v241\n"
    "v_mfma_f32_16x16x32_bf16 v[120:123], a[224:227], v[8:11], v[120:123]\n"
    "s_waitcnt vmcnt(16) lgkmcnt(0)\n"
    "s_barrier\n"
    "s_mov_b32 m0, s70\n"
    "v_mfma_f32_16x16x32_bf16 v[124:127], a[224:227], v[24:27], v[124:127]\n"
    "buffer_load_dwordx4 v244, s[48:51], s68 offen nt lds\n"
    "s_nop 3\n"
    "v_mfma_f32_16x16x32_bf16 v[128:131], a[224:227], v[40:43], v[128:131]\n"
    "v_exp_f32 v115, v115\n"
    "v_mfma_f32_16x16x32_bf16 v[132:135], a[224:227], v[56:59], v[132:135]\n"
    "ds_read_b128 a[216:219], v240 offset:0\n"
    "v_cvt_pk_bf16_f32 v185, v114, v115\n"
    "v_mfma_f32_16x16x32_bf16 v[136:139], a[224:227], v[72:75], v[136:139]\n"
    "v_exp_f32 v116, v116\n"
    "s_nop 3\n"
    "v_mfma_f32_16x16x32_bf16 v[140:143], a[224:227], v[88:91], v[140:143]\n"
    "buffer_load_dwordx4 v245, s[48:51], s68 offen offset:1024 nt lds\n"
    "v_mfma_f32_16x16x32_bf16 v[120:123], a[228:231], v[12:15], v[120:123]\n"
    "v_exp_f32 v117, v117\n"
    "v_mfma_f32_16x16x32_bf16 v[124:127], a[228:231], v[28:31], v[124:127]\n"
    "ds_read_b128 a[220:223], v241 offset:0\n"
    "v_cvt_pk_bf16_f32 v188, v116, v117\n"
    "v_mfma_f32_16x16x32_bf16 v[128:131], a[228:231], v[44:47], v[128:131]\n"
    "v_exp_f32 v118, v118\n"
    "s_nop 3\n"
    "v_mfma_f32_16x16x32_bf16 v[132:135], a[228:231], v[60:63], v[132:135]\n"
    "buffer_load_dwordx4 v244, s[48:51], s68 offen offset:2048 nt lds\n"
    "v_mfma_f32_16x16x32_bf16 v[136:139], a[228:231], v[76:79], v[136:139]\n"
    "v_exp_f32 v119, v119\n"
    "v_mfma_f32_16x16x32_bf16 v[140:143], a[228:231], v[92:95], v[140:143]\n"
    "ds_read_b128 a[224:227], v240 offset:1024\n"
    "v_cvt_pk_bf16_f32 v189, v118, v119\n"
    "v_mfma_f32_16x16x32_bf16 a[96:99], a[232:235], v[144:147], a[96:99]\n"
    "v_exp_f32 v120, v120\n"
    "s_nop 3\n"
    "v_mfma_f32_16x16x32_bf16 a[100:103], a[232:235], v[148:151], a[100:103]\n"
    "buffer_load_dwordx4 v245, s[48:51], s68 offen offset:3072 nt lds\n"
    "v_mfma_f32_16x16x32_bf16 a[104:107], a[232:235], v[152:155], a[104:107]\n"
    "s_mov_b32 m0, s71\n"
    "v_exp_f32 v121, v121\n"
    "v_mfma_f32_16x16x32_bf16 a[108:111], a[232:235], v[156:159], a[108:111]\n"
    "ds_read_b128 a[228:231], v241 offset:1024\n"
    "v_cvt_pk_bf16_f32 v170, v120, v121\n"
    "s_nop 3\n"
    "v_mfma_f32_16x16x32_bf16 a[112:115], a[232:235], v[160:163], a[112:115]\n"
    "v_exp_f32 v122, v122\n"
    "v_mfma_f32_16x16x32_bf16 a[116:119], a[232:235], v[164:167], a[116:119]\n"
    "buffer_load_dwordx4 v246, s[52:55], s69 offen lds\n"
    "v_mfma_f32_16x16x32_bf16 a[120:123], a[236:239], v[144:147], a[120:123]\n"
    "v_exp_f32 v123, v123\n"
    "v_mfma_f32_16x16x32_bf16 a[124:127], a[236:239], v[148:151], a[124:127]\n"
    "ds_read_b64_tr_b16 a[232:233], v242 offset:0\n"
    "v_cvt_pk_bf16_f32 v171, v122, v123\n"
    "s_nop 3\n"
    "v_mfma_f32_16x16x32_bf16 a[128:131], a[236:239], v[152:155], a[128:131]\n"
    "v_exp_f32 v124, v124\n"
    "v_mfma_f32_16x16x32_bf16 a[132:135], a[236:239], v[156:159], a[132:135]\n"
    "ds_read_b64_tr_b16 a[234:235], v242 offset:4096\n"
    "v_mfma_f32_16x16x32_bf16 a[136:139], a[236:239], v[160:163], a[136:139]\n"
    "v_exp_f32 v125, v125\n"
    "s_nop 3\n"
    "v_mfma_f32_16x16x32_bf16 a[140:143], a[236:239], v[164:167], a[140:143]\n"
    "buffer_load_dwordx4 v247, s[52:55], s69 offen offset:1024 lds\n"
    "v_mfma_f32_16x16x32_bf16 a[144:147], a[240:243], v[144:147], a[144:147]\n"
    "v_exp_f32 v126, v126\n"
    "v_mfma_f32_16x16x32_bf16 a[148:151], a[240:243], v[148:151], a[148:151]\n"
    "ds_read_b64_tr_b16 a[236:237], v243 offset:0\n"
    "v_cvt_pk_bf16_f32 v174, v124, v125\n"
    "v_mfma_f32_16x16x32_bf16 a[152:155], a[240:243], v[152:155], a[152:155]\n"
    "v_exp_f32 v127, v127\n"
    "s_nop 3\n"
    "v_mfma_f32_16x16x32_bf16 a[156:159], a[240:243], v[156:159], a[156:159]\n"
    "ds_read_b64_tr_b16 a[238:239], v243 offset:4096\n"
    "v_cvt_pk_bf16_f32 v175, v126, v127\n"
    "v_mfma_f32_16x16x32_bf16 a[160:163], a[240:243], v[160:163], a[160:163]\n"
    "v_exp_f32 v128, v128\n"
    "v_mfma_f32_16x16x32_bf16 a[164:167], a[240:243], v[164:167], a[164:167]\n"
    "buffer_load_dwordx4 v246, s[52:55], s69 offen offset:2048 lds\n"
    "v_mfma_f32_16x16x32_bf16 a[168:171], a[244:247], v[144:147], a[168:171]\n"
    "v_exp_f32 v129, v129\n"
    "s_nop 3\n"
    "v_mfma_f32_16x16x32_bf16 a[172:175], a[244:247], v[148:151], a[172:175]\n"
    "ds_read_b64_tr_b16 a[240:241], v242 offset:512\n"
    "v_cvt_pk_bf16_f32 v178, v128, v129\n"
    "v_mfma_f32_16x16x32_bf16 a[176:179], a[244:247], v[152:155], a[176:179]\n"
    "v_exp_f32 v130, v130\n"
    "v_mfma_f32_16x16x32_bf16 a[180:183], a[244:247], v[156:159], a[180:183]\n"
    "ds_read_b64_tr_b16 a[242:243], v242 offset:4608\n"
    "s_nop 3\n"
    "v_mfma_f32_16x16x32_bf16 a[184:187], a[244:247], v[160:163], a[184:187]\n"
    "v_exp_f32 v131, v131\n"
    "v_mfma_f32_16x16x32_bf16 a[188:191], a[244:247], v[164:167], a[188:191]\n"
    "buffer_load_dwordx4 v247, s[52:55], s69 offen offset:3072 lds\n"
    "v_mfma_f32_16x16x32_bf16 a[192:195], a[248:251], v[144:147], a[192:195]\n"
    "s_cmp_lg_u32 s74, 2\n"
    "s_cbranch_scc1 LW%=\n"
    "buffer_load_dword v237, v233, s[20:23], 0 offen\n"
    "buffer_load_dword v237, v233, s[20:23], 0 offen offset:128\n"
    "buffer_load_dword v237, v233, s[20:23], 0 offen offset:256\n"
    "s_getpc_b64 s[82:83]\n"
    "LGP%=:\n"
    "s_add_u32 s82, s82, LQ%=-LGP%=\n"
    "s_addc_u32 s83, s83, 0\n"
    "global_load_dword v237, v239, s[82:83]\n"
    "LW%=:\n"
    "v_exp_f32 v132, v132\n"
    "v_mfma_f32_16x16x32_bf16 a[196:199], a[248:251], v[148:151], a[196:199]\n"
    "ds_read_b64_tr_b16 a[244:245], v243 offset:512\n"
    "v_cvt_pk_bf16_f32 v179, v130, v131\n"
    "s_nop 3\n"
    "v_mfma_f32_16x16x32_bf16 a[200:203], a[248:251], v[152:155], a[200:203]\n"
    "s_sub_u32 s74, s74, 1\n"
    "v_exp_f32 v133, v133\n"
    "v_mfma_f32_16x16x32_bf16 a[204:207], a[248:251], v[156:159], a[204:207]\n"
    "ds_read_b64_tr_b16 a[246:247], v243 offset:4608\n"
    "v_cvt_pk_bf16_f32 v182, v132, v133\n"
    "v_mfma_f32_16x16x32_bf16 a[208:211], a[248:251], v[160:163], a[208:211]\n"
    "s_waitcnt lgkmcnt(8)\n"
    "v_exp_f32 v134, v134\n"
    "v_mfma_f32_16x16x32_bf16 a[212:215], a[248:251], v[164:167], a[212:215]\n"
    "s_cmp_lg_u32 s74, 0\n"
    "s_cbranch_scc1 LC%=\n"
    "s_branch LQ%=\n"
    "LH%=:\n"
    "v_mfma_f32_16x16x32_bf16 v[96:99], a[216:219], v[0:3], v[192:195]\n"
    "s_mov_b32 s69, s68\n"
    "v_exp_f32 v135, v135\n"
    "s_nop 3\n"
    "v_mfma_f32_16x16x32_bf16 v[100:103], a[216:219], v[16:19], v[196:199]\n"
    "v_cvt_pk_bf16_f32 v183, v134, v135\n"
    "s_nop 3\n"
    "v_mfma_f32_16x16x32_bf16 v[104:107], a[216:219], v[32:35], v[200:203]\n"
    "s_add_u32 s68, s68, 0x2000\n"
    "v_exp_f32 v136, v136\n"
    "s_nop 3\n"
    "v_mfma_f32_16x16x32_bf16 v[108:111], a[216:219], v[48:51], v[204:207]\n"
    "s_nop 3\n"
    "v_mfma_f32_16x16x32_bf16 v[112:115], a[216:219], v[64:67], v[208:211]\n"
    "s_add_u32 s71, s70, 0x14000\n"
    "v_exp_f32 v137, v137\n"
    "v_mfma_f32_16x16x32_bf16 v[116:119], a[216:219], v[80:83], v[212:215]\n"
    "v_cvt_pk_bf16_f32 v186, v136, v137\n"
    "s_nop 3\n"
    "v_mfma_f32_16x16x32_bf16 v[96:99], a[220:223], v[4:7], v[96:99]\n"
    "s_add_u32 s70, s70, s84\n"
    "v_exp_f32 v138, v138\n"
    "s_nop 3\n"
    "v_mfma_f32_16x16x32_bf16 v[100:103], a[220:223], v[20:23], v[100:103]\n"
    "ds_read_b128 a[216:219], v240 offset:4096\n"
    "s_nop 3\n"
    "v_mfma_f32_16x16x32_bf16 v[104:107], a[220:223], v[36:39], v[104:107]\n"
    "s_and_b32 s68, s68, 0x3ffff\n"
    "v_exp_f32 v139, v139\n"
    "v_mfma_f32_16x16x32_bf16 v[108:111], a[220:223], v[52:55], v[108:111]\n"
    "v_cvt_pk_bf16_f32 v187, v138, v139\n"
    "s_nop 3\n"
    "v_mfma_f32_16x16x32_bf16 v[112:115], a[220:223], v[68:71], v[112:115]\n"
    "s_mov_b32 s54, s50\n"
    "v_exp_f32 v140, v140\n"
    "s_nop 3\n"
    "v_mfma_f32_16x16x32_bf16 v[116:119], a[220:223], v[84:87], v[116:119]\n"
    "s_nop 3\n"
    "v_mfma_f32_16x16x32_bf16 v[96:99], a[224:227], v[8:11], v[96:99]\n"
    "s_cmp_le_i32 s63, 0\n"
    "v_exp_f32 v141, v141\n"
    "v_mfma_f32_16x16x32_bf16 v[100:103], a[224:227], v[24:27], v[100:103]\n"
    "ds_read_b128 a[220:223], v241 offset:4096\n"
    "v_cvt_pk_bf16_f32 v190, v140, v141\n"
    "s_nop 3\n"
    "v_mfma_f32_16x16x32_bf16 v[104:107], a[224:227], v[40:43], v[104:107]\n"
    "s_cselect_b32 s50, 0, s50\n"
    "v_exp_f32 v142, v142\n"
    "s_nop 3\n"
    "v_mfma_f32_16x16x32_bf16 v[108:111], a[224:227], v[56:59], v[108:111]\n"
    "s_nop 3\n"
    "v_mfma_f32_16x16x32_bf16 v[112:115], a[224:227], v[72:75], v[112:115]\n"
    "s_sub_u32 s63, s63, 1\n"
    "v_exp_f32 v143, v143\n"
    "v_mfma_f32_16x16x32_bf16 v[116:119], a[224:227], v[88:91], v[116:119]\n"
    "v_cvt_pk_bf16_f32 v191, v142, v143\n"
    "s_nop 3\n"
    "v_mfma_f32_16x16x32_bf16 v[96:99], a[228:231], v[12:15], v[96:99]\n"
    "s_nop 3\n"
    "v_mfma_f32_16x16x32_bf16 v[100:103], a[228:231], v[28:31], v[100:103]\n"
    "ds_read_b128 a[224:227], v240 offset:5120\n"
    "s_nop 3\n"
    "v_mfma_f32_16x16x32_bf16 v[104:107], a[228:231], v[44:47], v[104:107]\n"
    "v_mfma_f32_16x16x32_bf16 v[108:111], a[228:231], v[60:63], v[108:111]\n"
    "s_nop 3\n"
    "v_mfma_f32_16x16x32_bf16 v[112:115], a[228:231], v[76:79], v[112:115]\n"
    "s_waitcnt lgkmcnt(3)\n"
    "s_nop 3\n"
    "v_mfma_f32_16x16x32_bf16 v[116:119], a[228:231], v[92:95], v[116:119]\n"
    "s_nop 3\n"
    "v_mfma_f32_16x16x32_bf16 a[0:3], a[232:235], v[168:171], a[0:3]\n"
    "v_exp_f32 v96, v96\n"
    "v_mfma_f32_16x16x32_bf16 a[4:7], a[232:235], v[172:175], a[4:7]\n"
    "ds_read_b128 a[228:231], v241 offset:5120\n"
    "s_nop 3\n"
    "v_mfma_f32_16x16x32_bf16 a[8:11], a[232:235], v[176:179], a[8:11]\n"
    "v_exp_f32 v97, v97\n"
    "s_nop 3\n"
    "v_mfma_f32_16x16x32_bf16 a[12:15], a[232:235], v[180:183], a[12:15]\n"
    "v_exp_f32 v98, v98\n"
    "s_nop 3\n"
    "v_mfma_f32_16x16x32_bf16 a[16:19], a[232:235], v[184:187], a[16:19]\n"
    "v_exp_f32 v99, v99\n"
    "v_mfma_f32_16x16x32_bf16 a[20:23], a[232:235], v[188:191], a[20:23]\n"
    "v_cvt_pk_bf16_f32 v144, v96, v97\n"
    "v_cvt_pk_bf16_f32 v145, v98, v99\n"
    "s_nop 3\n"
    "v_mfma_f32_16x16x32_bf16 a[24:27], a[236:239], v[168:171], a[24:27]\n"
    "v_exp_f32 v100, v100\n"
    "s_nop 3\n"
    "v_mfma_f32_16x16x32_bf16 a[28:31], a[236:239], v[172:175], a[28:31]\n"
    "ds_read_b64_tr_b16 a[232:233], v242 offset:1024\n"
    "s_nop 3\n"
    "v_mfma_f32_16x16x32_bf16 a[32:35], a[236:239], v[176:179], a[32:35]\n"
    "v_exp_f32 v101, v101\n"
    "v_mfma_f32_16x16x32_bf16 a[36:39], a[236:239], v[180:183], a[36:39]\n"
    "ds_read_b64_tr_b16 a[234:235], v242 offset:5120\n"
    "v_cvt_pk_bf16_f32 v148, v100, v101\n"
    "s_nop 3\n"
    "v_mfma_f32_16x16x32_bf16 a[40:43], a[236:239], v[184:187], a[40:43]\n"
    "v_exp_f32 v102, v102\n"
    "s_nop 3\n"
    "v_mfma_f32_16x16x32_bf16 a[44:47], a[236:239], v[188:191], a[44:47]\n"
    "s_nop 3\n"
    "v_mfma_f32_16x16x32_bf16 a[48:51], a[240:243], v[168:171], a[48:51]\n"
    "v_exp_f32 v103, v103\n"
    "v_mfma_f32_16x16x32_bf16 a[52:55], a[240:243], v[172:175], a[52:55]\n"
    "ds_read_b64_tr_b16 a[236:237], v243 offset:1024\n"
    "v_cvt_pk_bf16_f32 v149, v102, v103\n"
    "s_nop 3\n"
    "v_mfma_f32_16x16x32_bf16 a[56:59], a[240:243], v[176:179], a[56:59]\n"
    "v_exp_f32 v104, v104\n"
    "s_nop 3\n"
    "v_mfma_f32_16x16x32_bf16 a[60:63], a[240:243], v[180:183], a[60:63]\n"
    "ds_read_b64_tr_b16 a[238:239], v243 offset:5120\n"
    "s_nop 3\n"
    "v_mfma_f32_16x16x32_bf16 a[64:67], a[240:243], v[184:187], a[64:67]\n"
    "v_exp_f32 v105, v105\n"
    "v_mfma_f32_16x16x32_bf16 a[68:71], a[240:243], v[188:191], a[68:71]\n"
    "v_cvt_pk_bf16_f32 v152, v104, v105\n"
    "s_nop 3\n"
    "v_mfma_f32_16x16x32_bf16 a[72:75], a[244:247], v[168:171], a[72:75]\n"
    "v_exp_f32 v106, v106\n"
    "s_nop 3\n"
    "v_mfma_f32_16x16x32_bf16 a[76:79], a[244:247], v[172:175], a[76:79]\n"
    "ds_read_b64_tr_b16 a[240:241], v242 offset:1536\n"
    "s_nop 3\n"
    "v_mfma_f32_16x16x32_bf16 a[80:83], a[244:247], v[176:179], a[80:83]\n"
    "v_exp_f32 v107, v107\n"
    "v_mfma_f32_16x16x32_bf16 a[84:87], a[244:247], v[180:183], a[84:87]\n"
    "ds_read_b64_tr_b16 a[242:243], v242 offset:5632\n"
    "v_cvt_pk_bf16_f32 v153, v106, v107\n"
    "s_nop 3\n"
    "v_mfma_f32_16x16x32_bf16 a[88:91], a[244:247], v[184:187], a[88:91]\n"
    "s_waitcnt lgkmcnt(6)\n"
    "v_exp_f32 v108, v108\n"
    "s_nop 3\n"
    "v_mfma_f32_16x16x32_bf16 a[92:95], a[244:247], v[188:191], a[92:95]\n"
    "s_nop 3\n"
    "v_mfma_f32_16x16x32_bf16 v[120:123], a[216:219], v[0:3], v[192:195]\n"
    "s_cmp_ge_u32 s70, s76\n"
    "v_exp_f32 v109, v109\n"
    "v_mfma_f32_16x16x32_bf16 v[124:127], a[216:219], v[16:19], v[196:199]\n"
    "ds_read_b64_tr_b16 a[244:245], v243 offset:1536\n"
    "v_cvt_pk_bf16_f32 v156, v108, v109\n"
    "s_nop 3\n"
    "v_mfma_f32_16x16x32_bf16 v[128:131], a[216:219], v[32:35], v[200:203]\n"
    "s_cselect_b32 s84, s75, 0x4000\n"
    "v_exp_f32 v110, v110\n"
    "s_nop 3\n"
    "v_mfma_f32_16x16x32_bf16 v[132:135], a[216:219], v[48:51], v[204:207]\n"
    "ds_read_b64_tr_b16 a[246:247], v243 offset:5632\n"
    "s_nop 3\n"
    "v_mfma_f32_16x16x32_bf16 v[136:139], a[216:219], v[64:67], v[208:211]\n"
    "s_mov_b32 s73, s72\n"
    "v_exp_f32 v111, v111\n"
    "v_mfma_f32_16x16x32_bf16 v[140:143], a[216:219], v[80:83], v[212:215]\n"
    "v_cvt_pk_bf16_f32 v157, v110, v111\n"
    "s_nop 3\n"
    "v_mfma_f32_16x16x32_bf16 v[120:123], a[220:223], v[4:7], v[120:123]\n"
    "s_cmp_eq_u32 s70, s64\n"
    "v_exp_f32 v112, v112\n"
    "s_nop 3\n"
    "v_mfma_f32_16x16x32_bf16 v[124:127], a[220:223], v[20:23], v[124:127]\n"
    "v_add_u32 v242, s73, v242\n"
    "v_add_u32 v243, s73, v243\n"
    "s_nop 3\n"
    "v_mfma_f32_16x16x32_bf16 v[128:131], a[220:223], v[36:39], v[128:131]\n"
    "s_cselect_b32 s72, s75, 0x4000\n"
    "v_exp_f32 v113, v113\n"
    "v_mfma_f32_16x16x32_bf16 v[132:135], a[220:223], v[52:55], v[132:135]\n"
    "v_cvt_pk_bf16_f32 v160, v112, v113\n"
    "s_nop 3\n"
    "v_mfma_f32_16x16x32_bf16 v[136:139], a[220:223], v[68:71], v[136:139]\n"
    "v_exp_f32 v114, v114\n"
    "s_nop 3\n"
    "v_mfma_f32_16x16x32_bf16 v[140:143], a[220:223], v[84:87], v[140:143]\n"
    "v_add_u32 v240, s72, v240\n"
    "v_add_u32 v241, s72, v241\n"
    "s_nop 3\n"
    "v_mfma_f32_16x16x32_bf16 v[120:123], a[224:227], v[8:11], v[120:123]\n"
    "s_waitcnt vmcnt(16) lgkmcnt(0)\n"
    "s_barrier\n"
    "s_mov_b32 m0, s70\n"
    "v_mfma_f32_16x16x32_bf16 v[124:127], a[224:227], v[24:27], v[124:127]\n"
    "buffer_load_dwordx4 v244, s[48:51], s68 offen nt lds\n"
    "s_nop 3\n"
    "v_mfma_f32_16x16x32_bf16 v[128:131], a[224:227], v[40:43], v[128:131]\n"
    "v_exp_f32 v115, v115\n"
    "s_nop 3\n"
    "v_mfma_f32_16x16x32_bf16 v[132:135], a[224:227], v[56:59], v[132:135]\n"
    "ds_read_b128 a[216:219], v240 offset:0\n"
    "v_cvt_pk_bf16_f32 v161, v114, v115\n"
    "s_nop 3\n"
    "v_mfma_f32_16x16x32_bf16 v[136:139], a[224:227], v[72:75], v[136:139]\n"
    "v_exp_f32 v116, v116\n"
    "v_mfma_f32_16x16x32_bf16 v[140:143], a[224:227], v[88:91], v[140:143]\n"
    "buffer_load_dwordx4 v245, s[48:51], s68 offen offset:1024 nt lds\n"
    "s_nop 3\n"
    "v_mfma_f32_16x16x32_bf16 v[120:123], a[228:231], v[12:15], v[120:123]\n"
    "v_exp_f32 v117, v117\n"
    "s_nop 3\n"
    "v_mfma_f32_16x16x32_bf16 v[124:127], a[228:231], v[28:31], v[124:127]\n"
    "ds_read_b128 a[220:223], v241 offset:0\n"
    "v_cvt_pk_bf16_f32 v164, v116, v117\n"
    "s_nop 3\n"
    "v_mfma_f32_16x16x32_bf16 v[128:131], a[228:231], v[44:47], v[128:131]\n"
    "v_exp_f32 v118, v118\n"
    "v_mfma_f32_16x16x32_bf16 v[132:135], a[228:231], v[60:63], v[132:135]\n"
    "buffer_load_dwordx4 v244, s[48:51], s68 offen offset:2048 nt lds\n"
    "s_nop 3\n"
    "v_mfma_f32_16x16x32_bf16 v[136:139], a[228:231], v[76:79], v[136:139]\n"
    "v_exp_f32 v119, v119\n"
    "s_nop 3\n"
    "v_mfma_f32_16x16x32_bf16 v[140:143], a[228:231], v[92:95], v[140:143]\n"
    "ds_read_b128 a[224:227], v240 offset:1024\n"
    "v_cvt_pk_bf16_f32 v165, v118, v119\n"
    "s_nop 3\n"
    "v_mfma_f32_16x16x32_bf16 a[96:99], a[232:235], v[168:171], a[96:99]\n"
    "v_exp_f32 v120, v120\n"
    "v_mfma_f32_16x16x32_bf16 a[100:103], a[232:235], v[172:175], a[100:103]\n"
    "buffer_load_dwordx4 v245, s[48:51], s68 offen offset:3072 nt lds\n"
    "s_nop 3\n"
    "v_mfma_f32_16x16x32_bf16 a[104:107], a[232:235], v[176:179], a[104:107]\n"
    "s_mov_b32 m0, s71\n"
    "v_exp_f32 v121, v121\n"
    "s_nop 3\n"
    "v_mfma_f32_16x16x32_bf16 a[108:111], a[232:235], v[180:183], a[108:111]\n"
    "ds_read_b128 a[228:231], v241 offset:1024\n"
    "v_cvt_pk_bf16_f32 v146, v120, v121\n"
    "s_nop 3\n"
    "v_mfma_f32_16x16x32_bf16 a[112:115], a[232:235], v[184:187], a[112:115]\n"
    "v_exp_f32 v122, v122\n"
    "v_mfma_f32_16x16x32_bf16 a[116:119], a[232:235], v[188:191], a[116:119]\n"
    "buffer_load_dwordx4 v246, s[52:55], s69 offen lds\n"
    "s_nop 3\n"
    "v_mfma_f32_16x16x32_bf16 a[120:123], a[236:239], v[168:171], a[120:123]\n"
    "v_exp_f32 v123, v123\n"
    "s_nop 3\n"
    "v_mfma_f32_16x16x32_bf16 a[124:127], a[236:239], v[172:175], a[124:127]\n"
    "ds_read_b64_tr_b16 a[232:233], v242 offset:0\n"
    "v_cvt_pk_bf16_f32 v147, v122, v123\n"
    "s_nop 3\n"
    "v_mfma_f32_16x16x32_bf16 a[128:131], a[236:239], v[176:179], a[128:131]\n"
    "v_exp_f32 v124, v124\n"
    "v_mfma_f32_16x16x32_bf16 a[132:135], a[236:239], v[180:183], a[132:135]\n"
    "ds_read_b64_tr_b16 a[234:235], v242 offset:4096\n"
    "s_nop 3\n"
    "v_mfma_f32_16x16x32_bf16 a[136:139], a[236:239], v[184:187], a[136:139]\n"
    "v_exp_f32 v125, v125\n"
    "s_nop 3\n"
    "v_mfma_f32_16x16x32_bf16 a[140:143], a[236:239], v[188:191], a[140:143]\n"
    "buffer_load_dwordx4 v247, s[52:55], s69 offen offset:1024 lds\n"
    "s_nop 3\n"
    "v_mfma_f32_16x16x32_bf16 a[144:147], a[240:243], v[168:171], a[144:147]\n"
    "v_exp_f32 v126, v126\n"
    "v_mfma_f32_16x16x32_bf16 a[148:151], a[240:243], v[172:175], a[148:151]\n"
    "ds_read_b64_tr_b16 a[236:237], v243 offset:0\n"
    "v_cvt_pk_bf16_f32 v150, v124, v125\n"
    "s_nop 3\n"
    "v_mfma_f32_16x16x32_bf16 a[152:155], a[240:243], v[176:179], a[152:155]\n"
    "v_exp_f32 v127, v127\n"
    "s_nop 3\n"
    "v_mfma_f32_16x16x32_bf16 a[156:159], a[240:243], v[180:183], a[156:159]\n"
    "ds_read_b64_tr_b16 a[238:239], v243 offset:4096\n"
    "v_cvt_pk_bf16_f32 v151, v126, v127\n"
    "s_nop 3\n"
    "v_mfma_f32_16x16x32_bf16 a[160:163], a[240:243], v[184:187], a[160:163]\n"
    "v_exp_f32 v128, v128\n"
    "v_mfma_f32_16x16x32_bf16 a[164:167], a[240:243], v[188:191], a[164:167]\n"
    "buffer_load_dwordx4 v246, s[52:55], s69 offen offset:2048 lds\n"
    "s_nop 3\n"
    "v_mfma_f32_16x16x32_bf16 a[168:171], a[244:247], v[168:171], a[168:171]\n"
    "v_exp_f32 v129, v129\n"
    "s_nop 3\n"
    "v_mfma_f32_16x16x32_bf16 a[172:175], a[244:247], v[172:175], a[172:175]\n"
    "ds_read_b64_tr_b16 a[240:241], v242 offset:512\n"
    "v_cvt_pk_bf16_f32 v154, v128, v129\n"
    "s_nop 3\n"
    "v_mfma_f32_16x16x32_bf16 a[176:179], a[244:247], v[176:179], a[176:179]\n"
    "v_exp_f32 v130, v130\n"
    "v_mfma_f32_16x16x32_bf16 a[180:183], a[244:247], v[180:183], a[180:183]\n"
    "ds_read_b64_tr_b16 a[242:243], v242 offset:4608\n"
    "s_nop 3\n"
    "v_mfma_f32_16x16x32_bf16 a[184:187], a[244:247], v[184:187], a[184:187]\n"
    "v_exp_f32 v131, v131\n"
    "s_nop 3\n"
    "v_mfma_f32_16x16x32_bf16 a[188:191], a[244:247], v[188:191], a[188:191]\n"
    "buffer_load_dwordx4 v247, s[52:55], s69 offen offset:3072 lds\n"
    "s_nop 3\n"
    "v_mfma_f32_16x16x32_bf16 a[192:195], a[248:251], v[168:171], a[192:195]\n"
    "v_exp_f32 v132, v132\n"
    "v_mfma_f32_16x16x32_bf16 a[196:199], a[248:251], v[172:175], a[196:199]\n"
    "ds_read_b64_tr_b16 a[244:245], v243 offset:512\n"
    "v_cvt_pk_bf16_f32 v155, v130, v131\n"
    "s_nop 3\n"
    "v_mfma_f32_16x16x32_bf16 a[200:203], a[248:251], v[176:179], a[200:203]\n"
    "v_exp_f32 v133, v133\n"
    "s_nop 3\n"
    "v_mfma_f32_16x16x32_bf16 a[204:207], a[248:251], v[180:183], a[204:207]\n"
    "ds_read_b64_tr_b16 a[246:247], v243 offset:4608\n"
    "v_cvt_pk_bf16_f32 v158, v132, v133\n"
    "s_nop 3\n"
    "v_mfma_f32_16x16x32_bf16 a[208:211], a[248:251], v[184:187], a[208:211]\n"
    "s_waitcnt lgkmcnt(8)\n"
    "v_exp_f32 v134, v134\n"
    "v_mfma_f32_16x16x32_bf16 a[212:215], a[248:251], v[188:191], a[212:215]\n"
    "v_mfma_f32_16x16x32_bf16 v[96:99], a[216:219], v[0:3], v[192:195]\n"
    "s_mov_b32 s69, s68\n"
    "v_exp_f32 v135, v135\n"
    "s_nop 3\n"
    "v_mfma_f32_16x16x32_bf16 v[100:103], a[216:219], v[16:19], v[196:199]\n"
    "v_cvt_pk_bf16_f32 v159, v134, v135\n"
    "s_nop 3\n"
    "v_mfma_f32_16x16x32_bf16 v[104:107], a[216:219], v[32:35], v[200:203]\n"
    "s_add_u32 s68, s68, 0x2000\n"
    "v_exp_f32 v136, v136\n"
    "s_nop 3\n"
    "v_mfma_f32_16x16x32_bf16 v[108:111], a[216:219], v[48:51], v[204:207]\n"
    "s_nop 3\n"
    "v_mfma_f32_16x16x32_bf16 v[112:115], a[216:219], v[64:67], v[208:211]\n"
    "s_add_u32 s71, s70, 0x14000\n"
    "v_exp_f32 v137, v137\n"
    "v_mfma_f32_16x16x32_bf16 v[116:119], a[216:219], v[80:83], v[212:215]\n"
    "v_cvt_pk_bf16_f32 v162, v136, v137\n"
    "s_nop 3\n"
    "v_mfma_f32_16x16x32_bf16 v[96:99], a[220:223], v[4:7], v[96:99]\n"
    "s_add_u32 s70, s70, s84\n"
    "v_exp_f32 v138, v138\n"
    "s_nop 3\n"
    "v_mfma_f32_16x16x32_bf16 v[100:103], a[220:223], v[20:23], v[100:103]\n"
    "ds_read_b128 a[216:219], v240 offset:4096\n"
    "s_nop 3\n"
    "v_mfma_f32_16x16x32_bf16 v[104:107], a[220:223], v[36:39], v[104:107]\n"
    "s_and_b32 s68, s68, 0x3ffff\n"
    "v_exp_f32 v139, v139\n"
    "v_mfma_f32_16x16x32_bf16 v[108:111], a[220:223], v[52:55], v[108:111]\n"
    "v_cvt_pk_bf16_f32 v163, v138, v139\n"
    "s_nop 3\n"
    "v_mfma_f32_16x16x32_bf16 v[112:115], a[220:223], v[68:71], v[112:115]\n"
    "s_mov_b32 s54, s50\n"
    "v_exp_f32 v140, v140\n"
    "s_nop 3\n"
    "v_mfma_f32_16x16x32_bf16 v[116:119], a[220:223], v[84:87], v[116:119]\n"
    "s_nop 3\n"
    "v_mfma_f32_16x16x32_bf16 v[96:99], a[224:227], v[8:11], v[96:99]\n"
    "s_cmp_le_i32 s63, 0\n"
    "v_exp_f32 v141, v141\n"
    "v_mfma_f32_16x16x32_bf16 v[100:103], a[224:227], v[24:27], v[100:103]\n"
    "ds_read_b128 a[220:223], v241 offset:4096\n"
    "v_cvt_pk_bf16_f32 v166, v140, v141\n"
    "s_nop 3\n"
    "v_mfma_f32_16x16x32_bf16 v[104:107], a[224:227], v[40:43], v[104:107]\n"
    "s_cselect_b32 s50, 0, s50\n"
    "v_exp_f32 v142, v142\n"
    "s_nop 3\n"
    "v_mfma_f32_16x16x32_bf16 v[108:111], a[224:227], v[56:59], v[108:111]\n"
    "s_nop 3\n"
    "v_mfma_f32_16x16x32_bf16 v[112:115], a[224:227], v[72:75], v[112:115]\n"
    "s_sub_u32 s63, s63, 1\n"
    "v_exp_f32 v143, v143\n"
    "v_mfma_f32_16x16x32_bf16 v[116:119], a[224:227], v[88:91], v[116:119]\n"
    "v_cvt_pk_bf16_f32 v167, v142, v143\n"
    "s_nop 3\n"
    "v_mfma_f32_16x16x32_bf16 v[96:99], a[228:231], v[12:15], v[96:99]\n"
    "s_nop 3\n"
    "v_mfma_f32_16x16x32_bf16 v[100:103], a[228:231], v[28:31], v[100:103]\n"
    "ds_read_b128 a[224:227], v240 offset:5120\n"
    "s_nop 3\n"
    "v_mfma_f32_16x16x32_bf16 v[104:107], a[228:231], v[44:47], v[104:107]\n"
    "v_mfma_f32_16x16x32_bf16 v[108:111], a[228:231], v[60:63], v[108:111]\n"
    "s_nop 3\n"
    "v_mfma_f32_16x16x32_bf16 v[112:115], a[228:231], v[76:79], v[112:115]\n"
    "s_waitcnt lgkmcnt(3)\n"
    "s_nop 3\n"
    "v_mfma_f32_16x16x32_bf16 v[116:119], a[228:231], v[92:95], v[116:119]\n"
    "s_nop 3\n"
    "v_mfma_f32_16x16x32_bf16 a[0:3], a[232:235], v[144:147], a[0:3]\n"
    "v_exp_f32 v96, v96\n"
    "v_mfma_f32_16x16x32_bf16 a[4:7], a[232:235], v[148:151], a[4:7]\n"
    "ds_read_b128 a[228:231], v241 offset:5120\n"
    "s_nop 3\n"
    "v_mfma_f32_16x16x32_bf16 a[8:11], a[232:235], v[152:155], a[8:11]\n"
    "v_exp_f32 v97, v97\n"
    "s_nop 3\n"
    "v_mfma_f32_16x16x32_bf16 a[12:15], a[232:235], v[156:159], a[12:15]\n"
    "v_exp_f32 v98, v98\n"
    "s_nop 3\n"
    "v_mfma_f32_16x16x32_bf16 a[16:19], a[232:235], v[160:163], a[16:19]\n"
    "v_exp_f32 v99, v99\n"
    "v_mfma_f32_16x16x32_bf16 a[20:23], a[232:235], v[164:167], a[20:23]\n"
    "v_cvt_pk_bf16_f32 v168, v96, v97\n"
    "v_cvt_pk_bf16_f32 v169, v98, v99\n"
    "s_nop 3\n"
    "v_mfma_f32_16x16x32_bf16 a[24:27], a[236:239], v[144:147], a[24:27]\n"
    "v_exp_f32 v100, v100\n"
    "s_nop 3\n"
    "v_mfma_f32_16x16x32_bf16 a[28:31], a[236:239], v[148:151], a[28:31]\n"
    "ds_read_b64_tr_b16 a[232:233], v242 offset:1024\n"
    "s_nop 3\n"
    "v_mfma_f32_16x16x32_bf16 a[32:35], a[236:239], v[152:155], a[32:35]\n"
    "v_exp_f32 v101, v101\n"
    "v_mfma_f32_16x16x32_bf16 a[36:39], a[236:239], v[156:159], a[36:39]\n"
    "ds_read_b64_tr_b16 a[234:235], v242 offset:5120\n"
    "v_cvt_pk_bf16_f32 v172, v100, v101\n"
    "s_nop 3\n"
    "v_mfma_f32_16x16x32_bf16 a[40:43], a[236:239], v[160:163], a[40:43]\n"
    "v_exp_f32 v102, v102\n"
    "s_nop 3\n"
    "v_mfma_f32_16x16x32_bf16 a[44:47], a[236:239], v[164:167], a[44:47]\n"
    "s_nop 3\n"
    "v_mfma_f32_16x16x32_bf16 a[48:51], a[240:243], v[144:147], a[48:51]\n"
    "v_exp_f32 v103, v103\n"
    "v_mfma_f32_16x16x32_bf16 a[52:55], a[240:243], v[148:151], a[52:55]\n"
    "ds_read_b64_tr_b16 a[236:237], v243 offset:1024\n"
    "v_cvt_pk_bf16_f32 v173, v102, v103\n"
    "s_nop 3\n"
    "v_mfma_f32_16x16x32_bf16 a[56:59], a[240:243], v[152:155], a[56:59]\n"
    "v_exp_f32 v104, v104\n"
    "s_nop 3\n"
    "v_mfma_f32_16x16x32_bf16 a[60:63], a[240:243], v[156:159], a[60:63]\n"
    "ds_read_b64_tr_b16 a[238:239], v243 offset:5120\n"
    "s_nop 3\n"
    "v_mfma_f32_16x16x32_bf16 a[64:67], a[240:243], v[160:163], a[64:67]\n"
    "v_exp_f32 v105, v105\n"
    "v_mfma_f32_16x16x32_bf16 a[68:71], a[240:243], v[164:167], a[68:71]\n"
    "v_cvt_pk_bf16_f32 v176, v104, v105\n"
    "s_nop 3\n"
    "v_mfma_f32_16x16x32_bf16 a[72:75], a[244:247], v[144:147], a[72:75]\n"
    "v_exp_f32 v106, v106\n"
    "s_nop 3\n"
    "v_mfma_f32_16x16x32_bf16 a[76:79], a[244:247], v[148:151], a[76:79]\n"
    "ds_read_b64_tr_b16 a[240:241], v242 offset:1536\n"
    "s_nop 3\n"
    "v_mfma_f32_16x16x32_bf16 a[80:83], a[244:247], v[152:155], a[80:83]\n"
    "v_exp_f32 v107, v107\n"
    "v_mfma_f32_16x16x32_bf16 a[84:87], a[244:247], v[156:159], a[84:87]\n"
    "ds_read_b64_tr_b16 a[242:243], v242 offset:5632\n"
    "v_cvt_pk_bf16_f32 v177, v106, v107\n"
    "s_nop 3\n"
    "v_mfma_f32_16x16x32_bf16 a[88:91], a[244:247], v[160:163], a[88:91]\n"
    "s_waitcnt lgkmcnt(6)\n"
    "v_exp_f32 v108, v108\n"
    "s_nop 3\n"
    "v_mfma_f32_16x16x32_bf16 a[92:95], a[244:247], v[164:167], a[92:95]\n"
    "s_nop 3\n"
    "v_mfma_f32_16x16x32_bf16 v[120:123], a[216:219], v[0:3], v[192:195]\n"
    "s_cmp_ge_u32 s70, s76\n"
    "v_exp_f32 v109, v109\n"
    "v_mfma_f32_16x16x32_bf16 v[124:127], a[216:219], v[16:19], v[196:199]\n"
    "ds_read_b64_tr_b16 a[244:245], v243 offset:1536\n"
    "v_cvt_pk_bf16_f32 v180, v108, v109\n"
    "s_nop 3\n"
    "v_mfma_f32_16x16x32_bf16 v[128:131], a[216:219], v[32:35], v[200:203]\n"
    "s_cselect_b32 s84, s75, 0x4000\n"
    "v_exp_f32 v110, v110\n"
    "s_nop 3\n"
    "v_mfma_f32_16x16x32_bf16 v[132:135], a[216:219], v[48:51], v[204:207]\n"
    "ds_read_b64_tr_b16 a[246:247], v243 offset:5632\n"
    "s_nop 3\n"
    "v_mfma_f32_16x16x32_bf16 v[136:139], a[216:219], v[64:67], v[208:211]\n"
    "s_mov_b32 s73, s72\n"
    "v_exp_f32 v111, v111\n"
    "v_mfma_f32_16x16x32_bf16 v[140:143], a[216:219], v[80:83], v[212:215]\n"
    "v_cvt_pk_bf16_f32 v181, v110, v111\n"
    "s_nop 3\n"
    "v_mfma_f32_16x16x32_bf16 v[120:123], a[220:223], v[4:7], v[120:123]\n"
    "s_cmp_eq_u32 s70, s64\n"
    "v_exp_f32 v112, v112\n"
    "s_nop 3\n"
    "v_mfma_f32_16x16x32_bf16 v[124:127], a[220:223], v[20:23], v[124:127]\n"
    "v_add_u32 v242, s73, v242\n"
    "v_add_u32 v243, s73, v243\n"
    "s_nop 3\n"
    "v_mfma_f32_16x16x32_bf16 v[128:131], a[220:223], v[36:39], v[128:131]\n"
    "s_cselect_b32 s72, s75, 0x4000\n"
    "v_exp_f32 v113, v113\n"
    "v_mfma_f32_16x16x32_bf16 v[132:135], a[220:223], v[52:55], v[132:135]\n"
    "v_cvt_pk_bf16_f32 v184, v112, v113\n"
    "s_nop 3\n"
    "v_mfma_f32_16x16x32_bf16 v[136:139], a[220:223], v[68:71], v[136:139]\n"
    "v_exp_f32 v114, v114\n"
    "s_nop 3\n"
    "v_mfma_f32_16x16x32_bf16 v[140:143], a[220:223], v[84:87], v[140:143]\n"
    "v_add_u32 v240, s72, v240\n"
    "v_add_u32 v241, s72, v241\n"
    "s_nop 3\n"
    "v_mfma_f32_16x16x32_bf16 v[120:123], a[224:227], v[8:11], v[120:123]\n"
    "s_waitcnt vmcnt(16) lgkmcnt(0)\n"
    "s_barrier\n"
    "s_mov_b32 m0, s70\n"
    "v_mfma_f32_16x16x32_bf16 v[124:127], a[224:227], v[24:27], v[124:127]\n"
    "buffer_load_dwordx4 v244, s[48:51], s68 offen nt lds\n"
    "s_nop 3\n"
    "v_mfma_f32_16x16x32_bf16 v[128:131], a[224:227], v[40:43], v[128:131]\n"
    "v_exp_f32 v115, v115\n"
    "s_nop 3\n"
    "v_mfma_f32_16x16x32_bf16 v[132:135], a[224:227], v[56:59], v[132:135]\n"
    "ds_read_b128 a[216:219], v240 offset:0\n"
    "v_cvt_pk_bf16_f32 v185, v114, v115\n"
    "s_nop 3\n"
    "v_mfma_f32_16x16x32_bf16 v[136:139], a[224:227], v[72:75], v[136:139]\n"
    "v_exp_f32 v116, v116\n"
    "v_mfma_f32_16x16x32_bf16 v[140:143], a[224:227], v[88:91], v[140:143]\n"
    "buffer_load_dwordx4 v245, s[48:51], s68 offen offset:1024 nt lds\n"
    "s_nop 3\n"
    "v_mfma_f32_16x16x32_bf16 v[120:123], a[228:231], v[12:15], v[120:123]\n"
    "v_exp_f32 v117, v117\n"
    "s_nop 3\n"
    "v_mfma_f32_16x16x32_bf16 v[124:127], a[228:231], v[28:31], v[124:127]\n"
    "ds_read_b128 a[220:223], v241 offset:0\n"
    "v_cvt_pk_bf16_f32 v188, v116, v117\n"
    "s_nop 3\n"
    "v_mfma_f32_16x16x32_bf16 v[128:131], a[228:231], v[44:47], v[128:131]\n"
    "v_exp_f32 v118, v118\n"
    "v_mfma_f32_16x16x32_bf16 v[132:135], a[228:231], v[60:63], v[132:135]\n"
    "buffer_load_dwordx4 v244, s[48:51], s68 offen offset:2048 nt lds\n"
    "s_nop 3\n"
    "v_mfma_f32_16x16x32_bf16 v[136:139], a[228:231], v[76:79], v[136:139]\n"
    "v_exp_f32 v119, v119\n"
    "s_nop 3\n"
    "v_mfma_f32_16x16x32_bf16 v[140:143], a[228:231], v[92:95], v[140:143]\n"
    "ds_read_b128 a[224:227], v240 offset:1024\n"
    "v_cvt_pk_bf16_f32 v189, v118, v119\n"
    "s_nop 3\n"
    "v_mfma_f32_16x16x32_bf16 a[96:99], a[232:235], v[144:147], a[96:99]\n"
    "v_exp_f32 v120, v120\n"
    "v_mfma_f32_16x16x32_bf16 a[100:103], a[232:235], v[148:151], a[100:103]\n"
    "buffer_load_dwordx4 v245, s[48:51], s68 offen offset:3072 nt lds\n"
    "s_nop 3\n"
    "v_mfma_f32_16x16x32_bf16 a[104:107], a[232:235], v[152:155], a[104:107]\n"
    "s_mov_b32 m0, s71\n"
    "v_exp_f32 v121, v121\n"
    "s_nop 3\n"
    "v_mfma_f32_16x16x32_bf16 a[108:111], a[232:235], v[156:159], a[108:111]\n"
    "ds_read_b128 a[228:231], v241 offset:1024\n"
    "v_cvt_pk_bf16_f32 v170, v120, v121\n"
    "s_nop 3\n"
    "v_mfma_f32_16x16x32_bf16 a[112:115], a[232:235], v[160:163], a[112:115]\n"
    "v_exp_f32 v122, v122\n"
    "v_mfma_f32_16x16x32_bf16 a[116:119], a[232:235], v[164:167], a[116:119]\n"
    "buffer_load_dwordx4 v246, s[52:55], s69 offen lds\n"
    "s_nop 3\n"
    "v_mfma_f32_16x16x32_bf16 a[120:123], a[236:239], v[144:147], a[120:123]\n"
    "v_exp_f32 v123, v123\n"
    "s_nop 3\n"
    "v_mfma_f32_16x16x32_bf16 a[124:127], a[236:239], v[148:151], a[124:127]\n"
    "ds_read_b64_tr_b16 a[232:233], v242 offset:0\n"
    "v_cvt_pk_bf16_f32 v171, v122, v123\n"
    "s_nop 3\n"
    "v_mfma_f32_16x16x32_bf16 a[128:131], a[236:239], v[152:155], a[128:131]\n"
    "v_exp_f32 v124, v124\n"
    "v_mfma_f32_16x16x32_bf16 a[132:135], a[236:239], v[156:159], a[132:135]\n"
    "ds_read_b64_tr_b16 a[234:235], v242 offset:4096\n"
    "s_nop 3\n"
    "v_mfma_f32_16x16x32_bf16 a[136:139], a[236:239], v[160:163], a[136:139]\n"
    "v_exp_f32 v125, v125\n"
    "s_nop 3\n"
    "v_mfma_f32_16x16x32_bf16 a[140:143], a[236:239], v[164:167], a[140:143]\n"
    "buffer_load_dwordx4 v247, s[52:55], s69 offen offset:1024 lds\n"
    "s_nop 3\n"
    "v_mfma_f32_16x16x32_bf16 a[144:147], a[240:243], v[144:147], a[144:147]\n"
    "v_exp_f32 v126, v126\n"
    "v_mfma_f32_16x16x32_bf16 a[148:151], a[240:243], v[148:151], a[148:151]\n"
    "ds_read_b64_tr_b16 a[236:237], v243 offset:0\n"
    "v_cvt_pk_bf16_f32 v174, v124, v125\n"
    "s_nop 3\n"
    "v_mfma_f32_16x16x32_bf16 a[152:155], a[240:243], v[152:155], a[152:155]\n"
    "v_exp_f32 v127, v127\n"
    "s_nop 3\n"
    "v_mfma_f32_16x16x32_bf16 a[156:159], a[240:243], v[156:159], a[156:159]\n"
    "ds_read_b64_tr_b16 a[238:239], v243 offset:4096\n"
    "v_cvt_pk_bf16_f32 v175, v126, v127\n"
    "s_nop 3\n"
    "v_mfma_f32_16x16x32_bf16 a[160:163], a[240:243], v[160:163], a[160:163]\n"
    "v_exp_f32 v128, v128\n"
    "v_mfma_f32_16x16x32_bf16 a[164:167], a[240:243], v[164:167], a[164:167]\n"
    "buffer_load_dwordx4 v246, s[52:55], s69 offen offset:2048 lds\n"
    "s_nop 3\n"
    "v_mfma_f32_16x16x32_bf16 a[168:171], a[244:247], v[144:147], a[168:171]\n"
    "v_exp_f32 v129, v129\n"
    "s_nop 3\n"
    "v_mfma_f32_16x16x32_bf16 a[172:175], a[244:247], v[148:151], a[172:175]\n"
    "ds_read_b64_tr_b16 a[240:241], v242 offset:512\n"
    "v_cvt_pk_bf16_f32 v178, v128, v129\n"
    "s_nop 3\n"
    "v_mfma_f32_16x16x32_bf16 a[176:179], a[244:247], v[152:155], a[176:179]\n"
    "v_exp_f32 v130, v130\n"
    "v_mfma_f32_16x16x32_bf16 a[180:183], a[244:247], v[156:159], a[180:183]\n"
    "ds_read_b64_tr_b16 a[242:243], v242 offset:4608\n"
    "s_nop 3\n"
    "v_mfma_f32_16x16x32_bf16 a[184:187], a[244:247], v[160:163], a[184:187]\n"
    "v_exp_f32 v131, v131\n"
    "s_nop 3\n"
    "v_mfma_f32_16x16x32_bf16 a[188:191], a[244:247], v[164:167], a[188:191]\n"
    "buffer_load_dwordx4 v247, s[52:55], s69 offen offset:3072 lds\n"
    "s_nop 3\n"
    "v_mfma_f32_16x16x32_bf16 a[192:195], a[248:251], v[144:147], a[192:195]\n"
    "s_cmp_lg_u32 s74, 2\n"
    "s_cbranch_scc1 LWh%=\n"
    "buffer_load_dword v237, v233, s[20:23], 0 offen\n"
    "buffer_load_dword v237, v233, s[20:23], 0 offen offset:128\n"
    "buffer_load_dword v237, v233, s[20:23], 0 offen offset:256\n"
    "s_getpc_b64 s[82:83]\n"
    "LGhP%=:\n"
    "s_add_u32 s82, s82, LQ%=-LGhP%=\n"
    "s_addc_u32 s83, s83, 0\n"
    "global_load_dword v237, v239, s[82:83]\n"
    "LWh%=:\n"
    "v_exp_f32 v132, v132\n"
    "v_mfma_f32_16x16x32_bf16 a[196:199], a[248:251], v[148:151], a[196:199]\n"
    "ds_read_b64_tr_b16 a[244:245], v243 offset:512\n"
    "v_cvt_pk_bf16_f32 v179, v130, v131\n"
    "s_nop 3\n"
    "v_mfma_f32_16x16x32_bf16 a[200:203], a[248:251], v[152:155], a[200:203]\n"
    "s_sub_u32 s74, s74, 1\n"
    "v_exp_f32 v133, v133\n"
    "s_nop 3\n"
    "v_mfma_f32_16x16x32_bf16 a[204:207], a[248:251], v[156:159], a[204:207]\n"
    "ds_read_b64_tr_b16 a[246:247], v243 offset:4608\n"
    "v_cvt_pk_bf16_f32 v182, v132, v133\n"
    "s_nop 3\n"
    "v_mfma_f32_16x16x32_bf16 a[208:211], a[248:251], v[160:163], a[208:211]\n"
    "s_waitcnt lgkmcnt(8)\n"
    "v_exp_f32 v134, v134\n"
    "v_mfma_f32_16x16x32_bf16 a[212:215], a[248:251], v[164:167], a[212:215]\n"
    "s_cmp_gt_u32 s74, 4\n"
    "s_cbranch_scc1 LH%=\n"
    "s_cmp_lg_u32 s74, 0\n"
    "s_cbranch_scc1 LZ%=\n"
    "s_branch LQ%=\n"
    "LZ%=:\n"
    "v_mfma_f32_16x16x32_bf16 v[96:99], a[216:219], v[0:3], v[192:195]\n"
    "s_mov_b32 s69, s68\n"
    "v_exp_f32 v135, v135\n"
    "v_mfma_f32_16x16x32_bf16 v[100:103], a[216:219], v[16:19], v[196:199]\n"
    "v_cvt_pk_bf16_f32 v183, v134, v135\n"
    "v_mfma_f32_16x16x32_bf16 v[104:107], a[216:219], v[32:35], v[200:203]\n"
    "s_add_u32 s68, s68, 0x2000\n"
    "v_exp_f32 v136, v136\n"
    "v_mfma_f32_16x16x32_bf16 v[108:111], a[216:219], v[48:51], v[204:207]\n"
    "v_mfma_f32_16x16x32_bf16 v[112:115], a[216:219], v[64:67], v[208:211]\n"
    "s_add_u32 s71, s70, 0x14000\n"
    "v_exp_f32 v137, v137\n"
    "v_mfma_f32_16x16x32_bf16 v[116:119], a[216:219], v[80:83], v[212:215]\n"
    "v_cvt_pk_bf16_f32 v186, v136, v137\n"
    "v_mfma_f32_16x16x32_bf16 v[96:99], a[220:223], v[4:7], v[96:99]\n"
    "s_add_u32 s70, s70, s84\n"
    "v_exp_f32 v138, v138\n"
    "v_mfma_f32_16x16x32_bf16 v[100:103], a[220:223], v[20:23], v[100:103]\n"
    "ds_read_b128 a[216:219], v240 offset:4096\n"
    "v_mfma_f32_16x16x32_bf16 v[104:107], a[220:223], v[36:39], v[104:107]\n"
    "s_and_b32 s68, s68, 0x3ffff\n"
    "v_exp_f32 v139, v139\n"
    "v_mfma_f32_16x16x32_bf16 v[108:111], a[220:223], v[52:55], v[108:111]\n"
    "v_cvt_pk_bf16_f32 v187, v138, v139\n"
    "v_mfma_f32_16x16x32_bf16 v[112:115], a[220:223], v[68:71], v[112:115]\n"
    "s_mov_b32 s54, s50\n"
    "v_exp_f32 v140, v140\n"
    "v_mfma_f32_16x16x32_bf16 v[116:119], a[220:223], v[84:87], v[116:119]\n"
    "v_mfma_f32_16x16x32_bf16 v[96:99], a[224:227], v[8:11], v[96:99]\n"
    "s_cmp_le_i32 s63, 0\n"
    "v_exp_f32 v141, v141\n"
    "v_mfma_f32_16x16x32_bf16 v[100:103], a[224:227], v[24:27], v[100:103]\n"
    "ds_read_b128 a[220:223], v241 offset:4096\n"
    "v_cvt_pk_bf16_f32 v190, v140, v141\n"
    "v_mfma_f32_16x16x32_bf16 v[104:107], a[224:227], v[40:43], v[104:107]\n"
    "s_cselect_b32 s50, 0, s50\n"
    "v_exp_f32 v142, v142\n"
    "v_mfma_f32_16x16x32_bf16 v[108:111], a[224:227], v[56:59], v[108:111]\n"
    "v_mfma_f32_16x16x32_bf16 v[112:115], a[224:227], v[72:75], v[112:115]\n"
    "s_sub_u32 s63, s63, 1\n"
    "v_exp_f32 v143, v143\n"
    "v_mfma_f32_16x16x32_bf16 v[116:119], a[224:227], v[88:91], v[116:119]\n"
    "v_cvt_pk_bf16_f32 v191, v142, v143\n"
    "v_mfma_f32_16x16x32_bf16 v[96:99], a[228:231], v[12:15], v[96:99]\n"
    "v_mfma_f32_16x16x32_bf16 v[100:103], a[228:231], v[28:31], v[100:103]\n"
    "ds_read_b128 a[224:227], v240 offset:5120\n"
    "v_mfma_f32_16x16x32_bf16 v[104:107], a[228:231], v[44:47], v[104:107]\n"
    "v_mfma_f32_16x16x32_bf16 v[108:111], a[228:231], v[60:63], v[108:111]\n"
    "v_mfma_f32_16x16x32_bf16 v[112:115], a[228:231], v[76:79], v[112:115]\n"
    "s_waitcnt lgkmcnt(3)\n"
    "v_mfma_f32_16x16x32_bf16 v[116:119], a[228:231], v[92:95], v[116:119]\n"
    "v_mfma_f32_16x16x32_bf16 a[0:3], a[232:235], v[168:171], a[0:3]\n"
    "v_exp_f32 v96, v96\n"
    "v_mfma_f32_16x16x32_bf16 a[4:7], a[232:235], v[172:175], a[4:7]\n"
    "ds_read_b128 a[228:231], v241 offset:5120\n"
    "v_mfma_f32_16x16x32_bf16 a[8:11], a[232:235], v[176:179], a[8:11]\n"
    "v_exp_f32 v97, v97\n"
    "v_mfma_f32_16x16x32_bf16 a[12:15], a[232:235], v[180:183], a[12:15]\n"
    "v_exp_f32 v98, v98\n"
    "v_mfma_f32_16x16x32_bf16 a[16:19], a[232:235], v[184:187], a[16:19]\n"
    "v_exp_f32 v99, v99\n"
    "v_mfma_f32_16x16x32_bf16 a[20:23], a[232:235], v[188:191], a[20:23]\n"
    "v_cvt_pk_bf16_f32 v144, v96, v97\n"
    "v_cvt_pk_bf16_f32 v145, v98, v99\n"
    "v_mfma_f32_16x16x32_bf16 a[24:27], a[236:239], v[168:171], a[24:27]\n"
    "v_exp_f32 v100, v100\n"
    "v_mfma_f32_16x16x32_bf16 a[28:31], a[236:239], v[172:175], a[28:31]\n"
    "ds_read_b64_tr_b16 a[232:233], v242 offset:1024\n"
    "v_mfma_f32_16x16x32_bf16 a[32:35], a[236:239], v[176:179], a[32:35]\n"
    "v_exp_f32 v101, v101\n"
    "v_mfma_f32_16x16x32_bf16 a[36:39], a[236:239], v[180:183], a[36:39]\n"
    "ds_read_b64_tr_b16 a[234:235], v242 offset:5120\n"
    "v_cvt_pk_bf16_f32 v148, v100, v101\n"
    "v_mfma_f32_16x16x32_bf16 a[40:43], a[236:239], v[184:187], a[40:43]\n"
    "v_exp_f32 v102, v102\n"
    "v_mfma_f32_16x16x32_bf16 a[44:47], a[236:239], v[188:191], a[44:47]\n"
    "v_mfma_f32_16x16x32_bf16 a[48:51], a[240:243], v[168:171], a[48:51]\n"
    "v_exp_f32 v103, v103\n"
    "v_mfma_f32_16x16x32_bf16 a[52:55], a[240:243], v[172:175], a[52:55]\n"
    "ds_read_b64_tr_b16 a[236:237], v243 offset:1024\n"
    "v_cvt_pk_bf16_f32 v149, v102, v103\n"
    "v_mfma_f32_16x16x32_bf16 a[56:59], a[240:243], v[176:179], a[56:59]\n"
    "v_exp_f32 v104, v104\n"
    "v_mfma_f32_16x16x32_bf16 a[60:63], a[240:243], v[180:183], a[60:63]\n"
    "ds_read_b64_tr_b16 a[238:239], v243 offset:5120\n"
    "v_mfma_f32_16x16x32_bf16 a[64:67], a[240:243], v[184:187], a[64:67]\n"
    "v_exp_f32 v105, v105\n"
    "v_mfma_f32_16x16x32_bf16 a[68:71], a[240:243], v[188:191], a[68:71]\n"
    "v_cvt_pk_bf16_f32 v152, v104, v105\n"
    "v_mfma_f32_16x16x32_bf16 a[72:75], a[244:247], v[168:171], a[72:75]\n"
    "v_exp_f32 v106, v106\n"
    "v_mfma_f32_16x16x32_bf16 a[76:79], a[244:247], v[172:175], a[76:79]\n"
    "ds_read_b64_tr_b16 a[240:241], v242 offset:1536\n"
    "v_mfma_f32_16x16x32_bf16 a[80:83], a[244:247], v[176:179], a[80:83]\n"
    "v_exp_f32 v107, v107\n"
    "v_mfma_f32_16x16x32_bf16 a[84:87], a[244:247], v[180:183], a[84:87]\n"
    "ds_read_b64_tr_b16 a[242:243], v242 offset:5632\n"
    "v_cvt_pk_bf16_f32 v153, v106, v107\n"
    "v_mfma_f32_16x16x32_bf16 a[88:91], a[244:247], v[184:187], a[88:91]\n"
    "s_waitcnt lgkmcnt(6)\n"
    "v_exp_f32 v108, v108\n"
    "v_mfma_f32_16x16x32_bf16 a[92:95], a[244:247], v[188:191], a[92:95]\n"
    "v_mfma_f32_16x16x32_bf16 v[120:123], a[216:219], v[0:3], v[192:195]\n"
    "s_cmp_ge_u32 s70, s76\n"
    "v_exp_f32 v109, v109\n"
    "v_mfma_f32_16x16x32_bf16 v[124:127], a[216:219], v[16:19], v[196:199]\n"
    "ds_read_b64_tr_b16 a[244:245], v243 offset:1536\n"
    "v_cvt_pk_bf16_f32 v156, v108, v109\n"
    "v_mfma_f32_16x16x32_bf16 v[128:131], a[216:219], v[32:35], v[200:203]\n"
    "s_cselect_b32 s84, s75, 0x4000\n"
    "v_exp_f32 v110, v110\n"
    "v_mfma_f32_16x16x32_bf16 v[132:135], a[216:219], v[48:51], v[204:207]\n"
    "ds_read_b64_tr_b16 a[246:247], v243 offset:5632\n"
    "v_mfma_f32_16x16x32_bf16 v[136:139], a[216:219], v[64:67], v[208:211]\n"
    "s_mov_b32 s73, s72\n"
    "v_exp_f32 v111, v111\n"
    "v_mfma_f32_16x16x32_bf16 v[140:143], a[216:219], v[80:83], v[212:215]\n"
    "v_cvt_pk_bf16_f32 v157, v110, v111\n"
    "v_mfma_f32_16x16x32_bf16 v[120:123], a[220:223], v[4:7], v[120:123]\n"
    "s_cmp_eq_u32 s70, s64\n"
    "v_exp_f32 v112, v112\n"
    "v_mfma_f32_16x16x32_bf16 v[124:127], a[220:223], v[20:23], v[124:127]\n"
    "v_add_u32 v242, s73, v242\n"
    "v_add_u32 v243, s73, v243\n"
    "v_mfma_f32_16x16x32_bf16 v[128:131], a[220:223], v[36:39], v[128:131]\n"
    "s_cselect_b32 s72, s75, 0x4000\n"
    "v_exp_f32 v113, v113\n"
    "v_mfma_f32_16x16x32_bf16 v[132:135], a[220:223], v[52:55], v[132:135]\n"
    "v_cvt_pk_bf16_f32 v160, v112, v113\n"
    "v_mfma_f32_16x16x32_bf16 v[136:139], a[220:223], v[68:71], v[136:139]\n"
    "v_exp_f32 v114, v114\n"
    "v_mfma_f32_16x16x32_bf16 v[140:143], a[220:223], v[84:87], v[140:143]\n"
    "v_add_u32 v240, s72, v240\n"
    "v_add_u32 v241, s72, v241\n"
    "v_mfma_f32_16x16x32_bf16 v[120:123], a[224:227], v[8:11], v[120:123]\n"
    "s_waitcnt vmcnt(16) lgkmcnt(0)\n"
    "s_barrier\n"
    "s_mov_b32 m0, s70\n"
    "v_mfma_f32_16x16x32_bf16 v[124:127], a[224:227], v[24:27], v[124:127]\n"
    "buffer_load_dwordx4 v244, s[48:51], s68 offen nt lds\n"
    "v_mfma_f32_16x16x32_bf16 v[128:131], a[224:227], v[40:43], v[128:131]\n"
    "v_exp_f32 v115, v115\n"
    "v_mfma_f32_16x16x32_bf16 v[132:135], a[224:227], v[56:59], v[132:135]\n"
    "ds_read_b128 a[216:219], v240 offset:0\n"
    "v_cvt_pk_bf16_f32 v161, v114, v115\n"
    "v_mfma_f32_16x16x32_bf16 v[136:139], a[224:227], v[72:75], v[136:139]\n"
    "v_exp_f32 v116, v116\n"
    "v_mfma_f32_16x16x32_bf16 v[140:143], a[224:227], v[88:91], v[140:143]\n"
    "buffer_load_dwordx4 v245, s[48:51], s68 offen offset:1024 nt lds\n"
    "v_mfma_f32_16x16x32_bf16 v[120:123], a[228:231], v[12:15], v[120:123]\n"
    "v_exp_f32 v117, v117\n"
    "v_mfma_f32_16x16x32_bf16 v[124:127], a[228:231], v[28:31], v[124:127]\n"
    "ds_read_b128 a[220:223], v241 offset:0\n"
    "v_cvt_pk_bf16_f32 v164, v116, v117\n"
    "v_mfma_f32_16x16x32_bf16 v[128:131], a[228:231], v[44:47], v[128:131]\n"
    "v_exp_f32 v118, v118\n"
    "v_mfma_f32_16x16x32_bf16 v[132:135], a[228:231], v[60:63], v[132:135]\n"
    "buffer_load_dwordx4 v244, s[48:51], s68 offen offset:2048 nt lds\n"
    "v_mfma_f32_16x16x32_bf16 v[136:139], a[228:231], v[76:79], v[136:139]\n"
    "v_exp_f32 v119, v119\n"
    "v_mfma_f32_16x16x32_bf16 v[140:143], a[228:231], v[92:95], v[140:143]\n"
    "ds_read_b128 a[224:227], v240 offset:1024\n"
    "v_cvt_pk_bf16_f32 v165, v118, v119\n"
    "v_mfma_f32_16x16x32_bf16 a[96:99], a[232:235], v[168:171], a[96:99]\n"
    "v_exp_f32 v120, v120\n"
    "v_mfma_f32_16x16x32_bf16 a[100:103], a[232:235], v[172:175], a[100:103]\n"
    "buffer_load_dwordx4 v245, s[48:51], s68 offen offset:3072 nt lds\n"
    "v_mfma_f32_16x16x32_bf16 a[104:107], a[232:235], v[176:179], a[104:107]\n"
    "s_mov_b32 m0, s71\n"
    "v_exp_f32 v121, v121\n"
    "v_mfma_f32_16x16x32_bf16 a[108:111], a[232:235], v[180:183], a[108:111]\n"
    "ds_read_b128 a[228:231], v241 offset:1024\n"
    "v_cvt_pk_bf16_f32 v146, v120, v121\n"
    "v_mfma_f32_16x16x32_bf16 a[112:115], a[232:235], v[184:187], a[112:115]\n"
    "v_exp_f32 v122, v122\n"
    "v_mfma_f32_16x16x32_bf16 a[116:119], a[232:235], v[188:191], a[116:119]\n"
    "buffer_load_dwordx4 v246, s[52:55], s69 offen lds\n"
    "v_mfma_f32_16x16x32_bf16 a[120:123], a[236:239], v[168:171], a[120:123]\n"
    "v_exp_f32 v123, v123\n"
    "v_mfma_f32_16x16x32_bf16 a[124:127], a[236:239], v[172:175], a[124:127]\n"
    "ds_read_b64_tr_b16 a[232:233], v242 offset:0\n"
    "v_cvt_pk_bf16_f32 v147, v122, v123\n"
    "v_mfma_f32_16x16x32_bf16 a[128:131], a[236:239], v[176:179], a[128:131]\n"
    "v_exp_f32 v124, v124\n"
    "v_mfma_f32_16x16x32_bf16 a[132:135], a[236:239], v[180:183], a[132:135]\n"
    "ds_read_b64_tr_b16 a[234:235], v242 offset:4096\n"
    "v_mfma_f32_16x16x32_bf16 a[136:139], a[236:239], v[184:187], a[136:139]\n"
    "v_exp_f32 v125, v125\n"
    "v_mfma_f32_16x16x32_bf16 a[140:143], a[236:239], v[188:191], a[140:143]\n"
    "buffer_load_dwordx4 v247, s[52:55], s69 offen offset:1024 lds\n"
    "v_mfma_f32_16x16x32_bf16 a[144:147], a[240:243], v[168:171], a[144:147]\n"
    "v_exp_f32 v126, v126\n"
    "v_mfma_f32_16x16x32_bf16 a[148:151], a[240:243], v[172:175], a[148:151]\n"
    "ds_read_b64_tr_b16 a[236:237], v243 offset:0\n"
    "v_cvt_pk_bf16_f32 v150, v124, v125\n"
    "v_mfma_f32_16x16x32_bf16 a[152:155], a[240:243], v[176:179], a[152:155]\n"
    "v_exp_f32 v127, v127\n"
    "v_mfma_f32_16x16x32_bf16 a[156:159], a[240:243], v[180:183], a[156:159]\n"
    "ds_read_b64_tr_b16 a[238:239], v243 offset:4096\n"
    "v_cvt_pk_bf16_f32 v151, v126, v127\n"
    "v_mfma_f32_16x16x32_bf16 a[160:163], a[240:243], v[184:187], a[160:163]\n"
    "v_exp_f32 v128, v128\n"
    "v_mfma_f32_16x16x32_bf16 a[164:167], a[240:243], v[188:191], a[164:167]\n"
    "buffer_load_dwordx4 v246, s[52:55], s69 offen offset:2048 lds\n"
    "v_mfma_f32_16x16x32_bf16 a[168:171], a[244:247], v[168:171], a[168:171]\n"
    "v_exp_f32 v129, v129\n"
    "v_mfma_f32_16x16x32_bf16 a[172:175], a[244:247], v[172:175], a[172:175]\n"
    "ds_read_b64_tr_b16 a[240:241], v242 offset:512\n"
    "v_cvt_pk_bf16_f32 v154, v128, v129\n"
    "v_mfma_f32_16x16x32_bf16 a[176:179], a[244:247], v[176:179], a[176:179]\n"
    "v_exp_f32 v130, v130\n"
    "v_mfma_f32_16x16x32_bf16 a[180:183], a[244:247], v[180:183], a[180:183]\n"
    "ds_read_b64_tr_b16 a[242:243], v242 offset:4608\n"
    "v_mfma_f32_16x16x32_bf16 a[184:187], a[244:247], v[184:187], a[184:187]\n"
    "v_exp_f32 v131, v131\n"
    "v_mfma_f32_16x16x32_bf16 a[188:191], a[244:247], v[188:191], a[188:191]\n"
    "buffer_load_dwordx4 v247, s[52:55], s69 offen offset:3072 lds\n"
    "v_mfma_f32_16x16x32_bf16 a[192:195], a[248:251], v[168:171], a[192:195]\n"
    "v_exp_f32 v132, v132\n"
    "v_mfma_f32_16x16x32_bf16 a[196:199], a[248:251], v[172:175], a[196:199]\n"
    "ds_read_b64_tr_b16 a[244:245], v243 offset:512\n"
    "v_cvt_pk_bf16_f32 v155, v130, v131\n"
    "v_mfma_f32_16x16x32_bf16 a[200:203], a[248:251], v[176:179], a[200:203]\n"
    "v_exp_f32 v133, v133\n"
    "v_mfma_f32_16x16x32_bf16 a[204:207], a[248:251], v[180:183], a[204:207]\n"
    "ds_read_b64_tr_b16 a[246:247], v243 offset:4608\n"
    "v_cvt_pk_bf16_f32 v158, v132, v133\n"
    "v_mfma_f32_16x16x32_bf16 a[208:211], a[248:251], v[184:187], a[208:211]\n"
    "s_waitcnt lgkmcnt(8)\n"
    "v_exp_f32 v134, v134\n"
    "v_mfma_f32_16x16x32_bf16 a[212:215], a[248:251], v[188:191], a[212:215]\n"
    "v_mfma_f32_16x16x32_bf16 v[96:99], a[216:219], v[0:3], v[192:195]\n"
    "s_mov_b32 s69, s68\n"
    "v_exp_f32 v135, v135\n"
    "v_mfma_f32_16x16x32_bf16 v[100:103], a[216:219], v[16:19], v[196:199]\n"
    "v_cvt_pk_bf16_f32 v159, v134, v135\n"
    "v_mfma_f32_16x16x32_bf16 v[104:107], a[216:219], v[32:35], v[200:203]\n"
    "s_add_u32 s68, s68, 0x2000\n"
    "v_exp_f32 v136, v136\n"
    "v_mfma_f32_16x16x32_bf16 v[108:111], a[216:219], v[48:51], v[204:207]\n"
    "v_mfma_f32_16x16x32_bf16 v[112:115], a[216:219], v[64:67], v[208:211]\n"
    "s_add_u32 s71, s70, 0x14000\n"
    "v_exp_f32 v137, v137\n"
    "v_mfma_f32_16x16x32_bf16 v[116:119], a[216:219], v[80:83], v[212:215]\n"
    "v_cvt_pk_bf16_f32 v162, v136, v137\n"
    "v_mfma_f32_16x16x32_bf16 v[96:99], a[220:223], v[4:7], v[96:99]\n"
    "s_add_u32 s70, s70, s84\n"
    "v_exp_f32 v138, v138\n"
    "v_mfma_f32_16x16x32_bf16 v[100:103], a[220:223], v[20:23], v[100:103]\n"
    "ds_read_b128 a[216:219], v240 offset:4096\n"
    "v_mfma_f32_16x16x32_bf16 v[104:107], a[220:223], v[36:39], v[104:107]\n"
    "s_and_b32 s68, s68, 0x3ffff\n"
    "v_exp_f32 v139, v139\n"
    "v_mfma_f32_16x16x32_bf16 v[108:111], a[220:223], v[52:55], v[108:111]\n"
    "v_cvt_pk_bf16_f32 v163, v138, v139\n"
    "v_mfma_f32_16x16x32_bf16 v[112:115], a[220:223], v[68:71], v[112:115]\n"
    "s_mov_b32 s54, s50\n"
    "v_exp_f32 v140, v140\n"
    "v_mfma_f32_16x16x32_bf16 v[116:119], a[220:223], v[84:87], v[116:119]\n"
    "v_mfma_f32_16x16x32_bf16 v[96:99], a[224:227], v[8:11], v[96:99]\n"
    "s_cmp_le_i32 s63, 0\n"
    "v_exp_f32 v141, v141\n"
    "v_mfma_f32_16x16x32_bf16 v[100:103], a[224:227], v[24:27], v[100:103]\n"
    "ds_read_b128 a[220:223], v241 offset:4096\n"
    "v_cvt_pk_bf16_f32 v166, v140, v141\n"
    "v_mfma_f32_16x16x32_bf16 v[104:107], a[224:227], v[40:43], v[104:107]\n"
    "s_cselect_b32 s50, 0, s50\n"
    "v_exp_f32 v142, v142\n"
    "v_mfma_f32_16x16x32_bf16 v[108:111], a[224:227], v[56:59], v[108:111]\n"
    "v_mfma_f32_16x16x32_bf16 v[112:115], a[224:227], v[72:75], v[112:115]\n"
    "s_sub_u32 s63, s63, 1\n"
    "v_exp_f32 v143, v143\n"
    "v_mfma_f32_16x16x32_bf16 v[116:119], a[224:227], v[88:91], v[116:119]\n"
    "v_cvt_pk_bf16_f32 v167, v142, v143\n"
    "v_mfma_f32_16x16x32_bf16 v[96:99], a[228:231], v[12:15], v[96:99]\n"
    "v_mfma_f32_16x16x32_bf16 v[100:103], a[228:231], v[28:31], v[100:103]\n"
    "ds_read_b128 a[224:227], v240 offset:5120\n"
    "v_mfma_f32_16x16x32_bf16 v[104:107], a[228:231], v[44:47], v[104:107]\n"
    "v_mfma_f32_16x16x32_bf16 v[108:111], a[228:231], v[60:63], v[108:111]\n"
    "v_mfma_f32_16x16x32_bf16 v[112:115], a[228:231], v[76:79], v[112:115]\n"
    "s_waitcnt lgkmcnt(3)\n"
    "v_mfma_f32_16x16x32_bf16 v[116:119], a[228:231], v[92:95], v[116:119]\n"
    "v_mfma_f32_16x16x32_bf16 a[0:3], a[232:235], v[144:147], a[0:3]\n"
    "v_exp_f32 v96, v96\n"
    "v_mfma_f32_16x16x32_bf16 a[4:7], a[232:235], v[148:151], a[4:7]\n"
    "ds_read_b128 a[228:231], v241 offset:5120\n"
    "v_mfma_f32_16x16x32_bf16 a[8:11], a[232:235], v[152:155], a[8:11]\n"
    "v_exp_f32 v97, v97\n"
    "v_mfma_f32_16x16x32_bf16 a[12:15], a[232:235], v[156:159], a[12:15]\n"
    "v_exp_f32 v98, v98\n"
    "v_mfma_f32_16x16x32_bf16 a[16:19], a[232:235], v[160:163], a[16:19]\n"
    "v_exp_f32 v99, v99\n"
    "v_mfma_f32_16x16x32_bf16 a[20:23], a[232:235], v[164:167], a[20:23]\n"
    "v_cvt_pk_bf16_f32 v168, v96, v97\n"
    "v_cvt_pk_bf16_f32 v169, v98, v99\n"
    "v_mfma_f32_16x16x32_bf16 a[24:27], a[236:239], v[144:147], a[24:27]\n"
    "v_exp_f32 v100, v100\n"
    "v_mfma_f32_16x16x32_bf16 a[28:31], a[236:239], v[148:151], a[28:31]\n"
    "ds_read_b64_tr_b16 a[232:233], v242 offset:1024\n"
    "v_mfma_f32_16x16x32_bf16 a[32:35], a[236:239], v[152:155], a[32:35]\n"
    "v_exp_f32 v101, v101\n"
    "v_mfma_f32_16x16x32_bf16 a[36:39], a[236:239], v[156:159], a[36:39]\n"
    "ds_read_b64_tr_b16 a[234:235], v242 offset:5120\n"
    "v_cvt_pk_bf16_f32 v172, v100, v101\n"
    "v_mfma_f32_16x16x32_bf16 a[40:43], a[236:239], v[160:163], a[40:43]\n"
    "v_exp_f32 v102, v102\n"
    "v_mfma_f32_16x16x32_bf16 a[44:47], a[236:239], v[164:167], a[44:47]\n"
    "v_mfma_f32_16x16x32_bf16 a[48:51], a[240:243], v[144:147], a[48:51]\n"
    "v_exp_f32 v103, v103\n"
    "v_mfma_f32_16x16x32_bf16 a[52:55], a[240:243], v[148:151], a[52:55]\n"
    "ds_read_b64_tr_b16 a[236:237], v243 offset:1024\n"
    "v_cvt_pk_bf16_f32 v173, v102, v103\n"
    "v_mfma_f32_16x16x32_bf16 a[56:59], a[240:243], v[152:155], a[56:59]\n"
    "v_exp_f32 v104, v104\n"
    "v_mfma_f32_16x16x32_bf16 a[60:63], a[240:243], v[156:159], a[60:63]\n"
    "ds_read_b64_tr_b16 a[238:239], v243 offset:5120\n"
    "v_mfma_f32_16x16x32_bf16 a[64:67], a[240:243], v[160:163], a[64:67]\n"
    "v_exp_f32 v105, v105\n"
    "v_mfma_f32_16x16x32_bf16 a[68:71], a[240:243], v[164:167], a[68:71]\n"
    "v_cvt_pk_bf16_f32 v176, v104, v105\n"
    "v_mfma_f32_16x16x32_bf16 a[72:75], a[244:247], v[144:147], a[72:75]\n"
    "v_exp_f32 v106, v106\n"
    "v_mfma_f32_16x16x32_bf16 a[76:79], a[244:247], v[148:151], a[76:79]\n"
    "ds_read_b64_tr_b16 a[240:241], v242 offset:1536\n"
    "v_mfma_f32_16x16x32_bf16 a[80:83], a[244:247], v[152:155], a[80:83]\n"
    "v_exp_f32 v107, v107\n"
    "v_mfma_f32_16x16x32_bf16 a[84:87], a[244:247], v[156:159], a[84:87]\n"
    "ds_read_b64_tr_b16 a[242:243], v242 offset:5632\n"
    "v_cvt_pk_bf16_f32 v177, v106, v107\n"
    "v_mfma_f32_16x16x32_bf16 a[88:91], a[244:247], v[160:163], a[88:91]\n"
    "s_waitcnt lgkmcnt(6)\n"
    "v_exp_f32 v108, v108\n"
    "v_mfma_f32_16x16x32_bf16 a[92:95], a[244:247], v[164:167], a[92:95]\n"
    "v_mfma_f32_16x16x32_bf16 v[120:123], a[216:219], v[0:3], v[192:195]\n"
    "s_cmp_ge_u32 s70, s76\n"
    "v_exp_f32 v109, v109\n"
    "v_mfma_f32_16x16x32_bf16 v[124:127], a[216:219], v[16:19], v[196:199]\n"
    "ds_read_b64_tr_b16 a[244:245], v243 offset:1536\n"
    "v_cvt_pk_bf16_f32 v180, v108, v109\n"
    "v_mfma_f32_16x16x32_bf16 v[128:131], a[216:219], v[32:35], v[200:203]\n"
    "s_cselect_b32 s84, s75, 0x4000\n"
    "v_exp_f32 v110, v110\n"
    "v_mfma_f32_16x16x32_bf16 v[132:135], a[216:219], v[48:51], v[204:207]\n"
    "ds_read_b64_tr_b16 a[246:247], v243 offset:5632\n"
    "v_mfma_f32_16x16x32_bf16 v[136:139], a[216:219], v[64:67], v[208:211]\n"
    "s_mov_b32 s73, s72\n"
    "v_exp_f32 v111, v111\n"
    "v_mfma_f32_16x16x32_bf16 v[140:143], a[216:219], v[80:83], v[212:215]\n"
    "v_cvt_pk_bf16_f32 v181, v110, v111\n"
    "v_mfma_f32_16x16x32_bf16 v[120:123], a[220:223], v[4:7], v[120:123]\n"
    "s_cmp_eq_u32 s70, s64\n"
    "v_exp_f32 v112, v112\n"
    "v_mfma_f32_16x16x32_bf16 v[124:127], a[220:223], v[20:23], v[124:127]\n"
    "v_add_u32 v242, s73, v242\n"
    "v_add_u32 v243, s73, v243\n"
    "v_mfma_f32_16x16x32_bf16 v[128:131], a[220:223], v[36:39], v[128:131]\n"
    "s_cselect_b32 s72, s75, 0x4000\n"
    "v_exp_f32 v113, v113\n"
    "v_mfma_f32_16x16x32_bf16 v[132:135], a[220:223], v[52:55], v[132:135]\n"
    "v_cvt_pk_bf16_f32 v184, v112, v113\n"
    "v_mfma_f32_16x16x32_bf16 v[136:139], a[220:223], v[68:71], v[136:139]\n"
    "v_exp_f32 v114, v114\n"
    "v_mfma_f32_16x16x32_bf16 v[140:143], a[220:223], v[84:87], v[140:143]\n"
    "v_add_u32 v240, s72, v240\n"
    "v_add_u32 v241, s72, v241\n"
    "v_mfma_f32_16x16x32_bf16 v[120:123], a[224:227], v[8:11], v[120:123]\n"
    "s_waitcnt vmcnt(16) lgkmcnt(0)\n"
    "s_barrier\n"
    "s_mov_b32 m0, s70\n"
    "v_mfma_f32_16x16x32_bf16 v[124:127], a[224:227], v[24:27], v[124:127]\n"
    "buffer_load_dwordx4 v244, s[48:51], s68 offen nt lds\n"
    "v_mfma_f32_16x16x32_bf16 v[128:131], a[224:227], v[40:43], v[128:131]\n"
    "v_exp_f32 v115, v115\n"
    "v_mfma_f32_16x16x32_bf16 v[132:135], a[224:227], v[56:59], v[132:135]\n"
    "ds_read_b128 a[216:219], v240 offset:0\n"
    "v_cvt_pk_bf16_f32 v185, v114, v115\n"
    "v_mfma_f32_16x16x32_bf16 v[136:139], a[224:227], v[72:75], v[136:139]\n"
    "v_exp_f32 v116, v116\n"
    "v_mfma_f32_16x16x32_bf16 v[140:143], a[224:227], v[88:91], v[140:143]\n"
    "buffer_load_dwordx4 v245, s[48:51], s68 offen offset:1024 nt lds\n"
    "v_mfma_f32_16x16x32_bf16 v[120:123], a[228:231], v[12:15], v[120:123]\n"
    "v_exp_f32 v117, v117\n"
    "v_mfma_f32_16x16x32_bf16 v[124:127], a[228:231], v[28:31], v[124:127]\n"
    "ds_read_b128 a[220:223], v241 offset:0\n"
    "v_cvt_pk_bf16_f32 v188, v116, v117\n"
    "v_mfma_f32_16x16x32_bf16 v[128:131], a[228:231], v[44:47], v[128:131]\n"
    "v_exp_f32 v118, v118\n"
    "v_mfma_f32_16x16x32_bf16 v[132:135], a[228:231], v[60:63], v[132:135]\n"
    "buffer_load_dwordx4 v244, s[48:51], s68 offen offset:2048 nt lds\n"
    "v_mfma_f32_16x16x32_bf16 v[136:139], a[228:231], v[76:79], v[136:139]\n"
    "v_exp_f32 v119, v119\n"
    "v_mfma_f32_16x16x32_bf16 v[140:143], a[228:231], v[92:95], v[140:143]\n"
    "ds_read_b128 a[224:227], v240 offset:1024\n"
    "v_cvt_pk_bf16_f32 v189, v118, v119\n"
    "v_mfma_f32_16x16x32_bf16 a[96:99], a[232:235], v[144:147], a[96:99]\n"
    "v_exp_f32 v120, v120\n"
    "v_mfma_f32_16x16x32_bf16 a[100:103], a[232:235], v[148:151], a[100:103]\n"
    "buffer_load_dwordx4 v245, s[48:51], s68 offen offset:3072 nt lds\n"
    "v_mfma_f32_16x16x32_bf16 a[104:107], a[232:235], v[152:155], a[104:107]\n"
    "s_mov_b32 m0, s71\n"
    "v_exp_f32 v121, v121\n"
    "v_mfma_f32_16x16x32_bf16 a[108:111], a[232:235], v[156:159], a[108:111]\n"
    "ds_read_b128 a[228:231], v241 offset:1024\n"
    "v_cvt_pk_bf16_f32 v170, v120, v121\n"
    "v_mfma_f32_16x16x32_bf16 a[112:115], a[232:235], v[160:163], a[112:115]\n"
    "v_exp_f32 v122, v122\n"
    "v_mfma_f32_16x16x32_bf16 a[116:119], a[232:235], v[164:167], a[116:119]\n"
    "buffer_load_dwordx4 v246, s[52:55], s69 offen lds\n"
    "v_mfma_f32_16x16x32_bf16 a[120:123], a[236:239], v[144:147], a[120:123]\n"
    "v_exp_f32 v123, v123\n"
    "v_mfma_f32_16x16x32_bf16 a[124:127], a[236:239], v[148:151], a[124:127]\n"
    "ds_read_b64_tr_b16 a[232:233], v242 offset:0\n"
    "v_cvt_pk_bf16_f32 v171, v122, v123\n"
    "v_mfma_f32_16x16x32_bf16 a[128:131], a[236:239], v[152:155], a[128:131]\n"
    "v_exp_f32 v124, v124\n"
    "v_mfma_f32_16x16x32_bf16 a[132:135], a[236:239], v[156:159], a[132:135]\n"
    "ds_read_b64_tr_b16 a[234:235], v242 offset:4096\n"
    "v_mfma_f32_16x16x32_bf16 a[136:139], a[236:239], v[160:163], a[136:139]\n"
    "v_exp_f32 v125, v125\n"
    "v_mfma_f32_16x16x32_bf16 a[140:143], a[236:239], v[164:167], a[140:143]\n"
    "buffer_load_dwordx4 v247, s[52:55], s69 offen offset:1024 lds\n"
    "v_mfma_f32_16x16x32_bf16 a[144:147], a[240:243], v[144:147], a[144:147]\n"
    "v_exp_f32 v126, v126\n"
    "v_mfma_f32_16x16x32_bf16 a[148:151], a[240:243], v[148:151], a[148:151]\n"
    "ds_read_b64_tr_b16 a[236:237], v243 offset:0\n"
    "v_cvt_pk_bf16_f32 v174, v124, v125\n"
    "v_mfma_f32_16x16x32_bf16 a[152:155], a[240:243], v[152:155], a[152:155]\n"
    "v_exp_f32 v127, v127\n"
    "v_mfma_f32_16x16x32_bf16 a[156:159], a[240:243], v[156:159], a[156:159]\n"
    "ds_read_b64_tr_b16 a[238:239], v243 offset:4096\n"
    "v_cvt_pk_bf16_f32 v175, v126, v127\n"
    "v_mfma_f32_16x16x32_bf16 a[160:163], a[240:243], v[160:163], a[160:163]\n"
    "v_exp_f32 v128, v128\n"
    "v_mfma_f32_16x16x32_bf16 a[164:167], a[240:243], v[164:167], a[164:167]\n"
    "buffer_load_dwordx4 v246, s[52:55], s69 offen offset:2048 lds\n"
    "v_mfma_f32_16x16x32_bf16 a[168:171], a[244:247], v[144:147], a[168:171]\n"
    "v_exp_f32 v129, v129\n"
    "v_mfma_f32_16x16x32_bf16 a[172:175], a[244:247], v[148:151], a[172:175]\n"
    "ds_read_b64_tr_b16 a[240:241], v242 offset:512\n"
    "v_cvt_pk_bf16_f32 v178, v128, v129\n"
    "v_mfma_f32_16x16x32_bf16 a[176:179], a[244:247], v[152:155], a[176:179]\n"
    "v_exp_f32 v130, v130\n"
    "v_mfma_f32_16x16x32_bf16 a[180:183], a[244:247], v[156:159], a[180:183]\n"
    "ds_read_b64_tr_b16 a[242:243], v242 offset:4608\n"
    "v_mfma_f32_16x16x32_bf16 a[184:187], a[244:247], v[160:163], a[184:187]\n"
    "v_exp_f32 v131, v131\n"
    "v_mfma_f32_16x16x32_bf16 a[188:191], a[244:247], v[164:167], a[188:191]\n"
    "buffer_load_dwordx4 v247, s[52:55], s69 offen offset:3072 lds\n"
    "v_mfma_f32_16x16x32_bf16 a[192:195], a[248:251], v[144:147], a[192:195]\n"
    "s_cmp_lg_u32 s74, 2\n"
    "s_cbranch_scc1 LWz%=\n"
    "buffer_load_dword v237, v233, s[20:23], 0 offen\n"
    "buffer_load_dword v237, v233, s[20:23], 0 offen offset:128\n"
    "buffer_load_dword v237, v233, s[20:23], 0 offen offset:256\n"
    "s_getpc_b64 s[82:83]\n"
    "LGzP%=:\n"
    "s_add_u32 s82, s82, LQ%=-LGzP%=\n"
    "s_addc_u32 s83, s83, 0\n"
    "global_load_dword v237, v239, s[82:83]\n"
    "LWz%=:\n"
    "v_exp_f32 v132, v132\n"
    "v_mfma_f32_16x16x32_bf16 a[196:199], a[248:251], v[148:151], a[196:199]\n"
    "ds_read_b64_tr_b16 a[244:245], v243 offset:512\n"
    "v_cvt_pk_bf16_f32 v179, v130, v131\n"
    "v_mfma_f32_16x16x32_bf16 a[200:203], a[248:251], v[152:155], a[200:203]\n"
    "s_sub_u32 s74, s74, 1\n"
    "v_exp_f32 v133, v133\n"
    "v_mfma_f32_16x16x32_bf16 a[204:207], a[248:251], v[156:159], a[204:207]\n"
    "ds_read_b64_tr_b16 a[246:247], v243 offset:4608\n"
    "v_cvt_pk_bf16_f32 v182, v132, v133\n"
    "v_mfma_f32_16x16x32_bf16 a[208:211], a[248:251], v[160:163], a[208:211]\n"
    "s_waitcnt lgkmcnt(8)\n"
    "v_exp_f32 v134, v134\n"
    "v_mfma_f32_16x16x32_bf16 a[212:215], a[248:251], v[164:167], a[212:215]\n"
    "s_cmp_lg_u32 s74, 0\n"
    "s_cbranch_scc1 LZ%=\n"
    "LQ%=:\n"
    "v_exp_f32 v135, v135\n"
    "v_exp_f32 v136, v136\n"
    "v_exp_f32 v137, v137\n"
    "v_exp_f32 v138, v138\n"
    "v_exp_f32 v139, v139\n"
    "v_exp_f32 v140, v140\n"
    "v_exp_f32 v141, v141\n"
    "v_exp_f32 v142, v142\n"
    "v_exp_f32 v143, v143\n"
    "s_waitcnt lgkmcnt(0)\n"
    "v_cvt_pk_bf16_f32 v183, v134, v135\n"
    "v_cvt_pk_bf16_f32 v186, v136, v137\n"
    "v_cvt_pk_bf16_f32 v187, v138, v139\n"
    "v_cvt_pk_bf16_f32 v190, v140, v141\n"
    "v_cvt_pk_bf16_f32 v191, v142, v143\n"
    "s_nop 1\n"
    "v_mfma_f32_16x16x32_bf16 a[0:3], a[232:235], v[168:171], a[0:3]\n"
    "v_mfma_f32_16x16x32_bf16 a[4:7], a[232:235], v[172:175], a[4:7]\n"
    "v_mfma_f32_16x16x32_bf16 a[8:11], a[232:235], v[176:179], a[8:11]\n"
    "v_mfma_f32_16x16x32_bf16 a[12:15], a[232:235], v[180:183], a[12:15]\n"
    "v_mfma_f32_16x16x32_bf16 a[16:19], a[232:235], v[184:187], a[16:19]\n"
    "v_mfma_f32_16x16x32_bf16 a[20:23], a[232:235], v[188:191], a[20:23]\n"
    "v_mfma_f32_16x16x32_bf16 a[24:27], a[236:239], v[168:171], a[24:27]\n"
    "v_mfma_f32_16x16x32_bf16 a[28:31], a[236:239], v[172:175], a[28:31]\n"
    "ds_read_b64_tr_b16 a[232:233], v242 offset:1024\n"
    "v_mfma_f32_16x16x32_bf16 a[32:35], a[236:239], v[176:179], a[32:35]\n"
    "v_mfma_f32_16x16x32_bf16 a[36:39], a[236:239], v[180:183], a[36:39]\n"
    "ds_read_b64_tr_b16 a[234:235], v242 offset:5120\n"
    "v_mfma_f32_16x16x32_bf16 a[40:43], a[236:239], v[184:187], a[40:43]\n"
    "v_mfma_f32_16x16x32_bf16 a[44:47], a[236:239], v[188:191], a[44:47]\n"
    "v_mfma_f32_16x16x32_bf16 a[48:51], a[240:243], v[168:171], a[48:51]\n"
    "v_mfma_f32_16x16x32_bf16 a[52:55], a[240:243], v[172:175], a[52:55]\n"
    "ds_read_b64_tr_b16 a[236:237], v243 offset:1024\n"
    "v_mfma_f32_16x16x32_bf16 a[56:59], a[240:243], v[176:179], a[56:59]\n"
    "v_mfma_f32_16x16x32_bf16 a[60:63], a[240:243], v[180:183], a[60:63]\n"
    "ds_read_b64_tr_b16 a[238:239], v243 offset:5120\n"
    "v_mfma_f32_16x16x32_bf16 a[64:67], a[240:243], v[184:187], a[64:67]\n"
    "v_mfma_f32_16x16x32_bf16 a[68:71], a[240:243], v[188:191], a[68:71]\n"
    "v_mfma_f32_16x16x32_bf16 a[72:75], a[244:247], v[168:171], a[72:75]\n"
    "v_mfma_f32_16x16x32_bf16 a[76:79], a[244:247], v[172:175], a[76:79]\n"
    "ds_read_b64_tr_b16 a[240:241], v242 offset:1536\n"
    "v_mfma_f32_16x16x32_bf16 a[80:83], a[244:247], v[176:179], a[80:83]\n"
    "v_mfma_f32_16x16x32_bf16 a[84:87], a[244:247], v[180:183], a[84:87]\n"
    "ds_read_b64_tr_b16 a[242:243], v242 offset:5632\n"
    "v_mfma_f32_16x16x32_bf16 a[88:91], a[244:247], v[184:187], a[88:91]\n"
    "v_mfma_f32_16x16x32_bf16 a[92:95], a[244:247], v[188:191], a[92:95]\n"
    "v_mfma_f32_16x16x32_bf16 a[192:195], a[248:251], v[168:171], a[192:195]\n"
    "v_mfma_f32_16x16x32_bf16 a[196:199], a[248:251], v[172:175], a[196:199]\n"
    "ds_read_b64_tr_b16 a[244:245], v243 offset:1536\n"
    "v_mfma_f32_16x16x32_bf16 a[200:203], a[248:251], v[176:179], a[200:203]\n"
    "v_mfma_f32_16x16x32_bf16 a[204:207], a[248:251], v[180:183], a[204:207]\n"
    "ds_read_b64_tr_b16 a[246:247], v243 offset:5632\n"
    "v_mfma_f32_16x16x32_bf16 a[208:211], a[248:251], v[184:187], a[208:211]\n"
    "v_mfma_f32_16x16x32_bf16 a[212:215], a[248:251], v[188:191], a[212:215]\n"
    "s_waitcnt lgkmcnt(2)\n"
    "v_mfma_f32_16x16x32_bf16 a[96:99], a[232:235], v[168:171], a[96:99]\n"
    "v_mfma_f32_16x16x32_bf16 a[100:103], a[232:235], v[172:175], a[100:103]\n"
    "v_mfma_f32_16x16x32_bf16 a[104:107], a[232:235], v[176:179], a[104:107]\n"
    "v_mfma_f32_16x16x32_bf16 a[108:111], a[232:235], v[180:183], a[108:111]\n"
    "v_mfma_f32_16x16x32_bf16 a[112:115], a[232:235], v[184:187], a[112:115]\n"
    "v_mfma_f32_16x16x32_bf16 a[116:119], a[232:235], v[188:191], a[116:119]\n"
    "v_mfma_f32_16x16x32_bf16 a[120:123], a[236:239], v[168:171], a[120:123]\n"
    "v_mfma_f32_16x16x32_bf16 a[124:127], a[236:239], v[172:175], a[124:127]\n"
    "v_mfma_f32_16x16x32_bf16 a[128:131], a[236:239], v[176:179], a[128:131]\n"
    "v_mfma_f32_16x16x32_bf16 a[132:135], a[236:239], v[180:183], a[132:135]\n"
    "v_mfma_f32_16x16x32_bf16 a[136:139], a[236:239], v[184:187], a[136:139]\n"
    "s_waitcnt vmcnt(0) lgkmcnt(0)\n"
    "s_barrier\n"
    "v_mfma_f32_16x16x32_bf16 a[140:143], a[236:239], v[188:191], a[140:143]\n"
    "s_add_u32 m0, s80, 0x0\n"
    "s_add_u32 s62, s27, 0x0\n"
    "buffer_load_dwordx4 v228, s[36:39], s62 offen lds\n"
    "v_mfma_f32_16x16x32_bf16 a[144:147], a[240:243], v[168:171], a[144:147]\n"
    "buffer_load_dwordx4 v229, s[36:39], s62 offen offset:1024 lds\n"
    "v_mfma_f32_16x16x32_bf16 a[148:151], a[240:243], v[172:175], a[148:151]\n"
    "buffer_load_dwordx4 v230, s[36:39], s62 offen offset:2048 lds\n"
    "v_mfma_f32_16x16x32_bf16 a[152:155], a[240:243], v[176:179], a[152:155]\n"
    "buffer_load_dwordx4 v231, s[36:39], s62 offen offset:3072 lds\n"
    "v_mfma_f32_16x16x32_bf16 a[156:159], a[240:243], v[180:183], a[156:159]\n"
    "s_add_u32 m0, s80, 0x1000\n"
    "s_nop 0\n"
    "buffer_load_dwordx4 v232, s[36:39], s62 offen lds\n"
    "v_mfma_f32_16x16x32_bf16 a[160:163], a[240:243], v[184:187], a[160:163]\n"
    "s_add_u32 m0, s80, 0x5000\n"
    "s_add_u32 s62, s27, 0x14000\n"
    "buffer_load_dwordx4 v228, s[36:39], s62 offen lds\n"
    "v_mfma_f32_16x16x32_bf16 a[164:167], a[240:243], v[188:191], a[164:167]\n"
    "buffer_load_dwordx4 v229, s[36:39], s62 offen offset:1024 lds\n"
    "v_mfma_f32_16x16x32_bf16 a[168:171], a[244:247], v[168:171], a[168:171]\n"
    "buffer_load_dwordx4 v230, s[36:39], s62 offen offset:2048 lds\n"
    "v_mfma_f32_16x16x32_bf16 a[172:175], a[244:247], v[172:175], a[172:175]\n"
    "buffer_load_dwordx4 v231, s[36:39], s62 offen offset:3072 lds\n"
    "v_mfma_f32_16x16x32_bf16 a[176:179], a[244:247], v[176:179], a[176:179]\n"
    "s_add_u32 m0, s80, 0x6000\n"
    "s_nop 0\n"
    "buffer_load_dwordx4 v232, s[36:39], s62 offen lds\n"
    "v_mfma_f32_16x16x32_bf16 a[180:183], a[244:247], v[180:183], a[180:183]\n"
    "v_mfma_f32_16x16x32_bf16 a[184:187], a[244:247], v[184:187], a[184:187]\n"
    "v_mfma_f32_16x16x32_bf16 a[188:191], a[244:247], v[188:191], a[188:191]\n"
    "v_mbcnt_lo_u32_b32 v248, -1, 0\n"
    "v_mbcnt_hi_u32_b32 v248, -1, v248\n"
    "v_lshlrev_b32 v248, 4, v248\n"
    "s_mov_b32 s84, 0x5d800000\n"
    "s_mov_b32 s81, 0\n"
    "v_mov_b32 v249, s84\n"
    "v_accvgpr_read_b32 v216, a192\n"
    "v_accvgpr_read_b32 v218, a196\n"
    "v_accvgpr_read_b32 v220, a200\n"
    "v_accvgpr_read_b32 v222, a204\n"
    "v_accvgpr_read_b32 v224, a208\n"
    "v_accvgpr_read_b32 v226, a212\n"
    "v_mov_b32 v217, v216\n"
    "v_mov_b32 v219, v218\n"
    "v_mov_b32 v221, v220\n"
    "v_mov_b32 v223, v222\n"
    "v_mov_b32 v225, v224\n"
    "v_mov_b32 v227, v226\n"
    "s_nop 0\n"
    "v_permlane16_swap_b32 v216, v217\n"
    "v_permlane16_swap_b32 v218, v219\n"
    "v_permlane16_swap_b32 v220, v221\n"
    "v_permlane16_swap_b32 v222, v223\n"
    "v_permlane16_swap_b32 v224, v225\n"
    "v_permlane16_swap_b32 v226, v227\n"
    "v_add_f32 v216, v216, v217\n"
    "v_add_f32 v218, v218, v219\n"
    "v_add_f32 v220, v220, v221\n"
    "v_add_f32 v222, v222, v223\n"
    "v_add_f32 v224, v224, v225\n"
    "v_add_f32 v226, v226, v227\n"
    "v_mov_b32 v217, v216\n"
    "v_mov_b32 v219, v218\n"
    "v_mov_b32 v221, v220\n"
    "v_mov_b32 v223, v222\n"
    "v_mov_b32 v225, v224\n"
    "v_mov_b32 v227, v226\n"
    "s_nop 0\n"
    "v_permlane32_swap_b32 v216, v217\n"
    "v_permlane32_swap_b32 v218, v219\n"
    "v_permlane32_swap_b32 v220, v221\n"
    "v_permlane32_swap_b32 v222, v223\n"
    "v_permlane32_swap_b32 v224, v225\n"
    "v_permlane32_swap_b32 v226, v227\n"
    "v_add_f32 v216, v216, v217\n"
    "v_add_f32 v218, v218, v219\n"
    "v_add_f32 v220, v220, v221\n"
    "v_add_f32 v222, v222, v223\n"
    "v_add_f32 v224, v224, v225\n"
    "v_add_f32 v226, v226, v227\n"
    "v_cmp_nlt_f32 vcc, v216, v249\n"
    "v_rcp_f32 v216, v216\n"
    "s_nop 0\n"
    "s_or_b32 s81, s81, vcc_lo\n"
    "s_or_b32 s81, s81, vcc_hi\n"
    "v_cmp_nlt_f32 vcc, v218, v249\n"
    "v_rcp_f32 v218, v218\n"
    "s_nop 0\n"
    "s_or_b32 s81, s81, vcc_lo\n"
    "s_or_b32 s81, s81, vcc_hi\n"
    "v_cmp_nlt_f32 vcc, v220, v249\n"
    "v_rcp_f32 v220, v220\n"
    "s_nop 0\n"
    "s_or_b32 s81, s81, vcc_lo\n"
    "s_or_b32 s81, s81, vcc_hi\n"
    "v_cmp_nlt_f32 vcc, v222, v249\n"
    "v_rcp_f32 v222, v222\n"
    "s_nop 0\n"
    "s_or_b32 s81, s81, vcc_lo\n"
    "s_or_b32 s81, s81, vcc_hi\n"
    "v_cmp_nlt_f32 vcc, v224, v249\n"
    "v_rcp_f32 v224, v224\n"
    "s_nop 0\n"
    "s_or_b32 s81, s81, vcc_lo\n"
    "s_or_b32 s81, s81, vcc_hi\n"
    "v_cmp_nlt_f32 vcc, v226, v249\n"
    "v_rcp_f32 v226, v226\n"
    "s_nop 0\n"
    "s_or_b32 s81, s81, vcc_lo\n"
    "s_or_b32 s81, s81, vcc_hi\n"
    "v_add_u32 v250, s19, v248\n"
    "s_add_u32 m0, s80, 0xa000\n"
    "s_add_u32 s62, s27, 0x28000\n"
    "buffer_load_dwordx4 v228, s[36:39], s62 offen lds\n"
    "buffer_load_dwordx4 v229, s[36:39], s62 offen offset:1024 lds\n"
    "buffer_load_dwordx4 v230, s[36:39], s62 offen offset:2048 lds\n"
    "buffer_load_dwordx4 v231, s[36:39], s62 offen offset:3072 lds\n"
    "s_add_u32 m0, s80, 0xb000\n"
    "s_nop 0\n"
    "buffer_load_dwordx4 v232, s[36:39], s62 offen lds\n"
    "s_mov_b32 s62, 0\n"
    "v_accvgpr_read_b32 v96, a0\n"
    "v_accvgpr_read_b32 v97, a1\n"
    "v_accvgpr_read_b32 v98, a2\n"
    "v_accvgpr_read_b32 v99, a3\n"
    "v_accvgpr_read_b32 v100, a24\n"
    "v_accvgpr_read_b32 v101, a25\n"
    "v_accvgpr_read_b32 v102, a26\n"
    "v_accvgpr_read_b32 v103, a27\n"
    "v_accvgpr_read_b32 v104, a48\n"
    "v_accvgpr_read_b32 v105, a49\n"
    "v_accvgpr_read_b32 v106, a50\n"
    "v_accvgpr_read_b32 v107, a51\n"
    "v_accvgpr_read_b32 v108, a72\n"
    "v_accvgpr_read_b32 v109, a73\n"
    "v_accvgpr_read_b32 v110, a74\n"
    "v_accvgpr_read_b32 v111, a75\n"
    "v_accvgpr_read_b32 v112, a96\n"
    "v_accvgpr_read_b32 v113, a97\n"
    "v_accvgpr_read_b32 v114, a98\n"
    "v_accvgpr_read_b32 v115, a99\n"
    "v_accvgpr_read_b32 v116, a120\n"
    "v_accvgpr_read_b32 v117, a121\n"
    "v_accvgpr_read_b32 v118, a122\n"
    "v_accvgpr_read_b32 v119, a123\n"
    "v_accvgpr_read_b32 v120, a144\n"
    "v_accvgpr_read_b32 v121, a145\n"
    "v_accvgpr_read_b32 v122, a146\n"
    "v_accvgpr_read_b32 v123, a147\n"
    "v_accvgpr_read_b32 v124, a168\n"
    "v_accvgpr_read_b32 v125, a169\n"
    "v_accvgpr_read_b32 v126, a170\n"
    "v_accvgpr_read_b32 v127, a171\n"
    "v_pk_mul_f32 v[96:97], v[96:97], v[216:217] op_sel_hi:[1,0]\n"
    "v_pk_mul_f32 v[98:99], v[98:99], v[216:217] op_sel_hi:[1,0]\n"
    "v_pk_mul_f32 v[100:101], v[100:101], v[216:217] op_sel_hi:[1,0]\n"
    "v_pk_mul_f32 v[102:103], v[102:103], v[216:217] op_sel_hi:[1,0]\n"
    "v_pk_mul_f32 v[104:105], v[104:105], v[216:217] op_sel_hi:[1,0]\n"
    "v_pk_mul_f32 v[106:107], v[106:107], v[216:217] op_sel_hi:[1,0]\n"
    "v_pk_mul_f32 v[108:109], v[108:109], v[216:217] op_sel_hi:[1,0]\n"
    "v_pk_mul_f32 v[110:111], v[110:111], v[216:217] op_sel_hi:[1,0]\n"
    "v_pk_mul_f32 v[112:113], v[112:113], v[216:217] op_sel_hi:[1,0]\n"
    "v_pk_mul_f32 v[114:115], v[114:115], v[216:217] op_sel_hi:[1,0]\n"
    "v_pk_mul_f32 v[116:117], v[116:117], v[216:217] op_sel_hi:[1,0]\n"
    "v_pk_mul_f32 v[118:119], v[118:119], v[216:217] op_sel_hi:[1,0]\n"
    "v_pk_mul_f32 v[120:121], v[120:121], v[216:217] op_sel_hi:[1,0]\n"
    "v_pk_mul_f32 v[122:123], v[122:123], v[216:217] op_sel_hi:[1,0]\n"
    "v_pk_mul_f32 v[124:125], v[124:125], v[216:217] op_sel_hi:[1,0]\n"
    "v_pk_mul_f32 v[126:127], v[126:127], v[216:217] op_sel_hi:[1,0]\n"
    "v_cvt_pk_bf16_f32 v128, v96, v97\n"
    "v_cvt_pk_bf16_f32 v129, v98, v99\n"
    "v_cvt_pk_bf16_f32 v130, v100, v101\n"
    "v_cvt_pk_bf16_f32 v131, v102, v103\n"
    "v_cvt_pk_bf16_f32 v132, v104, v105\n"
    "v_cvt_pk_bf16_f32 v133, v106, v107\n"
    "v_cvt_pk_bf16_f32 v134, v108, v109\n"
    "v_cvt_pk_bf16_f32 v135, v110, v111\n"
    "v_cvt_pk_bf16_f32 v136, v112, v113\n"
    "v_cvt_pk_bf16_f32 v137, v114, v115\n"
    "v_cvt_pk_bf16_f32 v138, v116, v117\n"
    "v_cvt_pk_bf16_f32 v139, v118, v119\n"
    "v_cvt_pk_bf16_f32 v140, v120, v121\n"
    "v_cvt_pk_bf16_f32 v141, v122, v123\n"
    "v_cvt_pk_bf16_f32 v142, v124, v125\n"
    "v_cvt_pk_bf16_f32 v143, v126, v127\n"
    "buffer_store_dwordx4 v[128:131], v248, s[56:59], s62 offen sc1\n"
    "ds_write_b128 v250, v[128:131] offset:0\n"
    "buffer_store_dwordx4 v[132:135], v248, s[56:59], s62 offen offset:1024 sc1\n"
    "ds_write_b128 v250, v[132:135] offset:1024\n"
    "buffer_store_dwordx4 v[136:139], v248, s[56:59], s62 offen offset:2048 sc1\n"
    "ds_write_b128 v250, v[136:139] offset:2048\n"
    "buffer_store_dwordx4 v[140:143], v248, s[56:59], s62 offen offset:3072 sc1\n"
    "ds_write_b128 v250, v[140:143] offset:3072\n"
    "s_add_u32 s62, s62, 0x2000\n"
    "v_accvgpr_read_b32 v144, a4\n"
    "v_accvgpr_read_b32 v145, a5\n"
    "v_accvgpr_read_b32 v146, a6\n"
    "v_accvgpr_read_b32 v147, a7\n"
    "v_accvgpr_read_b32 v148, a28\n"
    "v_accvgpr_read_b32 v149, a29\n"
    "v_accvgpr_read_b32 v150, a30\n"
    "v_accvgpr_read_b32 v151, a31\n"
    "v_accvgpr_read_b32 v152, a52\n"
    "v_accvgpr_read_b32 v153, a53\n"
    "v_accvgpr_read_b32 v154, a54\n"
    "v_accvgpr_read_b32 v155, a55\n"
    "v_accvgpr_read_b32 v156, a76\n"
    "v_accvgpr_read_b32 v157, a77\n"
    "v_accvgpr_read_b32 v158, a78\n"
    "v_accvgpr_read_b32 v159, a79\n"
    "v_accvgpr_read_b32 v160, a100\n"
    "v_accvgpr_read_b32 v161, a101\n"
    "v_accvgpr_read_b32 v162, a102\n"
    "v_accvgpr_read_b32 v163, a103\n"
    "v_accvgpr_read_b32 v164, a124\n"
    "v_accvgpr_read_b32 v165, a125\n"
    "v_accvgpr_read_b32 v166, a126\n"
    "v_accvgpr_read_b32 v167, a127\n"
    "v_accvgpr_read_b32 v168, a148\n"
    "v_accvgpr_read_b32 v169, a149\n"
    "v_accvgpr_read_b32 v170, a150\n"
    "v_accvgpr_read_b32 v171, a151\n"
    "v_accvgpr_read_b32 v172, a172\n"
    "v_accvgpr_read_b32 v173, a173\n"
    "v_accvgpr_read_b32 v174, a174\n"
    "v_accvgpr_read_b32 v175, a175\n"
    "v_pk_mul_f32 v[144:145], v[144:145], v[218:219] op_sel_hi:[1,0]\n"
    "v_pk_mul_f32 v[146:147], v[146:147], v[218:219] op_sel_hi:[1,0]\n"
    "v_pk_mul_f32 v[148:149], v[148:149], v[218:219] op_sel_hi:[1,0]\n"
    "v_pk_mul_f32 v[150:151], v[150:151], v[218:219] op_sel_hi:[1,0]\n"
    "v_pk_mul_f32 v[152:153], v[152:153], v[218:219] op_sel_hi:[1,0]\n"
    "v_pk_mul_f32 v[154:155], v[154:155], v[218:219] op_sel_hi:[1,0]\n"
    "v_pk_mul_f32 v[156:157], v[156:157], v[218:219] op_sel_hi:[1,0]\n"
    "v_pk_mul_f32 v[158:159], v[158:159], v[218:219] op_sel_hi:[1,0]\n"
    "v_pk_mul_f32 v[160:161], v[160:161], v[218:219] op_sel_hi:[1,0]\n"
    "v_pk_mul_f32 v[162:163], v[162:163], v[218:219] op_sel_hi:[1,0]\n"
    "v_pk_mul_f32 v[164:165], v[164:165], v[218:219] op_sel_hi:[1,0]\n"
    "v_pk_mul_f32 v[166:167], v[166:167], v[218:219] op_sel_hi:[1,0]\n"
    "v_pk_mul_f32 v[168:169], v[168:169], v[218:219] op_sel_hi:[1,0]\n"
    "v_pk_mul_f32 v[170:171], v[170:171], v[218:219] op_sel_hi:[1,0]\n"
    "v_pk_mul_f32 v[172:173], v[172:173], v[218:219] op_sel_hi:[1,0]\n"
    "v_pk_mul_f32 v[174:175], v[174:175], v[218:219] op_sel_hi:[1,0]\n"
    "v_cvt_pk_bf16_f32 v176, v144, v145\n"
    "v_cvt_pk_bf16_f32 v177, v146, v147\n"
    "v_cvt_pk_bf16_f32 v178, v148, v149\n"
    "v_cvt_pk_bf16_f32 v179, v150, v151\n"
    "v_cvt_pk_bf16_f32 v180, v152, v153\n"
    "v_cvt_pk_bf16_f32 v181, v154, v155\n"
    "v_cvt_pk_bf16_f32 v182, v156, v157\n"
    "v_cvt_pk_bf16_f32 v183, v158, v159\n"
    "v_cvt_pk_bf16_f32 v184, v160, v161\n"
    "v_cvt_pk_bf16_f32 v185, v162, v163\n"
    "v_cvt_pk_bf16_f32 v186, v164, v165\n"
    "v_cvt_pk_bf16_f32 v187, v166, v167\n"
    "v_cvt_pk_bf16_f32 v188, v168, v169\n"
    "v_cvt_pk_bf16_f32 v189, v170, v171\n"
    "v_cvt_pk_bf16_f32 v190, v172, v173\n"
    "v_cvt_pk_bf16_f32 v191, v174, v175\n"
    "buffer_store_dwordx4 v[176:179], v248, s[56:59], s62 offen sc1\n"
    "ds_write_b128 v250, v[176:179] offset:8192\n"
    "buffer_store_dwordx4 v[180:183], v248, s[56:59], s62 offen offset:1024 sc1\n"
    "ds_write_b128 v250, v[180:183] offset:9216\n"
    "buffer_store_dwordx4 v[184:187], v248, s[56:59], s62 offen offset:2048 sc1\n"
    "ds_write_b128 v250, v[184:187] offset:10240\n"
    "buffer_store_dwordx4 v[188:191], v248, s[56:59], s62 offen offset:3072 sc1\n"
    "ds_write_b128 v250, v[188:191] offset:11264\n"
    "s_add_u32 s62, s62, 0x2000\n"
    "v_accvgpr_read_b32 v96, a8\n"
    "v_accvgpr_read_b32 v97, a9\n"
    "v_accvgpr_read_b32 v98, a10\n"
    "v_accvgpr_read_b32 v99, a11\n"
    "v_accvgpr_read_b32 v100, a32\n"
    "v_accvgpr_read_b32 v101, a33\n"
    "v_accvgpr_read_b32 v102, a34\n"
    "v_accvgpr_read_b32 v103, a35\n"
    "v_accvgpr_read_b32 v104, a56\n"
    "v_accvgpr_read_b32 v105, a57\n"
    "v_accvgpr_read_b32 v106, a58\n"
    "v_accvgpr_read_b32 v107, a59\n"
    "v_accvgpr_read_b32 v108, a80\n"
    "v_accvgpr_read_b32 v109, a81\n"
    "v_accvgpr_read_b32 v110, a82\n"
    "v_accvgpr_read_b32 v111, a83\n"
    "v_accvgpr_read_b32 v112, a104\n"
    "v_accvgpr_read_b32 v113, a105\n"
    "v_accvgpr_read_b32 v114, a106\n"
    "v_accvgpr_read_b32 v115, a107\n"
    "v_accvgpr_read_b32 v116, a128\n"
    "v_accvgpr_read_b32 v117, a129\n"
    "v_accvgpr_read_b32 v118, a130\n"
    "v_accvgpr_read_b32 v119, a131\n"
    "v_accvgpr_read_b32 v120, a152\n"
    "v_accvgpr_read_b32 v121, a153\n"
    "v_accvgpr_read_b32 v122, a154\n"
    "v_accvgpr_read_b32 v123, a155\n"
    "v_accvgpr_read_b32 v124, a176\n"
    "v_accvgpr_read_b32 v125, a177\n"
    "v_accvgpr_read_b32 v126, a178\n"
    "v_accvgpr_read_b32 v127, a179\n"
    "v_pk_mul_f32 v[96:97], v[96:97], v[220:221] op_sel_hi:[1,0]\n"
    "v_pk_mul_f32 v[98:99], v[98:99], v[220:221] op_sel_hi:[1,0]\n"
    "v_pk_mul_f32 v[100:101], v[100:101], v[220:221] op_sel_hi:[1,0]\n"
    "v_pk_mul_f32 v[102:103], v[102:103], v[220:221] op_sel_hi:[1,0]\n"
    "v_pk_mul_f32 v[104:105], v[104:105], v[220:221] op_sel_hi:[1,0]\n"
    "v_pk_mul_f32 v[106:107], v[106:107], v[220:221] op_sel_hi:[1,0]\n"
    "v_pk_mul_f32 v[108:109], v[108:109], v[220:221] op_sel_hi:[1,0]\n"
    "v_pk_mul_f32 v[110:111], v[110:111], v[220:221] op_sel_hi:[1,0]\n"
    "v_pk_mul_f32 v[112:113], v[112:113], v[220:221] op_sel_hi:[1,0]\n"
    "v_pk_mul_f32 v[114:115], v[114:115], v[220:221] op_sel_hi:[1,0]\n"
    "v_pk_mul_f32 v[116:117], v[116:117], v[220:221] op_sel_hi:[1,0]\n"
    "v_pk_mul_f32 v[118:119], v[118:119], v[220:221] op_sel_hi:[1,0]\n"
    "v_pk_mul_f32 v[120:121], v[120:121], v[220:221] op_sel_hi:[1,0]\n"
    "v_pk_mul_f32 v[122:123], v[122:123], v[220:221] op_sel_hi:[1,0]\n"
    "v_pk_mul_f32 v[124:125], v[124:125], v[220:221] op_sel_hi:[1,0]\n"
    "v_pk_mul_f32 v[126:127], v[126:127], v[220:221] op_sel_hi:[1,0]\n"
    "v_cvt_pk_bf16_f32 v128, v96, v97\n"
    "v_cvt_pk_bf16_f32 v129, v98, v99\n"
    "v_cvt_pk_bf16_f32 v130, v100, v101\n"
    "v_cvt_pk_bf16_f32 v131, v102, v103\n"
    "v_cvt_pk_bf16_f32 v132, v104, v105\n"
    "v_cvt_pk_bf16_f32 v133, v106, v107\n"
    "v_cvt_pk_bf16_f32 v134, v108, v109\n"
    "v_cvt_pk_bf16_f32 v135, v110, v111\n"
    "v_cvt_pk_bf16_f32 v136, v112, v113\n"
    "v_cvt_pk_bf16_f32 v137, v114, v115\n"
    "v_cvt_pk_bf16_f32 v138, v116, v117\n"
    "v_cvt_pk_bf16_f32 v139, v118, v119\n"
    "v_cvt_pk_bf16_f32 v140, v120, v121\n"
    "v_cvt_pk_bf16_f32 v141, v122, v123\n"
    "v_cvt_pk_bf16_f32 v142, v124, v125\n"
    "v_cvt_pk_bf16_f32 v143, v126, v127\n"
    "buffer_store_dwordx4 v[128:131], v248, s[56:59], s62 offen sc1\n"
    "ds_write_b128 v250, v[128:131] offset:16384\n"
    "buffer_store_dwordx4 v[132:135], v248, s[56:59], s62 offen offset:1024 sc1\n"
    "ds_write_b128 v250, v[132:135] offset:17408\n"
    "buffer_store_dwordx4 v[136:139], v248, s[56:59], s62 offen offset:2048 sc1\n"
    "ds_write_b128 v250, v[136:139] offset:18432\n"
    "buffer_store_dwordx4 v[140:143], v248, s[56:59], s62 offen offset:3072 sc1\n"
    "ds_write_b128 v250, v[140:143] offset:19456\n"
    "s_add_u32 s62, s62, 0x2000\n"
    "v_accvgpr_read_b32 v144, a12\n"
    "v_accvgpr_read_b32 v145, a13\n"
    "v_accvgpr_read_b32 v146, a14\n"
    "v_accvgpr_read_b32 v147, a15\n"
    "v_accvgpr_read_b32 v148, a36\n"
    "v_accvgpr_read_b32 v149, a37\n"
    "v_accvgpr_read_b32 v150, a38\n"
    "v_accvgpr_read_b32 v151, a39\n"
    "v_accvgpr_read_b32 v152, a60\n"
    "v_accvgpr_read_b32 v153, a61\n"
    "v_accvgpr_read_b32 v154, a62\n"
    "v_accvgpr_read_b32 v155, a63\n"
    "v_accvgpr_read_b32 v156, a84\n"
    "v_accvgpr_read_b32 v157, a85\n"
    "v_accvgpr_read_b32 v158, a86\n"
    "v_accvgpr_read_b32 v159, a87\n"
    "v_accvgpr_read_b32 v160, a108\n"
    "v_accvgpr_read_b32 v161, a109\n"
    "v_accvgpr_read_b32 v162, a110\n"
    "v_accvgpr_read_b32 v163, a111\n"
    "v_accvgpr_read_b32 v164, a132\n"
    "v_accvgpr_read_b32 v165, a133\n"
    "v_accvgpr_read_b32 v166, a134\n"
    "v_accvgpr_read_b32 v167, a135\n"
    "v_accvgpr_read_b32 v168, a156\n"
    "v_accvgpr_read_b32 v169, a157\n"
    "v_accvgpr_read_b32 v170, a158\n"
    "v_accvgpr_read_b32 v171, a159\n"
    "v_accvgpr_read_b32 v172, a180\n"
    "v_accvgpr_read_b32 v173, a181\n"
    "v_accvgpr_read_b32 v174, a182\n"
    "v_accvgpr_read_b32 v175, a183\n"
    "v_pk_mul_f32 v[144:145], v[144:145], v[222:223] op_sel_hi:[1,0]\n"
    "v_pk_mul_f32 v[146:147], v[146:147], v[222:223] op_sel_hi:[1,0]\n"
    "v_pk_mul_f32 v[148:149], v[148:149], v[222:223] op_sel_hi:[1,0]\n"
    "v_pk_mul_f32 v[150:151], v[150:151], v[222:223] op_sel_hi:[1,0]\n"
    "v_pk_mul_f32 v[152:153], v[152:153], v[222:223] op_sel_hi:[1,0]\n"
    "v_pk_mul_f32 v[154:155], v[154:155], v[222:223] op_sel_hi:[1,0]\n"
    "v_pk_mul_f32 v[156:157], v[156:157], v[222:223] op_sel_hi:[1,0]\n"
    "v_pk_mul_f32 v[158:159], v[158:159], v[222:223] op_sel_hi:[1,0]\n"
    "v_pk_mul_f32 v[160:161], v[160:161], v[222:223] op_sel_hi:[1,0]\n"
    "v_pk_mul_f32 v[162:163], v[162:163], v[222:223] op_sel_hi:[1,0]\n"
    "v_pk_mul_f32 v[164:165], v[164:165], v[222:223] op_sel_hi:[1,0]\n"
    "v_pk_mul_f32 v[166:167], v[166:167], v[222:223] op_sel_hi:[1,0]\n"
    "v_pk_mul_f32 v[168:169], v[168:169], v[222:223] op_sel_hi:[1,0]\n"
    "v_pk_mul_f32 v[170:171], v[170:171], v[222:223] op_sel_hi:[1,0]\n"
    "v_pk_mul_f32 v[172:173], v[172:173], v[222:223] op_sel_hi:[1,0]\n"
    "v_pk_mul_f32 v[174:175], v[174:175], v[222:223] op_sel_hi:[1,0]\n"
    "v_cvt_pk_bf16_f32 v176, v144, v145\n"
    "v_cvt_pk_bf16_f32 v177, v146, v147\n"
    "v_cvt_pk_bf16_f32 v178, v148, v149\n"
    "v_cvt_pk_bf16_f32 v179, v150, v151\n"
    "v_cvt_pk_bf16_f32 v180, v152, v153\n"
    "v_cvt_pk_bf16_f32 v181, v154, v155\n"
    "v_cvt_pk_bf16_f32 v182, v156, v157\n"
    "v_cvt_pk_bf16_f32 v183, v158, v159\n"
    "v_cvt_pk_bf16_f32 v184, v160, v161\n"
    "v_cvt_pk_bf16_f32 v185, v162, v163\n"
    "v_cvt_pk_bf16_f32 v186, v164, v165\n"
    "v_cvt_pk_bf16_f32 v187, v166, v167\n"
    "v_cvt_pk_bf16_f32 v188, v168, v169\n"
    "v_cvt_pk_bf16_f32 v189, v170, v171\n"
    "v_cvt_pk_bf16_f32 v190, v172, v173\n"
    "v_cvt_pk_bf16_f32 v191, v174, v175\n"
    "buffer_store_dwordx4 v[176:179], v248, s[56:59], s62 offen sc1\n"
    "ds_write_b128 v250, v[176:179] offset:24576\n"
    "buffer_store_dwordx4 v[180:183], v248, s[56:59], s62 offen offset:1024 sc1\n"
    "ds_write_b128 v250, v[180:183] offset:25600\n"
    "buffer_store_dwordx4 v[184:187], v248, s[56:59], s62 offen offset:2048 sc1\n"
    "ds_write_b128 v250, v[184:187] offset:26624\n"
    "buffer_store_dwordx4 v[188:191], v248, s[56:59], s62 offen offset:3072 sc1\n"
    "ds_write_b128 v250, v[188:191] offset:27648\n"
    "s_add_u32 s62, s62, 0x2000\n"
    "v_accvgpr_read_b32 v96, a16\n"
    "v_accvgpr_read_b32 v97, a17\n"
    "v_accvgpr_read_b32 v98, a18\n"
    "v_accvgpr_read_b32 v99, a19\n"
    "v_accvgpr_read_b32 v100, a40\n"
    "v_accvgpr_read_b32 v101, a41\n"
    "v_accvgpr_read_b32 v102, a42\n"
    "v_accvgpr_read_b32 v103, a43\n"
    "v_accvgpr_read_b32 v104, a64\n"
    "v_accvgpr_read_b32 v105, a65\n"
    "v_accvgpr_read_b32 v106, a66\n"
    "v_accvgpr_read_b32 v107, a67\n"
    "v_accvgpr_read_b32 v108, a88\n"
    "v_accvgpr_read_b32 v109, a89\n"
    "v_accvgpr_read_b32 v110, a90\n"
    "v_accvgpr_read_b32 v111, a91\n"
    "v_accvgpr_read_b32 v112, a112\n"
    "v_accvgpr_read_b32 v113, a113\n"
    "v_accvgpr_read_b32 v114, a114\n"
    "v_accvgpr_read_b32 v115, a115\n"
    "v_accvgpr_read_b32 v116, a136\n"
    "v_accvgpr_read_b32 v117, a137\n"
    "v_accvgpr_read_b32 v118, a138\n"
    "v_accvgpr_read_b32 v119, a139\n"
    "v_accvgpr_read_b32 v120, a160\n"
    "v_accvgpr_read_b32 v121, a161\n"
    "v_accvgpr_read_b32 v122, a162\n"
    "v_accvgpr_read_b32 v123, a163\n"
    "v_accvgpr_read_b32 v124, a184\n"
    "v_accvgpr_read_b32 v125, a185\n"
    "v_accvgpr_read_b32 v126, a186\n"
    "v_accvgpr_read_b32 v127, a187\n"
    "v_pk_mul_f32 v[96:97], v[96:97], v[224:225] op_sel_hi:[1,0]\n"
    "v_pk_mul_f32 v[98:99], v[98:99], v[224:225] op_sel_hi:[1,0]\n"
    "v_pk_mul_f32 v[100:101], v[100:101], v[224:225] op_sel_hi:[1,0]\n"
    "v_pk_mul_f32 v[102:103], v[102:103], v[224:225] op_sel_hi:[1,0]\n"
    "v_pk_mul_f32 v[104:105], v[104:105], v[224:225] op_sel_hi:[1,0]\n"
    "v_pk_mul_f32 v[106:107], v[106:107], v[224:225] op_sel_hi:[1,0]\n"
    "v_pk_mul_f32 v[108:109], v[108:109], v[224:225] op_sel_hi:[1,0]\n"
    "v_pk_mul_f32 v[110:111], v[110:111], v[224:225] op_sel_hi:[1,0]\n"
    "v_pk_mul_f32 v[112:113], v[112:113], v[224:225] op_sel_hi:[1,0]\n"
    "v_pk_mul_f32 v[114:115], v[114:115], v[224:225] op_sel_hi:[1,0]\n"
    "v_pk_mul_f32 v[116:117], v[116:117], v[224:225] op_sel_hi:[1,0]\n"
    "v_pk_mul_f32 v[118:119], v[118:119], v[224:225] op_sel_hi:[1,0]\n"
    "v_pk_mul_f32 v[120:121], v[120:121], v[224:225] op_sel_hi:[1,0]\n"
    "v_pk_mul_f32 v[122:123], v[122:123], v[224:225] op_sel_hi:[1,0]\n"
    "v_pk_mul_f32 v[124:125], v[124:125], v[224:225] op_sel_hi:[1,0]\n"
    "v_pk_mul_f32 v[126:127], v[126:127], v[224:225] op_sel_hi:[1,0]\n"
    "v_cvt_pk_bf16_f32 v128, v96, v97\n"
    "v_cvt_pk_bf16_f32 v129, v98, v99\n"
    "v_cvt_pk_bf16_f32 v130, v100, v101\n"
    "v_cvt_pk_bf16_f32 v131, v102, v103\n"
    "v_cvt_pk_bf16_f32 v132, v104, v105\n"
    "v_cvt_pk_bf16_f32 v133, v106, v107\n"
    "v_cvt_pk_bf16_f32 v134, v108, v109\n"
    "v_cvt_pk_bf16_f32 v135, v110, v111\n"
    "v_cvt_pk_bf16_f32 v136, v112, v113\n"
    "v_cvt_pk_bf16_f32 v137, v114, v115\n"
    "v_cvt_pk_bf16_f32 v138, v116, v117\n"
    "v_cvt_pk_bf16_f32 v139, v118, v119\n"
    "v_cvt_pk_bf16_f32 v140, v120, v121\n"
    "v_cvt_pk_bf16_f32 v141, v122, v123\n"
    "v_cvt_pk_bf16_f32 v142, v124, v125\n"
    "v_cvt_pk_bf16_f32 v143, v126, v127\n"
    "buffer_store_dwordx4 v[128:131], v248, s[56:59], s62 offen sc1\n"
    "ds_write_b128 v250, v[128:131] offset:32768\n"
    "buffer_store_dwordx4 v[132:135], v248, s[56:59], s62 offen offset:1024 sc1\n"
    "ds_write_b128 v250, v[132:135] offset:33792\n"
    "buffer_store_dwordx4 v[136:139], v248, s[56:59], s62 offen offset:2048 sc1\n"
    "ds_write_b128 v250, v[136:139] offset:34816\n"
    "buffer_store_dwordx4 v[140:143], v248, s[56:59], s62 offen offset:3072 sc1\n"
    "ds_write_b128 v250, v[140:143] offset:35840\n"
    "s_add_u32 s62, s62, 0x2000\n"
    "v_accvgpr_read_b32 v144, a20\n"
    "v_accvgpr_read_b32 v145, a21\n"
    "v_accvgpr_read_b32 v146, a22\n"
    "v_accvgpr_read_b32 v147, a23\n"
    "v_accvgpr_read_b32 v148, a44\n"
    "v_accvgpr_read_b32 v149, a45\n"
    "v_accvgpr_read_b32 v150, a46\n"
    "v_accvgpr_read_b32 v151, a47\n"
    "v_accvgpr_read_b32 v152, a68\n"
    "v_accvgpr_read_b32 v153, a69\n"
    "v_accvgpr_read_b32 v154, a70\n"
    "v_accvgpr_read_b32 v155, a71\n"
    "v_accvgpr_read_b32 v156, a92\n"
    "v_accvgpr_read_b32 v157, a93\n"
    "v_accvgpr_read_b32 v158, a94\n"
    "v_accvgpr_read_b32 v159, a95\n"
    "v_accvgpr_read_b32 v160, a116\n"
    "v_accvgpr_read_b32 v161, a117\n"
    "v_accvgpr_read_b32 v162, a118\n"
    "v_accvgpr_read_b32 v163, a119\n"
    "v_accvgpr_read_b32 v164, a140\n"
    "v_accvgpr_read_b32 v165, a141\n"
    "v_accvgpr_read_b32 v166, a142\n"
    "v_accvgpr_read_b32 v167, a143\n"
    "v_accvgpr_read_b32 v168, a164\n"
    "v_accvgpr_read_b32 v169, a165\n"
    "v_accvgpr_read_b32 v170, a166\n"
    "v_accvgpr_read_b32 v171, a167\n"
    "v_accvgpr_read_b32 v172, a188\n"
    "v_accvgpr_read_b32 v173, a189\n"
    "v_accvgpr_read_b32 v174, a190\n"
    "v_accvgpr_read_b32 v175, a191\n"
    "v_pk_mul_f32 v[144:145], v[144:145], v[226:227] op_sel_hi:[1,0]\n"
    "v_pk_mul_f32 v[146:147], v[146:147], v[226:227] op_sel_hi:[1,0]\n"
    "v_pk_mul_f32 v[148:149], v[148:149], v[226:227] op_sel_hi:[1,0]\n"
    "v_pk_mul_f32 v[150:151], v[150:151], v[226:227] op_sel_hi:[1,0]\n"
    "v_pk_mul_f32 v[152:153], v[152:153], v[226:227] op_sel_hi:[1,0]\n"
    "v_pk_mul_f32 v[154:155], v[154:155], v[226:227] op_sel_hi:[1,0]\n"
    "v_pk_mul_f32 v[156:157], v[156:157], v[226:227] op_sel_hi:[1,0]\n"
    "v_pk_mul_f32 v[158:159], v[158:159], v[226:227] op_sel_hi:[1,0]\n"
    "v_pk_mul_f32 v[160:161], v[160:161], v[226:227] op_sel_hi:[1,0]\n"
    "v_pk_mul_f32 v[162:163], v[162:163], v[226:227] op_sel_hi:[1,0]\n"
    "v_pk_mul_f32 v[164:165], v[164:165], v[226:227] op_sel_hi:[1,0]\n"
    "v_pk_mul_f32 v[166:167], v[166:167], v[226:227] op_sel_hi:[1,0]\n"
    "v_pk_mul_f32 v[168:169], v[168:169], v[226:227] op_sel_hi:[1,0]\n"
    "v_pk_mul_f32 v[170:171], v[170:171], v[226:227] op_sel_hi:[1,0]\n"
    "v_pk_mul_f32 v[172:173], v[172:173], v[226:227] op_sel_hi:[1,0]\n"
    "v_pk_mul_f32 v[174:175], v[174:175], v[226:227] op_sel_hi:[1,0]\n"
    "v_cvt_pk_bf16_f32 v176, v144, v145\n"
    "v_cvt_pk_bf16_f32 v177, v146, v147\n"
    "v_cvt_pk_bf16_f32 v178, v148, v149\n"
    "v_cvt_pk_bf16_f32 v179, v150, v151\n"
    "v_cvt_pk_bf16_f32 v180, v152, v153\n"
    "v_cvt_pk_bf16_f32 v181, v154, v155\n"
    "v_cvt_pk_bf16_f32 v182, v156, v157\n"
    "v_cvt_pk_bf16_f32 v183, v158, v159\n"
    "v_cvt_pk_bf16_f32 v184, v160, v161\n"
    "v_cvt_pk_bf16_f32 v185, v162, v163\n"
    "v_cvt_pk_bf16_f32 v186, v164, v165\n"
    "v_cvt_pk_bf16_f32 v187, v166, v167\n"
    "v_cvt_pk_bf16_f32 v188, v168, v169\n"
    "v_cvt_pk_bf16_f32 v189, v170, v171\n"
    "v_cvt_pk_bf16_f32 v190, v172, v173\n"
    "v_cvt_pk_bf16_f32 v191, v174, v175\n"
    "buffer_store_dwordx4 v[176:179], v248, s[56:59], s62 offen sc1\n"
    "ds_write_b128 v250, v[176:179] offset:40960\n"
    "buffer_store_dwordx4 v[180:183], v248, s[56:59], s62 offen offset:1024 sc1\n"
    "ds_write_b128 v250, v[180:183] offset:41984\n"
    "buffer_store_dwordx4 v[184:187], v248, s[56:59], s62 offen offset:2048 sc1\n"
    "ds_write_b128 v250, v[184:187] offset:43008\n"
    "buffer_store_dwordx4 v[188:191], v248, s[56:59], s62 offen offset:3072 sc1\n"
    "ds_write_b128 v250, v[188:191] offset:44032\n"
    "s_add_u32 s62, s62, 0x2000\n"
      : "+{v240}"(kr0), "+{v241}"(kr1), "+{v242}"(vr0), "+{v243}"(vr1), "+{v244}"(kd0), "+{v245}"(kd1), "+{v246}"(vd0), "+{v247}"(vd1),
        "+{v248}"(l16), "+{v249}"(xr1), "+{v250}"(opb), "+{v251}"(wr1), "+{v252}"(wr2), "+{v253}"(wr3), "+{v254}"(xd), "+{v255}"(wd),
        "+{v227}"(vz), "+{v233}"(ct), "={v228}"(w0), "={v229}"(w1), "={v230}"(w2), "={v231}"(w3), "={v232}"(w4), "={v234}"(rd0), "={v235}"(rd1), "={v236}"(ocb),
        "+{s[36:39]}"(sWO), "+{s[56:59]}"(sO), "+{s[78:79]}"(sCnt), "+{s80}"(om0), "+{s26}"(rot2), "+{s28}"(dO), "+{s29}"(dP), "+{s34}"(iiv), "+{s[8:9]}"(sOut), "+{s10}"(dC),
        "+{s[48:51]}"(sK), "+{s[52:55]}"(sV), "+{s18}"(phase), "+{s71}"(wme), "+{s72}"(wml), "+{s84}"(wsl), "+{s73}"(xsl), "+{s74}"(xml), "+{s[44:47]}"(sW), "+{s65}"(xm0), "+{s64}"(xso), "+{s66}"(wso),
        "={s81}"(flag), "={s85}"(samp), "={s25}"(samp2)
      : "{s[40:43]}"(sX), "{s[60:61]}"(sc), "{s[20:23]}"(sWT),
        "{s67}"(wm0),
        "{s77}"(rfl(rh * 4096u + (KROT & 31u) * 8192u)), "{s75}"(0xffff0000u), "{s76}"(lb + 65536u),
        "{s27}"(rfl(G.ii * 327680u)), "{s19}"(rfl(lb + 65536u + (rh * 48u + j * 4u) * 1024u)), "{s35}"(rfl(lb + 65536u + wid * 24576u)), "{s6}"(rfl((bid >> 3) * 896u))
      : "v0", "v1", "v2", "v3", "v4", "v5", "v6", "v7", "v8", "v9", "v10", "v11", "v12", "v13", "v14", "v15", "v16", "v17", "v18", "v19", "v20", "v21", "v22", "v23", "v24", "v25", "v26", "v27", "v28", "v29", "v30", "v31", "v32", "v33", "v34", "v35", "v36", "v37", "v38", "v39", "v40", "v41", "v42", "v43", "v44", "v45", "v46", "v47", "v48", "v49", "v50", "v51", "v52", "v53", "v54", "v55", "v56", "v57", "v58", "v59", "v60", "v61", "v62", "v63", "v64", "v65", "v66", "v67", "v68", "v69", "v70", "v71", "v72", "v73", "v74", "v75", "v76", "v77", "v78", "v79", "v80", "v81", "v82", "v83", "v84", "v85", "v86", "v87", "v88", "v89", "v90", "v91", "v92", "v93", "v94", "v95", "v96", "v97", "v98", "v99", "v100", "v101", "v102", "v103", "v104", "v105", "v106", "v107", "v108", "v109", "v110", "v111", "v112", "v113", "v114", "v115", "v116", "v117", "v118", "v119", "v120", "v121", "v122", "v123", "v124", "v125", "v126", "v127", "v128", "v129", "v130", "v131", "v132", "v133", "v134", "v135", "v136", "v137", "v138", "v139", "v140", "v141", "v142", "v143", "v144", "v145", "v146", "v147", "v148", "v149", "v150", "v151", "v152", "v153", "v154", "v155", "v156", "v157", "v158", "v159", "v160", "v161", "v162", "v163", "v164", "v165", "v166", "v167", "v168", "v169", "v170", "v171", "v172", "v173", "v174", "v175", "v176", "v177", "v178", "v179", "v180", "v181", "v182", "v183", "v184", "v185", "v186", "v187", "v188", "v189", "v190", "v191", "v192", "v193", "v194", "v195", "v196", "v197", "v198", "v199", "v200", "v201", "v202", "v203", "v204", "v205", "v206", "v207", "v208", "v209", "v210", "v211", "v212", "v213", "v214", "v215", "v216", "v217", "v218", "v219", "v220", "v221", "v222", "v223", "v224", "v225", "v226", "v237", "v238", "v239", "a0", "a1", "a2", "a3", "a4", "a5", "a6", "a7", "a8", "a9", "a10", "a11", "a12", "a13", "a14", "a15", "a16", "a17", "a18", "a19", "a20", "a21", "a22", "a23", "a24", "a25", "a26", "a27", "a28", "a29", "a30", "a31", "a32", "a33", "a34", "a35", "a36", "a37", "a38", "a39", "a40", "a41", "a42", "a43", "a44", "a45", "a46", "a47", "a48", "a49", "a50", "a51", "a52", "a53", "a54", "a55", "a56", "a57", "a58", "a59", "a60", "a61", "a62", "a63", "a64", "a65", "a66", "a67", "a68", "a69", "a70", "a71", "a72", "a73", "a74", "a75", "a76", "a77", "a78", "a79", "a80", "a81", "a82", "a83", "a84", "a85", "a86", "a87", "a88", "a89", "a90", "a91", "a92", "a93", "a94", "a95", "a96", "a97", "a98", "a99", "a100", "a101", "a102", "a103", "a104", "a105", "a106", "a107", "a108", "a109", "a110", "a111", "a112", "a113", "a114", "a115", "a116", "a117", "a118", "a119", "a120", "a121", "a122", "a123", "a124", "a125", "a126", "a127", "a128", "a129", "a130", "a131", "a132", "a133", "a134", "a135", "a136", "a137", "a138", "a139", "a140", "a141", "a142", "a143", "a144", "a145", "a146", "a147", "a148", "a149", "a150", "a151", "a152", "a153", "a154", "a155", "a156", "a157", "a158", "a159", "a160", "a161", "a162", "a163", "a164", "a165", "a166", "a167", "a168", "a169", "a170", "a171", "a172", "a173", "a174", "a175", "a176", "a177", "a178", "a179", "a180", "a181", "a182", "a183", "a184", "a185", "a186", "a187", "a188", "a189", "a190", "a191", "a192", "a193", "a194", "a195", "a196", "a197", "a198", "a199", "a200", "a201", "a202", "a203", "a204", "a205", "a206", "a207", "a208", "a209", "a210", "a211", "a212", "a213", "a214", "a215", "a216", "a217", "a218", "a219", "a220", "a221", "a222", "a223", "a224", "a225", "a226", "a227", "a228", "a229", "a230", "a231", "a232", "a233", "a234", "a235", "a236", "a237", "a238", "a239", "a240", "a241", "a242", "a243", "a244", "a245", "a246", "a247", "a248", "a249", "a250", "a251", "a252", "a253", "a254", "a255", "s24", "s62", "s63", "s68", "s69", "s70", "s82", "s83", "vcc", "scc", "memory");
  }
  unsigned stat;
  if (flag) {
    const Geo G(wid0, bid);
    const unsigned wid = G.wid;
    asm volatile("s_waitcnt vmcnt(0)" ::: "memory");
    const unsigned lane = lane_id(), h = G.head();
    for (unsigned qb = 0; qb < 6; ++qb) {
      const OFrag r = slow_block(G.x + (size_t)((wid & 1u) * 96u + qb * 16u) * 640, G.wq + h * 128u, G.k + (size_t)h * 131072, G.v + (size_t)h * 131072, lane);
      const u32x4 of[4] = {r.f0, r.f1, r.f2, r.f3};
#pragma unroll
      for (int s = 0; s < 4; ++s) {
        st16_sc1(G.ofr() + qb * 8192u + s * 1024 + lane * 16u, of[s]);
        *(u32x4*)(lds + 65536u + (((wid & 1u) * 6u + qb) * 8u + (wid >> 1) * 4u + s) * 1024u + lane * 16u) = of[s];
      }
    }
    asm volatile("s_waitcnt vmcnt(0)" ::: "memory");
  }
  asm volatile(
    "s_waitcnt vmcnt(24) lgkmcnt(0)\n"
    "s_barrier\n"
    "s_mov_b32 s81, 0\n"
    "v_mov_b32 v252, 0\n"
    "v_add_u32 v242, 0x10000, v234\n"
    "v_add_u32 v243, 0x10000, v235\n"
    "v_add_u32 v244, 0x20000, v234\n"
    "v_add_u32 v245, 0x20000, v235\n"
    "ds_read_b128 v[0:3], v236 offset:0\n"
    "ds_read_b128 v[4:7], v236 offset:8192\n"
    "ds_read_b128 v[8:11], v236 offset:16384\n"
    "ds_read_b64_tr_b16 a[128:129], v234 offset:0\n"
    "ds_read_b64_tr_b16 a[130:131], v234 offset:5120\n"
    "ds_read_b64_tr_b16 a[132:133], v235 offset:0\n"
    "ds_read_b64_tr_b16 a[134:135], v235 offset:5120\n"
    "ds_read_b64_tr_b16 a[136:137], v234 offset:64\n"
    "ds_read_b64_tr_b16 a[138:139], v234 offset:5184\n"
    "ds_read_b64_tr_b16 a[140:141], v235 offset:64\n"
    "ds_read_b64_tr_b16 a[142:143], v235 offset:5184\n"
    "ds_read_b64_tr_b16 a[144:145], v234 offset:128\n"
    "ds_read_b64_tr_b16 a[146:147], v234 offset:5248\n"
    "ds_read_b64_tr_b16 a[148:149], v235 offset:128\n"
    "ds_read_b64_tr_b16 a[150:151], v235 offset:5248\n"
    "ds_read_b64_tr_b16 a[152:153], v234 offset:192\n"
    "ds_read_b64_tr_b16 a[154:155], v234 offset:5312\n"
    "ds_read_b64_tr_b16 a[156:157], v235 offset:192\n"
    "ds_read_b64_tr_b16 a[158:159], v235 offset:5312\n"
    "ds_read_b64_tr_b16 a[160:161], v234 offset:256\n"
    "ds_read_b64_tr_b16 a[162:163], v234 offset:5376\n"
    "ds_read_b64_tr_b16 a[164:165], v235 offset:256\n"
    "ds_read_b64_tr_b16 a[166:167], v235 offset:5376\n"
    "ds_read_b128 v[12:15], v236 offset:1024\n"
    "ds_read_b128 v[16:19], v236 offset:9216\n"
    "ds_read_b128 v[20:23], v236 offset:17408\n"
    "ds_read_b128 v[24:27], v236 offset:2048\n"
    "ds_read_b128 v[28:31], v236 offset:10240\n"
    "ds_read_b128 v[32:35], v236 offset:18432\n"
    "ds_read_b128 v[36:39], v236 offset:3072\n"
    "ds_read_b128 v[40:43], v236 offset:11264\n"
    "ds_read_b128 v[44:47], v236 offset:19456\n"
    "ds_read_b128 v[48:51], v236 offset:4096\n"
    "ds_read_b128 v[52:55], v236 offset:12288\n"
    "ds_read_b128 v[56:59], v236 offset:20480\n"
    "ds_read_b128 v[60:63], v236 offset:5120\n"
    "ds_read_b128 v[64:67], v236 offset:13312\n"
    "ds_read_b128 v[68:71], v236 offset:21504\n"
    "ds_read_b128 v[72:75], v236 offset:6144\n"
    "ds_read_b128 v[76:79], v236 offset:14336\n"
    "ds_read_b128 v[80:83], v236 offset:22528\n"
    "ds_read_b128 v[84:87], v236 offset:7168\n"
    "ds_read_b128 v[88:91], v236 offset:15360\n"
    "ds_read_b128 v[92:95], v236 offset:23552\n"
    "s_ashr_i32 s63, s28, 31\n"
    "s_add_u32 s40, s56, s28\n"
    "s_addc_u32 s41, s57, s63\n"
    "s_and_b32 s41, s41, 0xffff\n"
    "s_mov_b32 s42, 0x4e000\n"
    "s_mov_b32 s43, s59\n"
    "s_add_u32 s44, s56, s29\n"
    "s_addc_u32 s45, s57, 0\n"
    "s_mov_b32 s46, 0x3c00\n"
    "s_mov_b32 s47, s59\n"
    "s_add_u32 s63, s34, 0\n"
    "s_and_b32 s63, s63, 3\n"
    "s_mul_i32 s64, s63, 0x18000\n"
    "s_mul_i32 s48, s63, 0x50000\n"
    "s_add_u32 s63, s34, 1\n"
    "s_and_b32 s63, s63, 3\n"
    "s_mul_i32 s65, s63, 0x18000\n"
    "s_mul_i32 s49, s63, 0x50000\n"
    "s_add_u32 s63, s34, 2\n"
    "s_and_b32 s63, s63, 3\n"
    "s_mul_i32 s66, s63, 0x18000\n"
    "s_mul_i32 s50, s63, 0x50000\n"
    "s_add_u32 s63, s34, 3\n"
    "s_and_b32 s63, s63, 3\n"
    "s_mul_i32 s67, s63, 0x18000\n"
    "s_mul_i32 s51, s63, 0x50000\n"
    "s_waitcnt lgkmcnt(0)\n"
    "s_barrier\n"
    "v_mfma_f32_16x16x32_bf16 a[0:3], a[128:131], v[0:3], 0\n"
    "ds_read_b64_tr_b16 a[168:169], v234 offset:10240\n"
    "v_mfma_f32_16x16x32_bf16 a[4:7], a[128:131], v[4:7], 0\n"
    "ds_read_b64_tr_b16 a[170:171], v234 offset:15360\n"
    "s_add_u32 m0, s80, 0xf000\n"
    "v_mfma_f32_16x16x32_bf16 a[8:11], a[128:131], v[8:11], 0\n"
    "ds_read_b64_tr_b16 a[172:173], v235 offset:10240\n"
    "s_add_u32 s62, s48, 0x3c000\n"
    "v_mfma_f32_16x16x32_bf16 a[12:15], a[132:135], v[0:3], 0\n"
    "buffer_load_dwordx4 v228, s[36:39], s62 offen lds\n"
    "v_mfma_f32_16x16x32_bf16 a[16:19], a[132:135], v[4:7], 0\n"
    "ds_read_b64_tr_b16 a[174:175], v235 offset:15360\n"
    "v_mfma_f32_16x16x32_bf16 a[20:23], a[132:135], v[8:11], 0\n"
    "ds_read_b64_tr_b16 a[176:177], v234 offset:10304\n"
    "v_mfma_f32_16x16x32_bf16 a[24:27], a[136:139], v[0:3], 0\n"
    "ds_read_b64_tr_b16 a[178:179], v234 offset:15424\n"
    "v_mfma_f32_16x16x32_bf16 a[28:31], a[136:139], v[4:7], 0\n"
    "ds_read_b64_tr_b16 a[180:181], v235 offset:10304\n"
    "v_mfma_f32_16x16x32_bf16 a[32:35], a[136:139], v[8:11], 0\n"
    "buffer_load_dwordx4 v229, s[36:39], s62 offen offset:1024 lds\n"
    "v_mfma_f32_16x16x32_bf16 a[36:39], a[140:143], v[0:3], 0\n"
    "ds_read_b64_tr_b16 a[182:183], v235 offset:15424\n"
    "v_mfma_f32_16x16x32_bf16 a[40:43], a[140:143], v[4:7], 0\n"
    "ds_read_b64_tr_b16 a[184:185], v234 offset:10368\n"
    "v_mfma_f32_16x16x32_bf16 a[44:47], a[140:143], v[8:11], 0\n"
    "ds_read_b64_tr_b16 a[186:187], v234 offset:15488\n"
    "v_mfma_f32_16x16x32_bf16 a[48:51], a[144:147], v[0:3], 0\n"
    "ds_read_b64_tr_b16 a[188:189], v235 offset:10368\n"
    "v_mfma_f32_16x16x32_bf16 a[52:55], a[144:147], v[4:7], 0\n"
    "buffer_load_dwordx4 v230, s[36:39], s62 offen offset:2048 lds\n"
    "v_mfma_f32_16x16x32_bf16 a[56:59], a[144:147], v[8:11], 0\n"
    "ds_read_b64_tr_b16 a[190:191], v235 offset:15488\n"
    "v_mfma_f32_16x16x32_bf16 a[60:63], a[148:151], v[0:3], 0\n"
    "ds_read_b64_tr_b16 a[192:193], v234 offset:10432\n"
    "v_mfma_f32_16x16x32_bf16 a[64:67], a[148:151], v[4:7], 0\n"
    "ds_read_b64_tr_b16 a[194:195], v234 offset:15552\n"
    "v_mfma_f32_16x16x32_bf16 a[68:71], a[148:151], v[8:11], 0\n"
    "ds_read_b64_tr_b16 a[196:197], v235 offset:10432\n"
    "v_mfma_f32_16x16x32_bf16 a[72:75], a[152:155], v[0:3], 0\n"
    "buffer_load_dwordx4 v231, s[36:39], s62 offen offset:3072 lds\n"
    "v_mfma_f32_16x16x32_bf16 a[76:79], a[152:155], v[4:7], 0\n"
    "ds_read_b64_tr_b16 a[198:199], v235 offset:15552\n"
    "v_mfma_f32_16x16x32_bf16 a[80:83], a[152:155], v[8:11], 0\n"
    "ds_read_b64_tr_b16 a[200:201], v234 offset:10496\n"
    "v_mfma_f32_16x16x32_bf16 a[84:87], a[156:159], v[0:3], 0\n"
    "ds_read_b64_tr_b16 a[202:203], v234 offset:15616\n"
    "v_mfma_f32_16x16x32_bf16 a[88:91], a[156:159], v[4:7], 0\n"
    "ds_read_b64_tr_b16 a[204:205], v235 offset:10496\n"
    "s_add_u32 m0, s80, 0x10000\n"
    "v_mfma_f32_16x16x32_bf16 a[92:95], a[156:159], v[8:11], 0\n"
    "buffer_load_dwordx4 v232, s[36:39], s62 offen lds\n"
    "v_mfma_f32_16x16x32_bf16 a[96:99], a[160:163], v[0:3], 0\n"
    "ds_read_b64_tr_b16 a[206:207], v235 offset:15616\n"
    "v_mfma_f32_16x16x32_bf16 a[100:103], a[160:163], v[4:7], 0\n"
    "s_add_u32 m0, s80, 0x14000\n"
    "v_mfma_f32_16x16x32_bf16 a[104:107], a[160:163], v[8:11], 0\n"
    "s_add_u32 s62, s49, 0x0\n"
    "v_mfma_f32_16x16x32_bf16 a[108:111], a[164:167], v[0:3], 0\n"
    "buffer_load_dwordx4 v228, s[36:39], s62 offen lds\n"
    "v_mfma_f32_16x16x32_bf16 a[112:115], a[164:167], v[4:7], 0\n"
    "v_mfma_f32_16x16x32_bf16 a[116:119], a[164:167], v[8:11], 0\n"
    "s_waitcnt lgkmcnt(0)\n"
    "v_mfma_f32_16x16x32_bf16 a[0:3], a[168:171], v[12:15], a[0:3]\n"
    "s_waitcnt vmcnt(6)\n"
    "s_barrier\n"
    "s_cmp_lg_u32 s77, 0\n"
    "s_cbranch_scc1 LA%=\n"
    "v_mov_b32 v253, 1\n"
    "s_mov_b64 exec, 1\n"
    "global_atomic_add v252, v253, s[78:79]\n"
    "s_mov_b64 exec, -1\n"
    "s_nop 3\n"
    "LA%=:\n"
    "v_mfma_f32_16x16x32_bf16 a[4:7], a[168:171], v[16:19], a[4:7]\n"
    "ds_read_b64_tr_b16 a[128:129], v234 offset:20480\n"
    "v_mfma_f32_16x16x32_bf16 a[8:11], a[168:171], v[20:23], a[8:11]\n"
    "ds_read_b64_tr_b16 a[130:131], v234 offset:25600\n"
    "v_mfma_f32_16x16x32_bf16 a[12:15], a[172:175], v[12:15], a[12:15]\n"
    "buffer_load_dwordx4 v229, s[36:39], s62 offen offset:1024 lds\n"
    "v_mfma_f32_16x16x32_bf16 a[16:19], a[172:175], v[16:19], a[16:19]\n"
    "ds_read_b64_tr_b16 a[132:133], v235 offset:20480\n"
    "v_mfma_f32_16x16x32_bf16 a[20:23], a[172:175], v[20:23], a[20:23]\n"
    "ds_read_b64_tr_b16 a[134:135], v235 offset:25600\n"
    "v_mfma_f32_16x16x32_bf16 a[24:27], a[176:179], v[12:15], a[24:27]\n"
    "ds_read_b64_tr_b16 a[136:137], v234 offset:20544\n"
    "v_mfma_f32_16x16x32_bf16 a[28:31], a[176:179], v[16:19], a[28:31]\n"
    "ds_read_b64_tr_b16 a[138:139], v234 offset:25664\n"
    "v_mfma_f32_16x16x32_bf16 a[32:35], a[176:179], v[20:23], a[32:35]\n"
    "buffer_load_dwordx4 v230, s[36:39], s62 offen offset:2048 lds\n"
    "v_mfma_f32_16x16x32_bf16 a[36:39], a[180:183], v[12:15], a[36:39]\n"
    "ds_read_b64_tr_b16 a[140:141], v235 offset:20544\n"
    "v_mfma_f32_16x16x32_bf16 a[40:43], a[180:183], v[16:19], a[40:43]\n"
    "ds_read_b64_tr_b16 a[142:143], v235 offset:25664\n"
    "v_mfma_f32_16x16x32_bf16 a[44:47], a[180:183], v[20:23], a[44:47]\n"
    "ds_read_b64_tr_b16 a[144:145], v234 offset:20608\n"
    "v_mfma_f32_16x16x32_bf16 a[48:51], a[184:187], v[12:15], a[48:51]\n"
    "ds_read_b64_tr_b16 a[146:147], v234 offset:25728\n"
    "v_mfma_f32_16x16x32_bf16 a[52:55], a[184:187], v[16:19], a[52:55]\n"
    "buffer_load_dwordx4 v231, s[36:39], s62 offen offset:3072 lds\n"
    "v_mfma_f32_16x16x32_bf16 a[56:59], a[184:187], v[20:23], a[56:59]\n"
    "ds_read_b64_tr_b16 a[148:149], v235 offset:20608\n"
    "v_mfma_f32_16x16x32_bf16 a[60:63], a[188:191], v[12:15], a[60:63]\n"
    "ds_read_b64_tr_b16 a[150:151], v235 offset:25728\n"
    "v_mfma_f32_16x16x32_bf16 a[64:67], a[188:191], v[16:19], a[64:67]\n"
    "ds_read_b64_tr_b16 a[152:153], v234 offset:20672\n"
    "v_mfma_f32_16x16x32_bf16 a[68:71], a[188:191], v[20:23], a[68:71]\n"
    "ds_read_b64_tr_b16 a[154:155], v234 offset:25792\n"
    "s_add_u32 m0, s80, 0x15000\n"
    "v_mfma_f32_16x16x32_bf16 a[72:75], a[192:195], v[12:15], a[72:75]\n"
    "buffer_load_dwordx4 v232, s[36:39], s62 offen lds\n"
    "v_mfma_f32_16x16x32_bf16 a[76:79], a[192:195], v[16:19], a[76:79]\n"
    "ds_read_b64_tr_b16 a[156:157], v235 offset:20672\n"
    "v_mfma_f32_16x16x32_bf16 a[80:83], a[192:195], v[20:23], a[80:83]\n"
    "ds_read_b64_tr_b16 a[158:159], v235 offset:25792\n"
    "v_mfma_f32_16x16x32_bf16 a[84:87], a[196:199], v[12:15], a[84:87]\n"
    "ds_read_b64_tr_b16 a[160:161], v234 offset:20736\n"
    "s_add_u32 m0, s80, 0x19000\n"
    "v_mfma_f32_16x16x32_bf16 a[88:91], a[196:199], v[16:19], a[88:91]\n"
    "ds_read_b64_tr_b16 a[162:163], v234 offset:25856\n"
    "s_add_u32 s62, s49, 0x14000\n"
    "v_mfma_f32_16x16x32_bf16 a[92:95], a[196:199], v[20:23], a[92:95]\n"
    "buffer_load_dwordx4 v228, s[36:39], s62 offen lds\n"
    "v_mfma_f32_16x16x32_bf16 a[96:99], a[200:203], v[12:15], a[96:99]\n"
    "ds_read_b64_tr_b16 a[164:165], v235 offset:20736\n"
    "v_mfma_f32_16x16x32_bf16 a[100:103], a[200:203], v[16:19], a[100:103]\n"
    "ds_read_b64_tr_b16 a[166:167], v235 offset:25856\n"
    "v_mfma_f32_16x16x32_bf16 a[104:107], a[200:203], v[20:23], a[104:107]\n"
    "v_mfma_f32_16x16x32_bf16 a[108:111], a[204:207], v[12:15], a[108:111]\n"
    "buffer_load_dwordx4 v229, s[36:39], s62 offen offset:1024 lds\n"
    "v_mfma_f32_16x16x32_bf16 a[112:115], a[204:207], v[16:19], a[112:115]\n"
    "v_mfma_f32_16x16x32_bf16 a[116:119], a[204:207], v[20:23], a[116:119]\n"
    "s_waitcnt lgkmcnt(0)\n"
    "v_mfma_f32_16x16x32_bf16 a[0:3], a[128:131], v[24:27], a[0:3]\n"
    "ds_read_b64_tr_b16 a[168:169], v234 offset:30720\n"
    "v_mfma_f32_16x16x32_bf16 a[4:7], a[128:131], v[28:31], a[4:7]\n"
    "ds_read_b64_tr_b16 a[170:171], v234 offset:35840\n"
    "v_mfma_f32_16x16x32_bf16 a[8:11], a[128:131], v[32:35], a[8:11]\n"
    "ds_read_b64_tr_b16 a[172:173], v235 offset:30720\n"
    "v_mfma_f32_16x16x32_bf16 a[12:15], a[132:135], v[24:27], a[12:15]\n"
    "buffer_load_dwordx4 v230, s[36:39], s62 offen offset:2048 lds\n"
    "v_mfma_f32_16x16x32_bf16 a[16:19], a[132:135], v[28:31], a[16:19]\n"
    "ds_read_b64_tr_b16 a[174:175], v235 offset:35840\n"
    "v_mfma_f32_16x16x32_bf16 a[20:23], a[132:135], v[32:35], a[20:23]\n"
    "ds_read_b64_tr_b16 a[176:177], v234 offset:30784\n"
    "v_mfma_f32_16x16x32_bf16 a[24:27], a[136:139], v[24:27], a[24:27]\n"
    "ds_read_b64_tr_b16 a[178:179], v234 offset:35904\n"
    "v_mfma_f32_16x16x32_bf16 a[28:31], a[136:139], v[28:31], a[28:31]\n"
    "ds_read_b64_tr_b16 a[180:181], v235 offset:30784\n"
    "v_mfma_f32_16x16x32_bf16 a[32:35], a[136:139], v[32:35], a[32:35]\n"
    "buffer_load_dwordx4 v231, s[36:39], s62 offen offset:3072 lds\n"
    "v_mfma_f32_16x16x32_bf16 a[36:39], a[140:143], v[24:27], a[36:39]\n"
    "ds_read_b64_tr_b16 a[182:183], v235 offset:35904\n"
    "v_mfma_f32_16x16x32_bf16 a[40:43], a[140:143], v[28:31], a[40:43]\n"
    "ds_read_b64_tr_b16 a[184:185], v234 offset:30848\n"
    "v_mfma_f32_16x16x32_bf16 a[44:47], a[140:143], v[32:35], a[44:47]\n"
    "ds_read_b64_tr_b16 a[186:187], v234 offset:35968\n"
    "v_mfma_f32_16x16x32_bf16 a[48:51], a[144:147], v[24:27], a[48:51]\n"
    "ds_read_b64_tr_b16 a[188:189], v235 offset:30848\n"
    "s_add_u32 m0, s80, 0x1a000\n"
    "v_mfma_f32_16x16x32_bf16 a[52:55], a[144:147], v[28:31], a[52:55]\n"
    "buffer_load_dwordx4 v232, s[36:39], s62 offen lds\n"
    "v_mfma_f32_16x16x32_bf16 a[56:59], a[144:147], v[32:35], a[56:59]\n"
    "ds_read_b64_tr_b16 a[190:191], v235 offset:35968\n"
    "v_mfma_f32_16x16x32_bf16 a[60:63], a[148:151], v[24:27], a[60:63]\n"
    "ds_read_b64_tr_b16 a[192:193], v234 offset:30912\n"
    "v_mfma_f32_16x16x32_bf16 a[64:67], a[148:151], v[28:31], a[64:67]\n"
    "ds_read_b64_tr_b16 a[194:195], v234 offset:36032\n"
    "s_add_u32 m0, s80, 0x1e000\n"
    "v_mfma_f32_16x16x32_bf16 a[68:71], a[148:151], v[32:35], a[68:71]\n"
    "ds_read_b64_tr_b16 a[196:197], v235 offset:30912\n"
    "s_add_u32 s62, s49, 0x28000\n"
    "v_mfma_f32_16x16x32_bf16 a[72:75], a[152:155], v[24:27], a[72:75]\n"
    "buffer_load_dwordx4 v228, s[36:39], s62 offen lds\n"
    "v_mfma_f32_16x16x32_bf16 a[76:79], a[152:155], v[28:31], a[76:79]\n"
    "ds_read_b64_tr_b16 a[198:199], v235 offset:36032\n"
    "v_mfma_f32_16x16x32_bf16 a[80:83], a[152:155], v[32:35], a[80:83]\n"
    "ds_read_b64_tr_b16 a[200:201], v234 offset:30976\n"
    "v_mfma_f32_16x16x32_bf16 a[84:87], a[156:159], v[24:27], a[84:87]\n"
    "ds_read_b64_tr_b16 a[202:203], v234 offset:36096\n"
    "v_mfma_f32_16x16x32_bf16 a[88:91], a[156:159], v[28:31], a[88:91]\n"
    "ds_read_b64_tr_b16 a[204:205], v235 offset:30976\n"
    "v_mfma_f32_16x16x32_bf16 a[92:95], a[156:159], v[32:35], a[92:95]\n"
    "buffer_load_dwordx4 v229, s[36:39], s62 offen offset:1024 lds\n"
    "v_mfma_f32_16x16x32_bf16 a[96:99], a[160:163], v[24:27], a[96:99]\n"
    "ds_read_b64_tr_b16 a[206:207], v235 offset:36096\n"
    "v_mfma_f32_16x16x32_bf16 a[100:103], a[160:163], v[28:31], a[100:103]\n"
    "v_mfma_f32_16x16x32_bf16 a[104:107], a[160:163], v[32:35], a[104:107]\n"
    "v_mfma_f32_16x16x32_bf16 a[108:111], a[164:167], v[24:27], a[108:111]\n"
    "buffer_load_dwordx4 v230, s[36:39], s62 offen offset:2048 lds\n"
    "v_mfma_f32_16x16x32_bf16 a[112:115], a[164:167], v[28:31], a[112:115]\n"
    "v_mfma_f32_16x16x32_bf16 a[116:119], a[164:167], v[32:35], a[116:119]\n"
    "s_waitcnt lgkmcnt(0)\n"
    "v_mfma_f32_16x16x32_bf16 a[0:3], a[168:171], v[36:39], a[0:3]\n"
    "s_barrier\n"
    "v_mfma_f32_16x16x32_bf16 a[4:7], a[168:171], v[40:43], a[4:7]\n"
    "ds_read_b64_tr_b16 a[128:129], v234 offset:40960\n"
    "v_mfma_f32_16x16x32_bf16 a[8:11], a[168:171], v[44:47], a[8:11]\n"
    "ds_read_b64_tr_b16 a[130:131], v234 offset:46080\n"
    "v_mfma_f32_16x16x32_bf16 a[12:15], a[172:175], v[36:39], a[12:15]\n"
    "buffer_load_dwordx4 v231, s[36:39], s62 offen offset:3072 lds\n"
    "v_mfma_f32_16x16x32_bf16 a[16:19], a[172:175], v[40:43], a[16:19]\n"
    "ds_read_b64_tr_b16 a[132:133], v235 offset:40960\n"
    "v_mfma_f32_16x16x32_bf16 a[20:23], a[172:175], v[44:47], a[20:23]\n"
    "ds_read_b64_tr_b16 a[134:135], v235 offset:46080\n"
    "v_mfma_f32_16x16x32_bf16 a[24:27], a[176:179], v[36:39], a[24:27]\n"
    "ds_read_b64_tr_b16 a[136:137], v234 offset:41024\n"
    "v_mfma_f32_16x16x32_bf16 a[28:31], a[176:179], v[40:43], a[28:31]\n"
    "ds_read_b64_tr_b16 a[138:139], v234 offset:46144\n"
    "s_add_u32 m0, s80, 0x1f000\n"
    "v_mfma_f32_16x16x32_bf16 a[32:35], a[176:179], v[44:47], a[32:35]\n"
    "buffer_load_dwordx4 v232, s[36:39], s62 offen lds\n"
    "v_mfma_f32_16x16x32_bf16 a[36:39], a[180:183], v[36:39], a[36:39]\n"
    "ds_read_b64_tr_b16 a[140:141], v235 offset:41024\n"
    "v_mfma_f32_16x16x32_bf16 a[40:43], a[180:183], v[40:43], a[40:43]\n"
    "ds_read_b64_tr_b16 a[142:143], v235 offset:46144\n"
    "v_mfma_f32_16x16x32_bf16 a[44:47], a[180:183], v[44:47], a[44:47]\n"
    "ds_read_b64_tr_b16 a[144:145], v234 offset:41088\n"
    "s_add_u32 m0, s80, 0x23000\n"
    "v_mfma_f32_16x16x32_bf16 a[48:51], a[184:187], v[36:39], a[48:51]\n"
    "ds_read_b64_tr_b16 a[146:147], v234 offset:46208\n"
    "s_add_u32 s62, s49, 0x3c000\n"
    "v_mfma_f32_16x16x32_bf16 a[52:55], a[184:187], v[40:43], a[52:55]\n"
    "buffer_load_dwordx4 v228, s[36:39], s62 offen lds\n"
    "v_mfma_f32_16x16x32_bf16 a[56:59], a[184:187], v[44:47], a[56:59]\n"
    "ds_read_b64_tr_b16 a[148:149], v235 offset:41088\n"
    "v_mfma_f32_16x16x32_bf16 a[60:63], a[188:191], v[36:39], a[60:63]\n"
    "ds_read_b64_tr_b16 a[150:151], v235 offset:46208\n"
    "v_mfma_f32_16x16x32_bf16 a[64:67], a[188:191], v[40:43], a[64:67]\n"
    "global_load_dword v253, v252, s[78:79] sc1\n"
    "v_mfma_f32_16x16x32_bf16 a[68:71], a[188:191], v[44:47], a[68:71]\n"
    "ds_read_b64_tr_b16 a[152:153], v234 offset:41152\n"
    "v_mfma_f32_16x16x32_bf16 a[72:75], a[192:195], v[36:39], a[72:75]\n"
    "buffer_load_dwordx4 v229, s[36:39], s62 offen offset:1024 lds\n"
    "v_mfma_f32_16x16x32_bf16 a[76:79], a[192:195], v[40:43], a[76:79]\n"
    "ds_read_b64_tr_b16 a[154:155], v234 offset:46272\n"
    "v_mfma_f32_16x16x32_bf16 a[80:83], a[192:195], v[44:47], a[80:83]\n"
    "ds_read_b64_tr_b16 a[156:157], v235 offset:41152\n"
    "v_mfma_f32_16x16x32_bf16 a[84:87], a[196:199], v[36:39], a[84:87]\n"
    "ds_read_b64_tr_b16 a[158:159], v235 offset:46272\n"
    "v_mfma_f32_16x16x32_bf16 a[88:91], a[196:199], v[40:43], a[88:91]\n"
    "ds_read_b64_tr_b16 a[160:161], v234 offset:41216\n"
    "v_mfma_f32_16x16x32_bf16 a[92:95], a[196:199], v[44:47], a[92:95]\n"
    "buffer_load_dwordx4 v230, s[36:39], s62 offen offset:2048 lds\n"
    "v_mfma_f32_16x16x32_bf16 a[96:99], a[200:203], v[36:39], a[96:99]\n"
    "ds_read_b64_tr_b16 a[162:163], v234 offset:46336\n"
    "v_mfma_f32_16x16x32_bf16 a[100:103], a[200:203], v[40:43], a[100:103]\n"
    "ds_read_b64_tr_b16 a[164:165], v235 offset:41216\n"
    "v_mfma_f32_16x16x32_bf16 a[104:107], a[200:203], v[44:47], a[104:107]\n"
    "ds_read_b64_tr_b16 a[166:167], v235 offset:46336\n"
    "v_mfma_f32_16x16x32_bf16 a[108:111], a[204:207], v[36:39], a[108:111]\n"
    "buffer_load_dwordx4 v231, s[36:39], s62 offen offset:3072 lds\n"
    "v_mfma_f32_16x16x32_bf16 a[112:115], a[204:207], v[40:43], a[112:115]\n"
    "v_mfma_f32_16x16x32_bf16 a[116:119], a[204:207], v[44:47], a[116:119]\n"
    "s_waitcnt lgkmcnt(0)\n"
    "v_mfma_f32_16x16x32_bf16 a[0:3], a[128:131], v[48:51], a[0:3]\n"
    "s_waitcnt vmcnt(3)\n"
    "v_readfirstlane_b32 s63, v253\n"
    "s_nop 1\n"
    "s_sub_u32 s63, s63, s85\n"
    "s_cmp_eq_u32 s63, 4\n"
    "s_cbranch_scc1 LG%=\n"
    "s_memrealtime s[72:73]\n"
    "LP%=:\n"
    "global_load_dword v253, v252, s[78:79] sc1\n"
    "s_memrealtime s[74:75]\n"
    "s_waitcnt vmcnt(0) lgkmcnt(0)\n"
    "v_readfirstlane_b32 s63, v253\n"
    "s_sub_u32 s74, s74, s72\n"
    "s_nop 1\n"
    "s_sub_u32 s63, s63, s85\n"
    "s_cmp_eq_u32 s63, 4\n"
    "s_cbranch_scc1 LG%=\n"
    "s_cmp_lt_u32 s74, 0x200000\n"
    "s_cbranch_scc1 LP%=\n"
    "s_mov_b32 s81, 1\n"
    "s_mov_b32 s46, 0\n"
    "LG%=:\n"
    "v_mfma_f32_16x16x32_bf16 a[4:7], a[128:131], v[52:55], a[4:7]\n"
    "ds_read_b64_tr_b16 a[168:169], v234 offset:51200\n"
    "s_add_u32 s68, s65, 0x0\n"
    "v_mfma_f32_16x16x32_bf16 a[8:11], a[128:131], v[56:59], a[8:11]\n"
    "buffer_load_dwordx4 v[96:99], v248, s[40:43], s68 offen sc1\n"
    "s_add_u32 s69, s65, 0x2000\n"
    "v_mfma_f32_16x16x32_bf16 a[12:15], a[132:135], v[48:51], a[12:15]\n"
    "buffer_load_dwordx4 v[100:103], v248, s[40:43], s69 offen sc1\n"
    "s_add_u32 s70, s65, 0x4000\n"
    "v_mfma_f32_16x16x32_bf16 a[16:19], a[132:135], v[52:55], a[16:19]\n"
    "buffer_load_dwordx4 v[104:107], v248, s[40:43], s70 offen sc1\n"
    "v_mfma_f32_16x16x32_bf16 a[20:23], a[132:135], v[56:59], a[20:23]\n"
    "ds_read_b64_tr_b16 a[170:171], v234 offset:56320\n"
    "v_mfma_f32_16x16x32_bf16 a[24:27], a[136:139], v[48:51], a[24:27]\n"
    "ds_read_b64_tr_b16 a[172:173], v235 offset:51200\n"
    "v_mfma_f32_16x16x32_bf16 a[28:31], a[136:139], v[52:55], a[28:31]\n"
    "buffer_load_dwordx4 v[108:111], v248, s[40:43], s68 offen offset:1024 sc1\n"
    "v_mfma_f32_16x16x32_bf16 a[32:35], a[136:139], v[56:59], a[32:35]\n"
    "ds_read_b64_tr_b16 a[174:175], v235 offset:56320\n"
    "v_mfma_f32_16x16x32_bf16 a[36:39], a[140:143], v[48:51], a[36:39]\n"
    "ds_read_b64_tr_b16 a[176:177], v234 offset:51264\n"
    "v_mfma_f32_16x16x32_bf16 a[40:43], a[140:143], v[52:55], a[40:43]\n"
    "buffer_load_dwordx4 v[112:115], v248, s[40:43], s69 offen offset:1024 sc1\n"
    "v_mfma_f32_16x16x32_bf16 a[44:47], a[140:143], v[56:59], a[44:47]\n"
    "ds_read_b64_tr_b16 a[178:179], v234 offset:56384\n"
    "v_mfma_f32_16x16x32_bf16 a[48:51], a[144:147], v[48:51], a[48:51]\n"
    "ds_read_b64_tr_b16 a[180:181], v235 offset:51264\n"
    "v_mfma_f32_16x16x32_bf16 a[52:55], a[144:147], v[52:55], a[52:55]\n"
    "buffer_load_dwordx4 v[116:119], v248, s[40:43], s70 offen offset:1024 sc1\n"
    "v_mfma_f32_16x16x32_bf16 a[56:59], a[144:147], v[56:59], a[56:59]\n"
    "ds_read_b64_tr_b16 a[182:183], v235 offset:56384\n"
    "v_mfma_f32_16x16x32_bf16 a[60:63], a[148:151], v[48:51], a[60:63]\n"
    "ds_read_b64_tr_b16 a[184:185], v234 offset:51328\n"
    "v_mfma_f32_16x16x32_bf16 a[64:67], a[148:151], v[52:55], a[64:67]\n"
    "buffer_load_dwordx4 v[120:123], v248, s[40:43], s68 offen offset:2048 sc1\n"
    "v_mfma_f32_16x16x32_bf16 a[68:71], a[148:151], v[56:59], a[68:71]\n"
    "ds_read_b64_tr_b16 a[186:187], v234 offset:56448\n"
    "v_mfma_f32_16x16x32_bf16 a[72:75], a[152:155], v[48:51], a[72:75]\n"
    "ds_read_b64_tr_b16 a[188:189], v235 offset:51328\n"
    "v_mfma_f32_16x16x32_bf16 a[76:79], a[152:155], v[52:55], a[76:79]\n"
    "ds_read_b64_tr_b16 a[190:191], v235 offset:56448\n"
    "v_mfma_f32_16x16x32_bf16 a[80:83], a[152:155], v[56:59], a[80:83]\n"
    "buffer_load_dwordx4 v[124:127], v248, s[40:43], s69 offen offset:2048 sc1\n"
    "v_mfma_f32_16x16x32_bf16 a[84:87], a[156:159], v[48:51], a[84:87]\n"
    "ds_read_b64_tr_b16 a[192:193], v234 offset:51392\n"
    "v_mfma_f32_16x16x32_bf16 a[88:91], a[156:159], v[52:55], a[88:91]\n"
    "ds_read_b64_tr_b16 a[194:195], v234 offset:56512\n"
    "v_mfma_f32_16x16x32_bf16 a[92:95], a[156:159], v[56:59], a[92:95]\n"
    "ds_read_b64_tr_b16 a[196:197], v235 offset:51392\n"
    "v_mfma_f32_16x16x32_bf16 a[96:99], a[160:163], v[48:51], a[96:99]\n"
    "buffer_load_dwordx4 v[128:131], v248, s[40:43], s70 offen offset:2048 sc1\n"
    "v_mfma_f32_16x16x32_bf16 a[100:103], a[160:163], v[52:55], a[100:103]\n"
    "ds_read_b64_tr_b16 a[198:199], v235 offset:56512\n"
    "v_mfma_f32_16x16x32_bf16 a[104:107], a[160:163], v[56:59], a[104:107]\n"
    "ds_read_b64_tr_b16 a[200:201], v234 offset:51456\n"
    "v_mfma_f32_16x16x32_bf16 a[108:111], a[164:167], v[48:51], a[108:111]\n"
    "ds_read_b64_tr_b16 a[202:203], v234 offset:56576\n"
    "v_mfma_f32_16x16x32_bf16 a[112:115], a[164:167], v[52:55], a[112:115]\n"
    "ds_read_b64_tr_b16 a[204:205], v235 offset:51456\n"
    "v_mfma_f32_16x16x32_bf16 a[116:119], a[164:167], v[56:59], a[116:119]\n"
    "ds_read_b64_tr_b16 a[206:207], v235 offset:56576\n"
    "s_waitcnt lgkmcnt(0)\n"
    "v_mfma_f32_16x16x32_bf16 a[0:3], a[168:171], v[60:63], a[0:3]\n"
    "s_barrier\n"
    "v_mfma_f32_16x16x32_bf16 a[4:7], a[168:171], v[64:67], a[4:7]\n"
    "ds_read_b64_tr_b16 a[128:129], v234 offset:61440\n"
    "v_mfma_f32_16x16x32_bf16 a[8:11], a[168:171], v[68:71], a[8:11]\n"
    "ds_read_b64_tr_b16 a[130:131], v242 offset:1024\n"
    "v_mfma_f32_16x16x32_bf16 a[12:15], a[172:175], v[60:63], a[12:15]\n"
    "buffer_load_dwordx4 v[132:135], v248, s[40:43], s68 offen offset:3072 sc1\n"
    "v_mfma_f32_16x16x32_bf16 a[16:19], a[172:175], v[64:67], a[16:19]\n"
    "ds_read_b64_tr_b16 a[132:133], v235 offset:61440\n"
    "v_mfma_f32_16x16x32_bf16 a[20:23], a[172:175], v[68:71], a[20:23]\n"
    "ds_read_b64_tr_b16 a[134:135], v243 offset:1024\n"
    "v_mfma_f32_16x16x32_bf16 a[24:27], a[176:179], v[60:63], a[24:27]\n"
    "ds_read_b64_tr_b16 a[136:137], v234 offset:61504\n"
    "v_mfma_f32_16x16x32_bf16 a[28:31], a[176:179], v[64:67], a[28:31]\n"
    "ds_read_b64_tr_b16 a[138:139], v242 offset:1088\n"
    "v_mfma_f32_16x16x32_bf16 a[32:35], a[176:179], v[68:71], a[32:35]\n"
    "v_mfma_f32_16x16x32_bf16 a[36:39], a[180:183], v[60:63], a[36:39]\n"
    "ds_read_b64_tr_b16 a[140:141], v235 offset:61504\n"
    "v_mfma_f32_16x16x32_bf16 a[40:43], a[180:183], v[64:67], a[40:43]\n"
    "ds_read_b64_tr_b16 a[142:143], v243 offset:1088\n"
    "v_mfma_f32_16x16x32_bf16 a[44:47], a[180:183], v[68:71], a[44:47]\n"
    "ds_read_b64_tr_b16 a[144:145], v234 offset:61568\n"
    "v_mfma_f32_16x16x32_bf16 a[48:51], a[184:187], v[60:63], a[48:51]\n"
    "ds_read_b64_tr_b16 a[146:147], v242 offset:1152\n"
    "v_mfma_f32_16x16x32_bf16 a[52:55], a[184:187], v[64:67], a[52:55]\n"
    "buffer_load_dwordx4 v[136:139], v248, s[40:43], s69 offen offset:3072 sc1\n"
    "v_mfma_f32_16x16x32_bf16 a[56:59], a[184:187], v[68:71], a[56:59]\n"
    "ds_read_b64_tr_b16 a[148:149], v235 offset:61568\n"
    "v_mfma_f32_16x16x32_bf16 a[60:63], a[188:191], v[60:63], a[60:63]\n"
    "ds_read_b64_tr_b16 a[150:151], v243 offset:1152\n"
    "v_mfma_f32_16x16x32_bf16 a[64:67], a[188:191], v[64:67], a[64:67]\n"
    "ds_read_b64_tr_b16 a[152:153], v234 offset:61632\n"
    "v_mfma_f32_16x16x32_bf16 a[68:71], a[188:191], v[68:71], a[68:71]\n"
    "ds_read_b64_tr_b16 a[154:155], v242 offset:1216\n"
    "v_mfma_f32_16x16x32_bf16 a[72:75], a[192:195], v[60:63], a[72:75]\n"
    "v_mfma_f32_16x16x32_bf16 a[76:79], a[192:195], v[64:67], a[76:79]\n"
    "ds_read_b64_tr_b16 a[156:157], v235 offset:61632\n"
    "v_mfma_f32_16x16x32_bf16 a[80:83], a[192:195], v[68:71], a[80:83]\n"
    "ds_read_b64_tr_b16 a[158:159], v243 offset:1216\n"
    "v_mfma_f32_16x16x32_bf16 a[84:87], a[196:199], v[60:63], a[84:87]\n"
    "ds_read_b64_tr_b16 a[160:161], v234 offset:61696\n"
    "v_mfma_f32_16x16x32_bf16 a[88:91], a[196:199], v[64:67], a[88:91]\n"
    "ds_read_b64_tr_b16 a[162:163], v242 offset:1280\n"
    "v_mfma_f32_16x16x32_bf16 a[92:95], a[196:199], v[68:71], a[92:95]\n"
    "buffer_load_dwordx4 v[140:143], v248, s[40:43], s70 offen offset:3072 sc1\n"
    "v_mfma_f32_16x16x32_bf16 a[96:99], a[200:203], v[60:63], a[96:99]\n"
    "ds_read_b64_tr_b16 a[164:165], v235 offset:61696\n"
    "v_mfma_f32_16x16x32_bf16 a[100:103], a[200:203], v[64:67], a[100:103]\n"
    "ds_read_b64_tr_b16 a[166:167], v243 offset:1280\n"
    "v_mfma_f32_16x16x32_bf16 a[104:107], a[200:203], v[68:71], a[104:107]\n"
    "v_mfma_f32_16x16x32_bf16 a[108:111], a[204:207], v[60:63], a[108:111]\n"
    "v_mfma_f32_16x16x32_bf16 a[112:115], a[204:207], v[64:67], a[112:115]\n"
    "v_mfma_f32_16x16x32_bf16 a[116:119], a[204:207], v[68:71], a[116:119]\n"
    "s_waitcnt lgkmcnt(0)\n"
    "v_mfma_f32_16x16x32_bf16 a[0:3], a[128:131], v[72:75], a[0:3]\n"
    "ds_read_b64_tr_b16 a[168:169], v242 offset:6144\n"
    "v_mfma_f32_16x16x32_bf16 a[4:7], a[128:131], v[76:79], a[4:7]\n"
    "ds_read_b64_tr_b16 a[170:171], v242 offset:11264\n"
    "v_mfma_f32_16x16x32_bf16 a[8:11], a[128:131], v[80:83], a[8:11]\n"
    "ds_read_b64_tr_b16 a[172:173], v243 offset:6144\n"
    "s_add_u32 m0, s80, 0x24000\n"
    "v_mfma_f32_16x16x32_bf16 a[12:15], a[132:135], v[72:75], a[12:15]\n"
    "buffer_load_dwordx4 v232, s[36:39], s62 offen lds\n"
    "v_mfma_f32_16x16x32_bf16 a[16:19], a[132:135], v[76:79], a[16:19]\n"
    "ds_read_b64_tr_b16 a[174:175], v243 offset:11264\n"
    "v_mfma_f32_16x16x32_bf16 a[20:23], a[132:135], v[80:83], a[20:23]\n"
    "ds_read_b64_tr_b16 a[176:177], v242 offset:6208\n"
    "v_mfma_f32_16x16x32_bf16 a[24:27], a[136:139], v[72:75], a[24:27]\n"
    "ds_read_b64_tr_b16 a[178:179], v242 offset:11328\n"
    "v_mfma_f32_16x16x32_bf16 a[28:31], a[136:139], v[76:79], a[28:31]\n"
    "ds_read_b64_tr_b16 a[180:181], v243 offset:6208\n"
    "v_mfma_f32_16x16x32_bf16 a[32:35], a[136:139], v[80:83], a[32:35]\n"
    "v_mfma_f32_16x16x32_bf16 a[36:39], a[140:143], v[72:75], a[36:39]\n"
    "ds_read_b64_tr_b16 a[182:183], v243 offset:11328\n"
    "v_mfma_f32_16x16x32_bf16 a[40:43], a[140:143], v[76:79], a[40:43]\n"
    "ds_read_b64_tr_b16 a[184:185], v242 offset:6272\n"
    "v_mfma_f32_16x16x32_bf16 a[44:47], a[140:143], v[80:83], a[44:47]\n"
    "ds_read_b64_tr_b16 a[186:187], v242 offset:11392\n"
    "v_mfma_f32_16x16x32_bf16 a[48:51], a[144:147], v[72:75], a[48:51]\n"
    "ds_read_b64_tr_b16 a[188:189], v243 offset:6272\n"
    "s_add_u32 s68, s65, 0x1000\n"
    "v_mfma_f32_16x16x32_bf16 a[52:55], a[144:147], v[76:79], a[52:55]\n"
    "buffer_load_dwordx4 v[144:147], v248, s[40:43], s68 offen sc1\n"
    "v_mfma_f32_16x16x32_bf16 a[56:59], a[144:147], v[80:83], a[56:59]\n"
    "ds_read_b64_tr_b16 a[190:191], v243 offset:11392\n"
    "v_mfma_f32_16x16x32_bf16 a[60:63], a[148:151], v[72:75], a[60:63]\n"
    "ds_read_b64_tr_b16 a[192:193], v242 offset:6336\n"
    "v_mfma_f32_16x16x32_bf16 a[64:67], a[148:151], v[76:79], a[64:67]\n"
    "ds_read_b64_tr_b16 a[194:195], v242 offset:11456\n"
    "v_mfma_f32_16x16x32_bf16 a[68:71], a[148:151], v[80:83], a[68:71]\n"
    "ds_read_b64_tr_b16 a[196:197], v243 offset:6336\n"
    "v_mfma_f32_16x16x32_bf16 a[72:75], a[152:155], v[72:75], a[72:75]\n"
    "v_mfma_f32_16x16x32_bf16 a[76:79], a[152:155], v[76:79], a[76:79]\n"
    "ds_read_b64_tr_b16 a[198:199], v243 offset:11456\n"
    "v_mfma_f32_16x16x32_bf16 a[80:83], a[152:155], v[80:83], a[80:83]\n"
    "ds_read_b64_tr_b16 a[200:201], v242 offset:6400\n"
    "v_mfma_f32_16x16x32_bf16 a[84:87], a[156:159], v[72:75], a[84:87]\n"
    "ds_read_b64_tr_b16 a[202:203], v242 offset:11520\n"
    "v_mfma_f32_16x16x32_bf16 a[88:91], a[156:159], v[76:79], a[88:91]\n"
    "ds_read_b64_tr_b16 a[204:205], v243 offset:6400\n"
    "s_add_u32 s69, s65, 0x3000\n"
    "v_mfma_f32_16x16x32_bf16 a[92:95], a[156:159], v[80:83], a[92:95]\n"
    "buffer_load_dwordx4 v[148:151], v248, s[40:43], s69 offen sc1\n"
    "v_mfma_f32_16x16x32_bf16 a[96:99], a[160:163], v[72:75], a[96:99]\n"
    "ds_read_b64_tr_b16 a[206:207], v243 offset:11520\n"
    "v_mfma_f32_16x16x32_bf16 a[100:103], a[160:163], v[76:79], a[100:103]\n"
    "v_mfma_f32_16x16x32_bf16 a[104:107], a[160:163], v[80:83], a[104:107]\n"
    "v_mfma_f32_16x16x32_bf16 a[108:111], a[164:167], v[72:75], a[108:111]\n"
    "v_mfma_f32_16x16x32_bf16 a[112:115], a[164:167], v[76:79], a[112:115]\n"
    "v_mfma_f32_16x16x32_bf16 a[116:119], a[164:167], v[80:83], a[116:119]\n"
    "s_waitcnt lgkmcnt(0)\n"
    "v_mfma_f32_16x16x32_bf16 a[0:3], a[168:171], v[84:87], a[0:3]\n"
    "s_barrier\n"
    "v_mfma_f32_16x16x32_bf16 a[4:7], a[168:171], v[88:91], a[4:7]\n"
    "ds_read_b64_tr_b16 a[128:129], v242 offset:16384\n"
    "v_mfma_f32_16x16x32_bf16 a[8:11], a[168:171], v[92:95], a[8:11]\n"
    "ds_read_b64_tr_b16 a[130:131], v242 offset:21504\n"
    "s_add_u32 s70, s65, 0x5000\n"
    "v_mfma_f32_16x16x32_bf16 a[12:15], a[172:175], v[84:87], a[12:15]\n"
    "buffer_load_dwordx4 v[152:155], v248, s[40:43], s70 offen sc1\n"
    "v_mfma_f32_16x16x32_bf16 a[16:19], a[172:175], v[88:91], a[16:19]\n"
    "ds_read_b64_tr_b16 a[132:133], v243 offset:16384\n"
    "v_mfma_f32_16x16x32_bf16 a[20:23], a[172:175], v[92:95], a[20:23]\n"
    "ds_read_b64_tr_b16 a[134:135], v243 offset:21504\n"
    "v_mfma_f32_16x16x32_bf16 a[24:27], a[176:179], v[84:87], a[24:27]\n"
    "ds_read_b64_tr_b16 a[136:137], v242 offset:16448\n"
    "v_mfma_f32_16x16x32_bf16 a[28:31], a[176:179], v[88:91], a[28:31]\n"
    "ds_read_b64_tr_b16 a[138:139], v242 offset:21568\n"
    "v_mfma_f32_16x16x32_bf16 a[32:35], a[176:179], v[92:95], a[32:35]\n"
    "v_mfma_f32_16x16x32_bf16 a[36:39], a[180:183], v[84:87], a[36:39]\n"
    "ds_read_b64_tr_b16 a[140:141], v243 offset:16448\n"
    "v_mfma_f32_16x16x32_bf16 a[40:43], a[180:183], v[88:91], a[40:43]\n"
    "ds_read_b64_tr_b16 a[142:143], v243 offset:21568\n"
    "v_mfma_f32_16x16x32_bf16 a[44:47], a[180:183], v[92:95], a[44:47]\n"
    "ds_read_b64_tr_b16 a[144:145], v242 offset:16512\n"
    "v_mfma_f32_16x16x32_bf16 a[48:51], a[184:187], v[84:87], a[48:51]\n"
    "ds_read_b64_tr_b16 a[146:147], v242 offset:21632\n"
    "v_mfma_f32_16x16x32_bf16 a[52:55], a[184:187], v[88:91], a[52:55]\n"
    "buffer_load_dwordx4 v[156:159], v248, s[40:43], s68 offen offset:1024 sc1\n"
    "v_mfma_f32_16x16x32_bf16 a[56:59], a[184:187], v[92:95], a[56:59]\n"
    "ds_read_b64_tr_b16 a[148:149], v243 offset:16512\n"
    "v_mfma_f32_16x16x32_bf16 a[60:63], a[188:191], v[84:87], a[60:63]\n"
    "ds_read_b64_tr_b16 a[150:151], v243 offset:21632\n"
    "v_mfma_f32_16x16x32_bf16 a[64:67], a[188:191], v[88:91], a[64:67]\n"
    "ds_read_b64_tr_b16 a[152:153], v242 offset:16576\n"
    "v_mfma_f32_16x16x32_bf16 a[68:71], a[188:191], v[92:95], a[68:71]\n"
    "ds_read_b64_tr_b16 a[154:155], v242 offset:21696\n"
    "v_mfma_f32_16x16x32_bf16 a[72:75], a[192:195], v[84:87], a[72:75]\n"
    "v_mfma_f32_16x16x32_bf16 a[76:79], a[192:195], v[88:91], a[76:79]\n"
    "ds_read_b64_tr_b16 a[156:157], v243 offset:16576\n"
    "v_mfma_f32_16x16x32_bf16 a[80:83], a[192:195], v[92:95], a[80:83]\n"
    "ds_read_b64_tr_b16 a[158:159], v243 offset:21696\n"
    "v_mfma_f32_16x16x32_bf16 a[84:87], a[196:199], v[84:87], a[84:87]\n"
    "ds_read_b64_tr_b16 a[160:161], v242 offset:16640\n"
    "v_mfma_f32_16x16x32_bf16 a[88:91], a[196:199], v[88:91], a[88:91]\n"
    "ds_read_b64_tr_b16 a[162:163], v242 offset:21760\n"
    "v_mfma_f32_16x16x32_bf16 a[92:95], a[196:199], v[92:95], a[92:95]\n"
    "buffer_load_dwordx4 v[160:163], v248, s[40:43], s69 offen offset:1024 sc1\n"
    "v_mfma_f32_16x16x32_bf16 a[96:99], a[200:203], v[84:87], a[96:99]\n"
    "ds_read_b64_tr_b16 a[164:165], v243 offset:16640\n"
    "v_mfma_f32_16x16x32_bf16 a[100:103], a[200:203], v[88:91], a[100:103]\n"
    "ds_read_b64_tr_b16 a[166:167], v243 offset:21760\n"
    "v_mfma_f32_16x16x32_bf16 a[104:107], a[200:203], v[92:95], a[104:107]\n"
    "v_mfma_f32_16x16x32_bf16 a[108:111], a[204:207], v[84:87], a[108:111]\n"
    "v_mfma_f32_16x16x32_bf16 a[112:115], a[204:207], v[88:91], a[112:115]\n"
    "v_mfma_f32_16x16x32_bf16 a[116:119], a[204:207], v[92:95], a[116:119]\n"
    "s_waitcnt vmcnt(15) lgkmcnt(0)\n"
    "v_mfma_f32_16x16x32_bf16 a[0:3], a[128:131], v[96:99], a[0:3]\n"
    "ds_read_b64_tr_b16 a[168:169], v242 offset:26624\n"
    "v_mfma_f32_16x16x32_bf16 a[4:7], a[128:131], v[100:103], a[4:7]\n"
    "ds_read_b64_tr_b16 a[170:171], v242 offset:31744\n"
    "v_mfma_f32_16x16x32_bf16 a[8:11], a[128:131], v[104:107], a[8:11]\n"
    "ds_read_b64_tr_b16 a[172:173], v243 offset:26624\n"
    "v_mfma_f32_16x16x32_bf16 a[12:15], a[132:135], v[96:99], a[12:15]\n"
    "buffer_load_dwordx4 v[164:167], v248, s[40:43], s70 offen offset:1024 sc1\n"
    "v_mfma_f32_16x16x32_bf16 a[16:19], a[132:135], v[100:103], a[16:19]\n"
    "ds_read_b64_tr_b16 a[174:175], v243 offset:31744\n"
    "v_mfma_f32_16x16x32_bf16 a[20:23], a[132:135], v[104:107], a[20:23]\n"
    "ds_read_b64_tr_b16 a[176:177], v242 offset:26688\n"
    "v_mfma_f32_16x16x32_bf16 a[24:27], a[136:139], v[96:99], a[24:27]\n"
    "ds_read_b64_tr_b16 a[178:179], v242 offset:31808\n"
    "v_mfma_f32_16x16x32_bf16 a[28:31], a[136:139], v[100:103], a[28:31]\n"
    "ds_read_b64_tr_b16 a[180:181], v243 offset:26688\n"
    "v_mfma_f32_16x16x32_bf16 a[32:35], a[136:139], v[104:107], a[32:35]\n"
    "v_mfma_f32_16x16x32_bf16 a[36:39], a[140:143], v[96:99], a[36:39]\n"
    "ds_read_b64_tr_b16 a[182:183], v243 offset:31808\n"
    "v_mfma_f32_16x16x32_bf16 a[40:43], a[140:143], v[100:103], a[40:43]\n"
    "ds_read_b64_tr_b16 a[184:185], v242 offset:26752\n"
    "v_mfma_f32_16x16x32_bf16 a[44:47], a[140:143], v[104:107], a[44:47]\n"
    "ds_read_b64_tr_b16 a[186:187], v242 offset:31872\n"
    "v_mfma_f32_16x16x32_bf16 a[48:51], a[144:147], v[96:99], a[48:51]\n"
    "ds_read_b64_tr_b16 a[188:189], v243 offset:26752\n"
    "v_mfma_f32_16x16x32_bf16 a[52:55], a[144:147], v[100:103], a[52:55]\n"
    "buffer_load_dwordx4 v[168:171], v248, s[40:43], s68 offen offset:2048 sc1\n"
    "v_mfma_f32_16x16x32_bf16 a[56:59], a[144:147], v[104:107], a[56:59]\n"
    "ds_read_b64_tr_b16 a[190:191], v243 offset:31872\n"
    "v_mfma_f32_16x16x32_bf16 a[60:63], a[148:151], v[96:99], a[60:63]\n"
    "ds_read_b64_tr_b16 a[192:193], v242 offset:26816\n"
    "v_mfma_f32_16x16x32_bf16 a[64:67], a[148:151], v[100:103], a[64:67]\n"
    "ds_read_b64_tr_b16 a[194:195], v242 offset:31936\n"
    "v_mfma_f32_16x16x32_bf16 a[68:71], a[148:151], v[104:107], a[68:71]\n"
    "ds_read_b64_tr_b16 a[196:197], v243 offset:26816\n"
    "v_mfma_f32_16x16x32_bf16 a[72:75], a[152:155], v[96:99], a[72:75]\n"
    "v_mfma_f32_16x16x32_bf16 a[76:79], a[152:155], v[100:103], a[76:79]\n"
    "ds_read_b64_tr_b16 a[198:199], v243 offset:31936\n"
    "v_mfma_f32_16x16x32_bf16 a[80:83], a[152:155], v[104:107], a[80:83]\n"
    "ds_read_b64_tr_b16 a[200:201], v242 offset:26880\n"
    "v_mfma_f32_16x16x32_bf16 a[84:87], a[156:159], v[96:99], a[84:87]\n"
    "ds_read_b64_tr_b16 a[202:203], v242 offset:32000\n"
    "v_mfma_f32_16x16x32_bf16 a[88:91], a[156:159], v[100:103], a[88:91]\n"
    "ds_read_b64_tr_b16 a[204:205], v243 offset:26880\n"
    "v_mfma_f32_16x16x32_bf16 a[92:95], a[156:159], v[104:107], a[92:95]\n"
    "buffer_load_dwordx4 v[172:175], v248, s[40:43], s69 offen offset:2048 sc1\n"
    "v_mfma_f32_16x16x32_bf16 a[96:99], a[160:163], v[96:99], a[96:99]\n"
    "ds_read_b64_tr_b16 a[206:207], v243 offset:32000\n"
    "v_mfma_f32_16x16x32_bf16 a[100:103], a[160:163], v[100:103], a[100:103]\n"
    "v_mfma_f32_16x16x32_bf16 a[104:107], a[160:163], v[104:107], a[104:107]\n"
    "v_mfma_f32_16x16x32_bf16 a[108:111], a[164:167], v[96:99], a[108:111]\n"
    "v_mfma_f32_16x16x32_bf16 a[112:115], a[164:167], v[100:103], a[112:115]\n"
    "v_mfma_f32_16x16x32_bf16 a[116:119], a[164:167], v[104:107], a[116:119]\n"
    "s_waitcnt vmcnt(15) lgkmcnt(0)\n"
    "v_mfma_f32_16x16x32_bf16 a[0:3], a[168:171], v[108:111], a[0:3]\n"
    "s_barrier\n"
    "v_mfma_f32_16x16x32_bf16 a[4:7], a[168:171], v[112:115], a[4:7]\n"
    "ds_read_b64_tr_b16 a[128:129], v242 offset:36864\n"
    "v_mfma_f32_16x16x32_bf16 a[8:11], a[168:171], v[116:119], a[8:11]\n"
    "ds_read_b64_tr_b16 a[130:131], v242 offset:41984\n"
    "v_mfma_f32_16x16x32_bf16 a[12:15], a[172:175], v[108:111], a[12:15]\n"
    "buffer_load_dwordx4 v[176:179], v248, s[40:43], s70 offen offset:2048 sc1\n"
    "v_mfma_f32_16x16x32_bf16 a[16:19], a[172:175], v[112:115], a[16:19]\n"
    "ds_read_b64_tr_b16 a[132:133], v243 offset:36864\n"
    "v_mfma_f32_16x16x32_bf16 a[20:23], a[172:175], v[116:119], a[20:23]\n"
    "ds_read_b64_tr_b16 a[134:135], v243 offset:41984\n"
    "v_mfma_f32_16x16x32_bf16 a[24:27], a[176:179], v[108:111], a[24:27]\n"
    "ds_read_b64_tr_b16 a[136:137], v242 offset:36928\n"
    "v_mfma_f32_16x16x32_bf16 a[28:31], a[176:179], v[112:115], a[28:31]\n"
    "ds_read_b64_tr_b16 a[138:139], v242 offset:42048\n"
    "v_mfma_f32_16x16x32_bf16 a[32:35], a[176:179], v[116:119], a[32:35]\n"
    "v_mfma_f32_16x16x32_bf16 a[36:39], a[180:183], v[108:111], a[36:39]\n"
    "ds_read_b64_tr_b16 a[140:141], v243 offset:36928\n"
    "v_mfma_f32_16x16x32_bf16 a[40:43], a[180:183], v[112:115], a[40:43]\n"
    "ds_read_b64_tr_b16 a[142:143], v243 offset:42048\n"
    "v_mfma_f32_16x16x32_bf16 a[44:47], a[180:183], v[116:119], a[44:47]\n"
    "ds_read_b64_tr_b16 a[144:145], v242 offset:36992\n"
    "v_mfma_f32_16x16x32_bf16 a[48:51], a[184:187], v[108:111], a[48:51]\n"
    "ds_read_b64_tr_b16 a[146:147], v242 offset:42112\n"
    "v_mfma_f32_16x16x32_bf16 a[52:55], a[184:187], v[112:115], a[52:55]\n"
    "buffer_load_dwordx4 v[180:183], v248, s[40:43], s68 offen offset:3072 sc1\n"
    "v_mfma_f32_16x16x32_bf16 a[56:59], a[184:187], v[116:119], a[56:59]\n"
    "ds_read_b64_tr_b16 a[148:149], v243 offset:36992\n"
    "v_mfma_f32_16x16x32_bf16 a[60:63], a[188:191], v[108:111], a[60:63]\n"
    "ds_read_b64_tr_b16 a[150:151], v243 offset:42112\n"
    "v_mfma_f32_16x16x32_bf16 a[64:67], a[188:191], v[112:115], a[64:67]\n"
    "ds_read_b64_tr_b16 a[152:153], v242 offset:37056\n"
    "v_mfma_f32_16x16x32_bf16 a[68:71], a[188:191], v[116:119], a[68:71]\n"
    "ds_read_b64_tr_b16 a[154:155], v242 offset:42176\n"
    "v_mfma_f32_16x16x32_bf16 a[72:75], a[192:195], v[108:111], a[72:75]\n"
    "v_mfma_f32_16x16x32_bf16 a[76:79], a[192:195], v[112:115], a[76:79]\n"
    "ds_read_b64_tr_b16 a[156:157], v243 offset:37056\n"
    "v_mfma_f32_16x16x32_bf16 a[80:83], a[192:195], v[116:119], a[80:83]\n"
    "ds_read_b64_tr_b16 a[158:159], v243 offset:42176\n"
    "v_mfma_f32_16x16x32_bf16 a[84:87], a[196:199], v[108:111], a[84:87]\n"
    "ds_read_b64_tr_b16 a[160:161], v242 offset:37120\n"
    "v_mfma_f32_16x16x32_bf16 a[88:91], a[196:199], v[112:115], a[88:91]\n"
    "ds_read_b64_tr_b16 a[162:163], v242 offset:42240\n"
    "v_mfma_f32_16x16x32_bf16 a[92:95], a[196:199], v[116:119], a[92:95]\n"
    "buffer_load_dwordx4 v[184:187], v248, s[40:43], s69 offen offset:3072 sc1\n"
    "v_mfma_f32_16x16x32_bf16 a[96:99], a[200:203], v[108:111], a[96:99]\n"
    "ds_read_b64_tr_b16 a[164:165], v243 offset:37120\n"
    "v_mfma_f32_16x16x32_bf16 a[100:103], a[200:203], v[112:115], a[100:103]\n"
    "ds_read_b64_tr_b16 a[166:167], v243 offset:42240\n"
    "v_mfma_f32_16x16x32_bf16 a[104:107], a[200:203], v[116:119], a[104:107]\n"
    "v_mfma_f32_16x16x32_bf16 a[108:111], a[204:207], v[108:111], a[108:111]\n"
    "v_mfma_f32_16x16x32_bf16 a[112:115], a[204:207], v[112:115], a[112:115]\n"
    "v_mfma_f32_16x16x32_bf16 a[116:119], a[204:207], v[116:119], a[116:119]\n"
    "s_waitcnt vmcnt(15) lgkmcnt(0)\n"
    "v_mfma_f32_16x16x32_bf16 a[0:3], a[128:131], v[120:123], a[0:3]\n"
    "ds_read_b64_tr_b16 a[168:169], v242 offset:47104\n"
    "v_mfma_f32_16x16x32_bf16 a[4:7], a[128:131], v[124:127], a[4:7]\n"
    "ds_read_b64_tr_b16 a[170:171], v242 offset:52224\n"
    "v_mfma_f32_16x16x32_bf16 a[8:11], a[128:131], v[128:131], a[8:11]\n"
    "ds_read_b64_tr_b16 a[172:173], v243 offset:47104\n"
    "v_mfma_f32_16x16x32_bf16 a[12:15], a[132:135], v[120:123], a[12:15]\n"
    "buffer_load_dwordx4 v[188:191], v248, s[40:43], s70 offen offset:3072 sc1\n"
    "v_mfma_f32_16x16x32_bf16 a[16:19], a[132:135], v[124:127], a[16:19]\n"
    "ds_read_b64_tr_b16 a[174:175], v243 offset:52224\n"
    "v_mfma_f32_16x16x32_bf16 a[20:23], a[132:135], v[128:131], a[20:23]\n"
    "ds_read_b64_tr_b16 a[176:177], v242 offset:47168\n"
    "v_mfma_f32_16x16x32_bf16 a[24:27], a[136:139], v[120:123], a[24:27]\n"
    "ds_read_b64_tr_b16 a[178:179], v242 offset:52288\n"
    "s_add_u32 m0, s80, 0x0\n"
    "v_mfma_f32_16x16x32_bf16 a[28:31], a[136:139], v[124:127], a[28:31]\n"
    "ds_read_b64_tr_b16 a[180:181], v243 offset:47168\n"
    "s_add_u32 s62, s50, 0x0\n"
    "v_mfma_f32_16x16x32_bf16 a[32:35], a[136:139], v[128:131], a[32:35]\n"
    "buffer_load_dwordx4 v228, s[36:39], s62 offen lds\n"
    "v_mfma_f32_16x16x32_bf16 a[36:39], a[140:143], v[120:123], a[36:39]\n"
    "ds_read_b64_tr_b16 a[182:183], v243 offset:52288\n"
    "v_mfma_f32_16x16x32_bf16 a[40:43], a[140:143], v[124:127], a[40:43]\n"
    "ds_read_b64_tr_b16 a[184:185], v242 offset:47232\n"
    "v_mfma_f32_16x16x32_bf16 a[44:47], a[140:143], v[128:131], a[44:47]\n"
    "ds_read_b64_tr_b16 a[186:187], v242 offset:52352\n"
    "v_mfma_f32_16x16x32_bf16 a[48:51], a[144:147], v[120:123], a[48:51]\n"
    "ds_read_b64_tr_b16 a[188:189], v243 offset:47232\n"
    "v_mfma_f32_16x16x32_bf16 a[52:55], a[144:147], v[124:127], a[52:55]\n"
    "buffer_load_dwordx4 v229, s[36:39], s62 offen offset:1024 lds\n"
    "v_mfma_f32_16x16x32_bf16 a[56:59], a[144:147], v[128:131], a[56:59]\n"
    "ds_read_b64_tr_b16 a[190:191], v243 offset:52352\n"
    "v_mfma_f32_16x16x32_bf16 a[60:63], a[148:151], v[120:123], a[60:63]\n"
    "ds_read_b64_tr_b16 a[192:193], v242 offset:47296\n"
    "v_mfma_f32_16x16x32_bf16 a[64:67], a[148:151], v[124:127], a[64:67]\n"
    "ds_read_b64_tr_b16 a[194:195], v242 offset:52416\n"
    "v_mfma_f32_16x16x32_bf16 a[68:71], a[148:151], v[128:131], a[68:71]\n"
    "ds_read_b64_tr_b16 a[196:197], v243 offset:47296\n"
    "v_mfma_f32_16x16x32_bf16 a[72:75], a[152:155], v[120:123], a[72:75]\n"
    "buffer_load_dwordx4 v230, s[36:39], s62 offen offset:2048 lds\n"
    "v_mfma_f32_16x16x32_bf16 a[76:79], a[152:155], v[124:127], a[76:79]\n"
    "ds_read_b64_tr_b16 a[198:199], v243 offset:52416\n"
    "v_mfma_f32_16x16x32_bf16 a[80:83], a[152:155], v[128:131], a[80:83]\n"
    "ds_read_b64_tr_b16 a[200:201], v242 offset:47360\n"
    "v_mfma_f32_16x16x32_bf16 a[84:87], a[156:159], v[120:123], a[84:87]\n"
    "ds_read_b64_tr_b16 a[202:203], v242 offset:52480\n"
    "v_mfma_f32_16x16x32_bf16 a[88:91], a[156:159], v[124:127], a[88:91]\n"
    "ds_read_b64_tr_b16 a[204:205], v243 offset:47360\n"
    "v_mfma_f32_16x16x32_bf16 a[92:95], a[156:159], v[128:131], a[92:95]\n"
    "buffer_load_dwordx4 v231, s[36:39], s62 offen offset:3072 lds\n"
    "v_mfma_f32_16x16x32_bf16 a[96:99], a[160:163], v[120:123], a[96:99]\n"
    "ds_read_b64_tr_b16 a[206:207], v243 offset:52480\n"
    "v_mfma_f32_16x16x32_bf16 a[100:103], a[160:163], v[124:127], a[100:103]\n"
    "v_mfma_f32_16x16x32_bf16 a[104:107], a[160:163], v[128:131], a[104:107]\n"
    "s_add_u32 m0, s80, 0x1000\n"
    "v_mfma_f32_16x16x32_bf16 a[108:111], a[164:167], v[120:123], a[108:111]\n"
    "buffer_load_dwordx4 v232, s[36:39], s62 offen lds\n"
    "v_mfma_f32_16x16x32_bf16 a[112:115], a[164:167], v[124:127], a[112:115]\n"
    "v_mfma_f32_16x16x32_bf16 a[116:119], a[164:167], v[128:131], a[116:119]\n"
    "s_waitcnt vmcnt(18) lgkmcnt(0)\n"
    "v_mfma_f32_16x16x32_bf16 a[0:3], a[168:171], v[132:135], a[0:3]\n"
    "s_waitcnt vmcnt(17)\n"
    "s_barrier\n"
    "v_mfma_f32_16x16x32_bf16 a[4:7], a[168:171], v[136:139], a[4:7]\n"
    "ds_read_b64_tr_b16 a[128:129], v242 offset:57344\n"
    "s_add_u32 m0, s80, 0x5000\n"
    "v_mfma_f32_16x16x32_bf16 a[8:11], a[168:171], v[140:143], a[8:11]\n"
    "ds_read_b64_tr_b16 a[130:131], v242 offset:62464\n"
    "s_add_u32 s62, s50, 0x14000\n"
    "v_mfma_f32_16x16x32_bf16 a[12:15], a[172:175], v[132:135], a[12:15]\n"
    "buffer_load_dwordx4 v228, s[36:39], s62 offen lds\n"
    "v_mfma_f32_16x16x32_bf16 a[16:19], a[172:175], v[136:139], a[16:19]\n"
    "ds_read_b64_tr_b16 a[132:133], v243 offset:57344\n"
    "v_mfma_f32_16x16x32_bf16 a[20:23], a[172:175], v[140:143], a[20:23]\n"
    "ds_read_b64_tr_b16 a[134:135], v243 offset:62464\n"
    "v_mfma_f32_16x16x32_bf16 a[24:27], a[176:179], v[132:135], a[24:27]\n"
    "ds_read_b64_tr_b16 a[136:137], v242 offset:57408\n"
    "v_mfma_f32_16x16x32_bf16 a[28:31], a[176:179], v[136:139], a[28:31]\n"
    "ds_read_b64_tr_b16 a[138:139], v242 offset:62528\n"
    "v_mfma_f32_16x16x32_bf16 a[32:35], a[176:179], v[140:143], a[32:35]\n"
    "buffer_load_dwordx4 v229, s[36:39], s62 offen offset:1024 lds\n"
    "v_mfma_f32_16x16x32_bf16 a[36:39], a[180:183], v[132:135], a[36:39]\n"
    "ds_read_b64_tr_b16 a[140:141], v243 offset:57408\n"
    "v_mfma_f32_16x16x32_bf16 a[40:43], a[180:183], v[136:139], a[40:43]\n"
    "ds_read_b64_tr_b16 a[142:143], v243 offset:62528\n"
    "v_mfma_f32_16x16x32_bf16 a[44:47], a[180:183], v[140:143], a[44:47]\n"
    "ds_read_b64_tr_b16 a[144:145], v242 offset:57472\n"
    "v_mfma_f32_16x16x32_bf16 a[48:51], a[184:187], v[132:135], a[48:51]\n"
    "ds_read_b64_tr_b16 a[146:147], v242 offset:62592\n"
    "v_mfma_f32_16x16x32_bf16 a[52:55], a[184:187], v[136:139], a[52:55]\n"
    "buffer_load_dwordx4 v230, s[36:39], s62 offen offset:2048 lds\n"
    "v_mfma_f32_16x16x32_bf16 a[56:59], a[184:187], v[140:143], a[56:59]\n"
    "ds_read_b64_tr_b16 a[148:149], v243 offset:57472\n"
    "v_mfma_f32_16x16x32_bf16 a[60:63], a[188:191], v[132:135], a[60:63]\n"
    "ds_read_b64_tr_b16 a[150:151], v243 offset:62592\n"
    "v_mfma_f32_16x16x32_bf16 a[64:67], a[188:191], v[136:139], a[64:67]\n"
    "ds_read_b64_tr_b16 a[152:153], v242 offset:57536\n"
    "v_mfma_f32_16x16x32_bf16 a[68:71], a[188:191], v[140:143], a[68:71]\n"
    "ds_read_b64_tr_b16 a[154:155], v242 offset:62656\n"
    "v_mfma_f32_16x16x32_bf16 a[72:75], a[192:195], v[132:135], a[72:75]\n"
    "buffer_load_dwordx4 v231, s[36:39], s62 offen offset:3072 lds\n"
    "v_mfma_f32_16x16x32_bf16 a[76:79], a[192:195], v[136:139], a[76:79]\n"
    "ds_read_b64_tr_b16 a[156:157], v243 offset:57536\n"
    "v_mfma_f32_16x16x32_bf16 a[80:83], a[192:195], v[140:143], a[80:83]\n"
    "ds_read_b64_tr_b16 a[158:159], v243 offset:62656\n"
    "v_mfma_f32_16x16x32_bf16 a[84:87], a[196:199], v[132:135], a[84:87]\n"
    "ds_read_b64_tr_b16 a[160:161], v242 offset:57600\n"
    "v_mfma_f32_16x16x32_bf16 a[88:91], a[196:199], v[136:139], a[88:91]\n"
    "ds_read_b64_tr_b16 a[162:163], v242 offset:62720\n"
    "s_add_u32 m0, s80, 0x6000\n"
    "v_mfma_f32_16x16x32_bf16 a[92:95], a[196:199], v[140:143], a[92:95]\n"
    "buffer_load_dwordx4 v232, s[36:39], s62 offen lds\n"
    "v_mfma_f32_16x16x32_bf16 a[96:99], a[200:203], v[132:135], a[96:99]\n"
    "ds_read_b64_tr_b16 a[164:165], v243 offset:57600\n"
    "v_mfma_f32_16x16x32_bf16 a[100:103], a[200:203], v[136:139], a[100:103]\n"
    "ds_read_b64_tr_b16 a[166:167], v243 offset:62720\n"
    "v_mfma_f32_16x16x32_bf16 a[104:107], a[200:203], v[140:143], a[104:107]\n"
    "s_add_u32 s68, s66, 0x0\n"
    "v_mfma_f32_16x16x32_bf16 a[108:111], a[204:207], v[132:135], a[108:111]\n"
    "buffer_load_dwordx4 v[0:3], v248, s[40:43], s68 offen sc1\n"
    "v_mfma_f32_16x16x32_bf16 a[112:115], a[204:207], v[136:139], a[112:115]\n"
    "v_mfma_f32_16x16x32_bf16 a[116:119], a[204:207], v[140:143], a[116:119]\n"
    "s_waitcnt vmcnt(20) lgkmcnt(0)\n"
    "v_mfma_f32_16x16x32_bf16 a[0:3], a[128:131], v[144:147], a[0:3]\n"
    "ds_read_b64_tr_b16 a[168:169], v244 offset:2048\n"
    "v_mfma_f32_16x16x32_bf16 a[4:7], a[128:131], v[148:151], a[4:7]\n"
    "ds_read_b64_tr_b16 a[170:171], v244 offset:7168\n"
    "v_mfma_f32_16x16x32_bf16 a[8:11], a[128:131], v[152:155], a[8:11]\n"
    "ds_read_b64_tr_b16 a[172:173], v245 offset:2048\n"
    "s_add_u32 s69, s66, 0x2000\n"
    "v_mfma_f32_16x16x32_bf16 a[12:15], a[132:135], v[144:147], a[12:15]\n"
    "buffer_load_dwordx4 v[4:7], v248, s[40:43], s69 offen sc1\n"
    "v_mfma_f32_16x16x32_bf16 a[16:19], a[132:135], v[148:151], a[16:19]\n"
    "ds_read_b64_tr_b16 a[174:175], v245 offset:7168\n"
    "v_mfma_f32_16x16x32_bf16 a[20:23], a[132:135], v[152:155], a[20:23]\n"
    "ds_read_b64_tr_b16 a[176:177], v244 offset:2112\n"
    "v_mfma_f32_16x16x32_bf16 a[24:27], a[136:139], v[144:147], a[24:27]\n"
    "ds_read_b64_tr_b16 a[178:179], v244 offset:7232\n"
    "v_mfma_f32_16x16x32_bf16 a[28:31], a[136:139], v[148:151], a[28:31]\n"
    "ds_read_b64_tr_b16 a[180:181], v245 offset:2112\n"
    "s_add_u32 s70, s66, 0x4000\n"
    "v_mfma_f32_16x16x32_bf16 a[32:35], a[136:139], v[152:155], a[32:35]\n"
    "buffer_load_dwordx4 v[8:11], v248, s[40:43], s70 offen sc1\n"
    "v_mfma_f32_16x16x32_bf16 a[36:39], a[140:143], v[144:147], a[36:39]\n"
    "ds_read_b64_tr_b16 a[182:183], v245 offset:7232\n"
    "v_mfma_f32_16x16x32_bf16 a[40:43], a[140:143], v[148:151], a[40:43]\n"
    "ds_read_b64_tr_b16 a[184:185], v244 offset:2176\n"
    "v_mfma_f32_16x16x32_bf16 a[44:47], a[140:143], v[152:155], a[44:47]\n"
    "ds_read_b64_tr_b16 a[186:187], v244 offset:7296\n"
    "v_mfma_f32_16x16x32_bf16 a[48:51], a[144:147], v[144:147], a[48:51]\n"
    "ds_read_b64_tr_b16 a[188:189], v245 offset:2176\n"
    "v_mfma_f32_16x16x32_bf16 a[52:55], a[144:147], v[148:151], a[52:55]\n"
    "buffer_load_dwordx4 v[12:15], v248, s[40:43], s68 offen offset:1024 sc1\n"
    "v_mfma_f32_16x16x32_bf16 a[56:59], a[144:147], v[152:155], a[56:59]\n"
    "ds_read_b64_tr_b16 a[190:191], v245 offset:7296\n"
    "v_mfma_f32_16x16x32_bf16 a[60:63], a[148:151], v[144:147], a[60:63]\n"
    "ds_read_b64_tr_b16 a[192:193], v244 offset:2240\n"
    "v_mfma_f32_16x16x32_bf16 a[64:67], a[148:151], v[148:151], a[64:67]\n"
    "ds_read_b64_tr_b16 a[194:195], v244 offset:7360\n"
    "v_mfma_f32_16x16x32_bf16 a[68:71], a[148:151], v[152:155], a[68:71]\n"
    "ds_read_b64_tr_b16 a[196:197], v245 offset:2240\n"
    "v_mfma_f32_16x16x32_bf16 a[72:75], a[152:155], v[144:147], a[72:75]\n"
    "buffer_load_dwordx4 v[16:19], v248, s[40:43], s69 offen offset:1024 sc1\n"
    "v_mfma_f32_16x16x32_bf16 a[76:79], a[152:155], v[148:151], a[76:79]\n"
    "ds_read_b64_tr_b16 a[198:199], v245 offset:7360\n"
    "v_mfma_f32_16x16x32_bf16 a[80:83], a[152:155], v[152:155], a[80:83]\n"
    "ds_read_b64_tr_b16 a[200:201], v244 offset:2304\n"
    "v_mfma_f32_16x16x32_bf16 a[84:87], a[156:159], v[144:147], a[84:87]\n"
    "ds_read_b64_tr_b16 a[202:203], v244 offset:7424\n"
    "v_mfma_f32_16x16x32_bf16 a[88:91], a[156:159], v[148:151], a[88:91]\n"
    "ds_read_b64_tr_b16 a[204:205], v245 offset:2304\n"
    "v_mfma_f32_16x16x32_bf16 a[92:95], a[156:159], v[152:155], a[92:95]\n"
    "buffer_load_dwordx4 v[20:23], v248, s[40:43], s70 offen offset:1024 sc1\n"
    "v_mfma_f32_16x16x32_bf16 a[96:99], a[160:163], v[144:147], a[96:99]\n"
    "ds_read_b64_tr_b16 a[206:207], v245 offset:7424\n"
    "v_mfma_f32_16x16x32_bf16 a[100:103], a[160:163], v[148:151], a[100:103]\n"
    "v_mfma_f32_16x16x32_bf16 a[104:107], a[160:163], v[152:155], a[104:107]\n"
    "v_mfma_f32_16x16x32_bf16 a[108:111], a[164:167], v[144:147], a[108:111]\n"
    "buffer_load_dwordx4 v[24:27], v248, s[40:43], s68 offen offset:2048 sc1\n"
    "v_mfma_f32_16x16x32_bf16 a[112:115], a[164:167], v[148:151], a[112:115]\n"
    "v_mfma_f32_16x16x32_bf16 a[116:119], a[164:167], v[152:155], a[116:119]\n"
    "s_waitcnt vmcnt(23) lgkmcnt(0)\n"
    "v_mfma_f32_16x16x32_bf16 a[0:3], a[168:171], v[156:159], a[0:3]\n"
    "s_barrier\n"
    "v_mfma_f32_16x16x32_bf16 a[4:7], a[168:171], v[160:163], a[4:7]\n"
    "ds_read_b64_tr_b16 a[128:129], v244 offset:12288\n"
    "v_mfma_f32_16x16x32_bf16 a[8:11], a[168:171], v[164:167], a[8:11]\n"
    "ds_read_b64_tr_b16 a[130:131], v244 offset:17408\n"
    "v_mfma_f32_16x16x32_bf16 a[12:15], a[172:175], v[156:159], a[12:15]\n"
    "buffer_load_dwordx4 v[28:31], v248, s[40:43], s69 offen offset:2048 sc1\n"
    "v_mfma_f32_16x16x32_bf16 a[16:19], a[172:175], v[160:163], a[16:19]\n"
    "ds_read_b64_tr_b16 a[132:133], v245 offset:12288\n"
    "v_mfma_f32_16x16x32_bf16 a[20:23], a[172:175], v[164:167], a[20:23]\n"
    "ds_read_b64_tr_b16 a[134:135], v245 offset:17408\n"
    "v_mfma_f32_16x16x32_bf16 a[24:27], a[176:179], v[156:159], a[24:27]\n"
    "ds_read_b64_tr_b16 a[136:137], v244 offset:12352\n"
    "v_mfma_f32_16x16x32_bf16 a[28:31], a[176:179], v[160:163], a[28:31]\n"
    "ds_read_b64_tr_b16 a[138:139], v244 offset:17472\n"
    "v_mfma_f32_16x16x32_bf16 a[32:35], a[176:179], v[164:167], a[32:35]\n"
    "buffer_load_dwordx4 v[32:35], v248, s[40:43], s70 offen offset:2048 sc1\n"
    "v_mfma_f32_16x16x32_bf16 a[36:39], a[180:183], v[156:159], a[36:39]\n"
    "ds_read_b64_tr_b16 a[140:141], v245 offset:12352\n"
    "v_mfma_f32_16x16x32_bf16 a[40:43], a[180:183], v[160:163], a[40:43]\n"
    "ds_read_b64_tr_b16 a[142:143], v245 offset:17472\n"
    "v_mfma_f32_16x16x32_bf16 a[44:47], a[180:183], v[164:167], a[44:47]\n"
    "ds_read_b64_tr_b16 a[144:145], v244 offset:12416\n"
    "v_mfma_f32_16x16x32_bf16 a[48:51], a[184:187], v[156:159], a[48:51]\n"
    "ds_read_b64_tr_b16 a[146:147], v244 offset:17536\n"
    "v_mfma_f32_16x16x32_bf16 a[52:55], a[184:187], v[160:163], a[52:55]\n"
    "buffer_load_dwordx4 v[36:39], v248, s[40:43], s68 offen offset:3072 sc1\n"
    "v_mfma_f32_16x16x32_bf16 a[56:59], a[184:187], v[164:167], a[56:59]\n"
    "ds_read_b64_tr_b16 a[148:149], v245 offset:12416\n"
    "v_mfma_f32_16x16x32_bf16 a[60:63], a[188:191], v[156:159], a[60:63]\n"
    "ds_read_b64_tr_b16 a[150:151], v245 offset:17536\n"
    "v_mfma_f32_16x16x32_bf16 a[64:67], a[188:191], v[160:163], a[64:67]\n"
    "ds_read_b64_tr_b16 a[152:153], v244 offset:12480\n"
    "v_mfma_f32_16x16x32_bf16 a[68:71], a[188:191], v[164:167], a[68:71]\n"
    "ds_read_b64_tr_b16 a[154:155], v244 offset:17600\n"
    "v_mfma_f32_16x16x32_bf16 a[72:75], a[192:195], v[156:159], a[72:75]\n"
    "buffer_load_dwordx4 v[40:43], v248, s[40:43], s69 offen offset:3072 sc1\n"
    "v_mfma_f32_16x16x32_bf16 a[76:79], a[192:195], v[160:163], a[76:79]\n"
    "ds_read_b64_tr_b16 a[156:157], v245 offset:12480\n"
    "v_mfma_f32_16x16x32_bf16 a[80:83], a[192:195], v[164:167], a[80:83]\n"
    "ds_read_b64_tr_b16 a[158:159], v245 offset:17600\n"
    "v_mfma_f32_16x16x32_bf16 a[84:87], a[196:199], v[156:159], a[84:87]\n"
    "ds_read_b64_tr_b16 a[160:161], v244 offset:12544\n"
    "v_mfma_f32_16x16x32_bf16 a[88:91], a[196:199], v[160:163], a[88:91]\n"
    "ds_read_b64_tr_b16 a[162:163], v244 offset:17664\n"
    "v_mfma_f32_16x16x32_bf16 a[92:95], a[196:199], v[164:167], a[92:95]\n"
    "buffer_load_dwordx4 v[44:47], v248, s[40:43], s70 offen offset:3072 sc1\n"
    "v_mfma_f32_16x16x32_bf16 a[96:99], a[200:203], v[156:159], a[96:99]\n"
    "ds_read_b64_tr_b16 a[164:165], v245 offset:12544\n"
    "v_mfma_f32_16x16x32_bf16 a[100:103], a[200:203], v[160:163], a[100:103]\n"
    "ds_read_b64_tr_b16 a[166:167], v245 offset:17664\n"
    "s_add_u32 m0, s80, 0xa000\n"
    "v_mfma_f32_16x16x32_bf16 a[104:107], a[200:203], v[164:167], a[104:107]\n"
    "s_add_u32 s62, s50, 0x28000\n"
    "v_mfma_f32_16x16x32_bf16 a[108:111], a[204:207], v[156:159], a[108:111]\n"
    "buffer_load_dwordx4 v228, s[36:39], s62 offen lds\n"
    "v_mfma_f32_16x16x32_bf16 a[112:115], a[204:207], v[160:163], a[112:115]\n"
    "v_mfma_f32_16x16x32_bf16 a[116:119], a[204:207], v[164:167], a[116:119]\n"
    "s_waitcnt vmcnt(26) lgkmcnt(0)\n"
    "v_mfma_f32_16x16x32_bf16 a[0:3], a[128:131], v[168:171], a[0:3]\n"
    "ds_read_b64_tr_b16 a[168:169], v244 offset:22528\n"
    "v_mfma_f32_16x16x32_bf16 a[4:7], a[128:131], v[172:175], a[4:7]\n"
    "ds_read_b64_tr_b16 a[170:171], v244 offset:27648\n"
    "v_mfma_f32_16x16x32_bf16 a[8:11], a[128:131], v[176:179], a[8:11]\n"
    "ds_read_b64_tr_b16 a[172:173], v245 offset:22528\n"
    "v_mfma_f32_16x16x32_bf16 a[12:15], a[132:135], v[168:171], a[12:15]\n"
    "buffer_load_dwordx4 v229, s[36:39], s62 offen offset:1024 lds\n"
    "v_mfma_f32_16x16x32_bf16 a[16:19], a[132:135], v[172:175], a[16:19]\n"
    "ds_read_b64_tr_b16 a[174:175], v245 offset:27648\n"
    "v_mfma_f32_16x16x32_bf16 a[20:23], a[132:135], v[176:179], a[20:23]\n"
    "ds_read_b64_tr_b16 a[176:177], v244 offset:22592\n"
    "v_mfma_f32_16x16x32_bf16 a[24:27], a[136:139], v[168:171], a[24:27]\n"
    "ds_read_b64_tr_b16 a[178:179], v244 offset:27712\n"
    "v_mfma_f32_16x16x32_bf16 a[28:31], a[136:139], v[172:175], a[28:31]\n"
    "ds_read_b64_tr_b16 a[180:181], v245 offset:22592\n"
    "v_mfma_f32_16x16x32_bf16 a[32:35], a[136:139], v[176:179], a[32:35]\n"
    "buffer_load_dwordx4 v230, s[36:39], s62 offen offset:2048 lds\n"
    "v_mfma_f32_16x16x32_bf16 a[36:39], a[140:143], v[168:171], a[36:39]\n"
    "ds_read_b64_tr_b16 a[182:183], v245 offset:27712\n"
    "v_mfma_f32_16x16x32_bf16 a[40:43], a[140:143], v[172:175], a[40:43]\n"
    "ds_read_b64_tr_b16 a[184:185], v244 offset:22656\n"
    "v_mfma_f32_16x16x32_bf16 a[44:47], a[140:143], v[176:179], a[44:47]\n"
    "ds_read_b64_tr_b16 a[186:187], v244 offset:27776\n"
    "v_mfma_f32_16x16x32_bf16 a[48:51], a[144:147], v[168:171], a[48:51]\n"
    "ds_read_b64_tr_b16 a[188:189], v245 offset:22656\n"
    "v_mfma_f32_16x16x32_bf16 a[52:55], a[144:147], v[172:175], a[52:55]\n"
    "buffer_load_dwordx4 v231, s[36:39], s62 offen offset:3072 lds\n"
    "v_mfma_f32_16x16x32_bf16 a[56:59], a[144:147], v[176:179], a[56:59]\n"
    "ds_read_b64_tr_b16 a[190:191], v245 offset:27776\n"
    "v_mfma_f32_16x16x32_bf16 a[60:63], a[148:151], v[168:171], a[60:63]\n"
    "ds_read_b64_tr_b16 a[192:193], v244 offset:22720\n"
    "v_mfma_f32_16x16x32_bf16 a[64:67], a[148:151], v[172:175], a[64:67]\n"
    "ds_read_b64_tr_b16 a[194:195], v244 offset:27840\n"
    "v_mfma_f32_16x16x32_bf16 a[68:71], a[148:151], v[176:179], a[68:71]\n"
    "ds_read_b64_tr_b16 a[196:197], v245 offset:22720\n"
    "s_add_u32 m0, s80, 0xb000\n"
    "v_mfma_f32_16x16x32_bf16 a[72:75], a[152:155], v[168:171], a[72:75]\n"
    "buffer_load_dwordx4 v232, s[36:39], s62 offen lds\n"
    "v_mfma_f32_16x16x32_bf16 a[76:79], a[152:155], v[172:175], a[76:79]\n"
    "ds_read_b64_tr_b16 a[198:199], v245 offset:27840\n"
    "v_mfma_f32_16x16x32_bf16 a[80:83], a[152:155], v[176:179], a[80:83]\n"
    "ds_read_b64_tr_b16 a[200:201], v244 offset:22784\n"
    "v_mfma_f32_16x16x32_bf16 a[84:87], a[156:159], v[168:171], a[84:87]\n"
    "ds_read_b64_tr_b16 a[202:203], v244 offset:27904\n"
    "v_mfma_f32_16x16x32_bf16 a[88:91], a[156:159], v[172:175], a[88:91]\n"
    "ds_read_b64_tr_b16 a[204:205], v245 offset:22784\n"
    "s_add_u32 s68, s66, 0x1000\n"
    "v_mfma_f32_16x16x32_bf16 a[92:95], a[156:159], v[176:179], a[92:95]\n"
    "buffer_load_dwordx4 v[48:51], v248, s[40:43], s68 offen sc1\n"
    "v_mfma_f32_16x16x32_bf16 a[96:99], a[160:163], v[168:171], a[96:99]\n"
    "ds_read_b64_tr_b16 a[206:207], v245 offset:27904\n"
    "v_mfma_f32_16x16x32_bf16 a[100:103], a[160:163], v[172:175], a[100:103]\n"
    "v_mfma_f32_16x16x32_bf16 a[104:107], a[160:163], v[176:179], a[104:107]\n"
    "s_add_u32 s69, s66, 0x3000\n"
    "v_mfma_f32_16x16x32_bf16 a[108:111], a[164:167], v[168:171], a[108:111]\n"
    "buffer_load_dwordx4 v[52:55], v248, s[40:43], s69 offen sc1\n"
    "v_mfma_f32_16x16x32_bf16 a[112:115], a[164:167], v[172:175], a[112:115]\n"
    "v_mfma_f32_16x16x32_bf16 a[116:119], a[164:167], v[176:179], a[116:119]\n"
    "s_waitcnt vmcnt(29) lgkmcnt(0)\n"
    "v_mfma_f32_16x16x32_bf16 a[0:3], a[168:171], v[180:183], a[0:3]\n"
    "s_waitcnt vmcnt(19)\n"
    "s_barrier\n"
    "v_mfma_f32_16x16x32_bf16 a[4:7], a[168:171], v[184:187], a[4:7]\n"
    "ds_read_b64_tr_b16 a[128:129], v234 offset:0\n"
    "v_mfma_f32_16x16x32_bf16 a[8:11], a[168:171], v[188:191], a[8:11]\n"
    "ds_read_b64_tr_b16 a[130:131], v234 offset:5120\n"
    "s_add_u32 s70, s66, 0x5000\n"
    "v_mfma_f32_16x16x32_bf16 a[12:15], a[172:175], v[180:183], a[12:15]\n"
    "buffer_load_dwordx4 v[56:59], v248, s[40:43], s70 offen sc1\n"
    "v_mfma_f32_16x16x32_bf16 a[16:19], a[172:175], v[184:187], a[16:19]\n"
    "ds_read_b64_tr_b16 a[132:133], v235 offset:0\n"
    "v_mfma_f32_16x16x32_bf16 a[20:23], a[172:175], v[188:191], a[20:23]\n"
    "ds_read_b64_tr_b16 a[134:135], v235 offset:5120\n"
    "v_mfma_f32_16x16x32_bf16 a[24:27], a[176:179], v[180:183], a[24:27]\n"
    "ds_read_b64_tr_b16 a[136:137], v234 offset:64\n"
    "v_mfma_f32_16x16x32_bf16 a[28:31], a[176:179], v[184:187], a[28:31]\n"
    "ds_read_b64_tr_b16 a[138:139], v234 offset:5184\n"
    "v_mfma_f32_16x16x32_bf16 a[32:35], a[176:179], v[188:191], a[32:35]\n"
    "buffer_load_dwordx4 v[60:63], v248, s[40:43], s68 offen offset:1024 sc1\n"
    "v_mfma_f32_16x16x32_bf16 a[36:39], a[180:183], v[180:183], a[36:39]\n"
    "ds_read_b64_tr_b16 a[140:141], v235 offset:64\n"
    "v_mfma_f32_16x16x32_bf16 a[40:43], a[180:183], v[184:187], a[40:43]\n"
    "ds_read_b64_tr_b16 a[142:143], v235 offset:5184\n"
    "v_mfma_f32_16x16x32_bf16 a[44:47], a[180:183], v[188:191], a[44:47]\n"
    "ds_read_b64_tr_b16 a[144:145], v234 offset:128\n"
    "v_mfma_f32_16x16x32_bf16 a[48:51], a[184:187], v[180:183], a[48:51]\n"
    "ds_read_b64_tr_b16 a[146:147], v234 offset:5248\n"
    "v_mfma_f32_16x16x32_bf16 a[52:55], a[184:187], v[184:187], a[52:55]\n"
    "buffer_load_dwordx4 v[64:67], v248, s[40:43], s69 offen offset:1024 sc1\n"
    "v_mfma_f32_16x16x32_bf16 a[56:59], a[184:187], v[188:191], a[56:59]\n"
    "ds_read_b64_tr_b16 a[148:149], v235 offset:128\n"
    "v_mfma_f32_16x16x32_bf16 a[60:63], a[188:191], v[180:183], a[60:63]\n"
    "ds_read_b64_tr_b16 a[150:151], v235 offset:5248\n"
    "v_mfma_f32_16x16x32_bf16 a[64:67], a[188:191], v[184:187], a[64:67]\n"
    "ds_read_b64_tr_b16 a[152:153], v234 offset:192\n"
    "v_mfma_f32_16x16x32_bf16 a[68:71], a[188:191], v[188:191], a[68:71]\n"
    "ds_read_b64_tr_b16 a[154:155], v234 offset:5312\n"
    "v_mfma_f32_16x16x32_bf16 a[72:75], a[192:195], v[180:183], a[72:75]\n"
    "buffer_load_dwordx4 v[68:71], v248, s[40:43], s70 offen offset:1024 sc1\n"
    "v_mfma_f32_16x16x32_bf16 a[76:79], a[192:195], v[184:187], a[76:79]\n"
    "ds_read_b64_tr_b16 a[156:157], v235 offset:192\n"
    "v_mfma_f32_16x16x32_bf16 a[80:83], a[192:195], v[188:191], a[80:83]\n"
    "ds_read_b64_tr_b16 a[158:159], v235 offset:5312\n"
    "v_mfma_f32_16x16x32_bf16 a[84:87], a[196:199], v[180:183], a[84:87]\n"
    "ds_read_b64_tr_b16 a[160:161], v234 offset:256\n"
    "s_add_u32 m0, s80, 0xf000\n"
    "v_mfma_f32_16x16x32_bf16 a[88:91], a[196:199], v[184:187], a[88:91]\n"
    "ds_read_b64_tr_b16 a[162:163], v234 offset:5376\n"
    "s_add_u32 s62, s50, 0x3c000\n"
    "v_mfma_f32_16x16x32_bf16 a[92:95], a[196:199], v[188:191], a[92:95]\n"
    "buffer_load_dwordx4 v228, s[36:39], s62 offen lds\n"
    "v_mfma_f32_16x16x32_bf16 a[96:99], a[200:203], v[180:183], a[96:99]\n"
    "ds_read_b64_tr_b16 a[164:165], v235 offset:256\n"
    "v_mfma_f32_16x16x32_bf16 a[100:103], a[200:203], v[184:187], a[100:103]\n"
    "ds_read_b64_tr_b16 a[166:167], v235 offset:5376\n"
    "v_mfma_f32_16x16x32_bf16 a[104:107], a[200:203], v[188:191], a[104:107]\n"
    "v_mfma_f32_16x16x32_bf16 a[108:111], a[204:207], v[180:183], a[108:111]\n"
    "buffer_load_dwordx4 v229, s[36:39], s62 offen offset:1024 lds\n"
    "v_mfma_f32_16x16x32_bf16 a[112:115], a[204:207], v[184:187], a[112:115]\n"
    "v_mfma_f32_16x16x32_bf16 a[116:119], a[204:207], v[188:191], a[116:119]\n"
    "s_waitcnt vmcnt(22) lgkmcnt(0)\n"
    "v_mfma_f32_16x16x32_bf16 a[0:3], a[128:131], v[0:3], a[0:3]\n"
    "ds_read_b64_tr_b16 a[168:169], v234 offset:10240\n"
    "v_mfma_f32_16x16x32_bf16 a[4:7], a[128:131], v[4:7], a[4:7]\n"
    "ds_read_b64_tr_b16 a[170:171], v234 offset:15360\n"
    "v_mfma_f32_16x16x32_bf16 a[8:11], a[128:131], v[8:11], a[8:11]\n"
    "ds_read_b64_tr_b16 a[172:173], v235 offset:10240\n"
    "v_mfma_f32_16x16x32_bf16 a[12:15], a[132:135], v[0:3], a[12:15]\n"
    "buffer_load_dwordx4 v230, s[36:39], s62 offen offset:2048 lds\n"
    "v_mfma_f32_16x16x32_bf16 a[16:19], a[132:135], v[4:7], a[16:19]\n"
    "ds_read_b64_tr_b16 a[174:175], v235 offset:15360\n"
    "v_mfma_f32_16x16x32_bf16 a[20:23], a[132:135], v[8:11], a[20:23]\n"
    "ds_read_b64_tr_b16 a[176:177], v234 offset:10304\n"
    "v_mfma_f32_16x16x32_bf16 a[24:27], a[136:139], v[0:3], a[24:27]\n"
    "ds_read_b64_tr_b16 a[178:179], v234 offset:15424\n"
    "v_mfma_f32_16x16x32_bf16 a[28:31], a[136:139], v[4:7], a[28:31]\n"
    "ds_read_b64_tr_b16 a[180:181], v235 offset:10304\n"
    "v_mfma_f32_16x16x32_bf16 a[32:35], a[136:139], v[8:11], a[32:35]\n"
    "buffer_load_dwordx4 v231, s[36:39], s62 offen offset:3072 lds\n"
    "v_mfma_f32_16x16x32_bf16 a[36:39], a[140:143], v[0:3], a[36:39]\n"
    "ds_read_b64_tr_b16 a[182:183], v235 offset:15424\n"
    "v_mfma_f32_16x16x32_bf16 a[40:43], a[140:143], v[4:7], a[40:43]\n"
    "ds_read_b64_tr_b16 a[184:185], v234 offset:10368\n"
    "v_mfma_f32_16x16x32_bf16 a[44:47], a[140:143], v[8:11], a[44:47]\n"
    "ds_read_b64_tr_b16 a[186:187], v234 offset:15488\n"
    "v_mfma_f32_16x16x32_bf16 a[48:51], a[144:147], v[0:3], a[48:51]\n"
    "ds_read_b64_tr_b16 a[188:189], v235 offset:10368\n"
    "s_add_u32 m0, s80, 0x10000\n"
    "v_mfma_f32_16x16x32_bf16 a[52:55], a[144:147], v[4:7], a[52:55]\n"
    "buffer_load_dwordx4 v232, s[36:39], s62 offen lds\n"
    "v_mfma_f32_16x16x32_bf16 a[56:59], a[144:147], v[8:11], a[56:59]\n"
    "ds_read_b64_tr_b16 a[190:191], v235 offset:15488\n"
    "v_mfma_f32_16x16x32_bf16 a[60:63], a[148:151], v[0:3], a[60:63]\n"
    "ds_read_b64_tr_b16 a[192:193], v234 offset:10432\n"
    "v_mfma_f32_16x16x32_bf16 a[64:67], a[148:151], v[4:7], a[64:67]\n"
    "ds_read_b64_tr_b16 a[194:195], v234 offset:15552\n"
    "v_mfma_f32_16x16x32_bf16 a[68:71], a[148:151], v[8:11], a[68:71]\n"
    "ds_read_b64_tr_b16 a[196:197], v235 offset:10432\n"
    "v_mfma_f32_16x16x32_bf16 a[72:75], a[152:155], v[0:3], a[72:75]\n"
    "buffer_load_dwordx4 v[72:75], v248, s[40:43], s68 offen offset:2048 sc1\n"
    "v_mfma_f32_16x16x32_bf16 a[76:79], a[152:155], v[4:7], a[76:79]\n"
    "ds_read_b64_tr_b16 a[198:199], v235 offset:15552\n"
    "v_mfma_f32_16x16x32_bf16 a[80:83], a[152:155], v[8:11], a[80:83]\n"
    "ds_read_b64_tr_b16 a[200:201], v234 offset:10496\n"
    "v_mfma_f32_16x16x32_bf16 a[84:87], a[156:159], v[0:3], a[84:87]\n"
    "ds_read_b64_tr_b16 a[202:203], v234 offset:15616\n"
    "v_mfma_f32_16x16x32_bf16 a[88:91], a[156:159], v[4:7], a[88:91]\n"
    "ds_read_b64_tr_b16 a[204:205], v235 offset:10496\n"
    "v_mfma_f32_16x16x32_bf16 a[92:95], a[156:159], v[8:11], a[92:95]\n"
    "buffer_load_dwordx4 v[76:79], v248, s[40:43], s69 offen offset:2048 sc1\n"
    "v_mfma_f32_16x16x32_bf16 a[96:99], a[160:163], v[0:3], a[96:99]\n"
    "ds_read_b64_tr_b16 a[206:207], v235 offset:15616\n"
    "v_mfma_f32_16x16x32_bf16 a[100:103], a[160:163], v[4:7], a[100:103]\n"
    "v_mfma_f32_16x16x32_bf16 a[104:107], a[160:163], v[8:11], a[104:107]\n"
    "v_mfma_f32_16x16x32_bf16 a[108:111], a[164:167], v[0:3], a[108:111]\n"
    "buffer_load_dwordx4 v[80:83], v248, s[40:43], s70 offen offset:2048 sc1\n"
    "v_mfma_f32_16x16x32_bf16 a[112:115], a[164:167], v[4:7], a[112:115]\n"
    "v_mfma_f32_16x16x32_bf16 a[116:119], a[164:167], v[8:11], a[116:119]\n"
    "s_waitcnt vmcnt(25) lgkmcnt(0)\n"
    "v_mfma_f32_16x16x32_bf16 a[0:3], a[168:171], v[12:15], a[0:3]\n"
    "ds_read_b64_tr_b16 a[128:129], v234 offset:20480\n"
    "v_mfma_f32_16x16x32_bf16 a[4:7], a[168:171], v[16:19], a[4:7]\n"
    "ds_read_b64_tr_b16 a[130:131], v234 offset:25600\n"
    "v_mfma_f32_16x16x32_bf16 a[8:11], a[168:171], v[20:23], a[8:11]\n"
    "ds_read_b64_tr_b16 a[132:133], v235 offset:20480\n"
    "v_mfma_f32_16x16x32_bf16 a[12:15], a[172:175], v[12:15], a[12:15]\n"
    "buffer_load_dwordx4 v[84:87], v248, s[40:43], s68 offen offset:3072 sc1\n"
    "v_mfma_f32_16x16x32_bf16 a[16:19], a[172:175], v[16:19], a[16:19]\n"
    "ds_read_b64_tr_b16 a[134:135], v235 offset:25600\n"
    "v_mfma_f32_16x16x32_bf16 a[20:23], a[172:175], v[20:23], a[20:23]\n"
    "ds_read_b64_tr_b16 a[136:137], v234 offset:20544\n"
    "v_mfma_f32_16x16x32_bf16 a[24:27], a[176:179], v[12:15], a[24:27]\n"
    "ds_read_b64_tr_b16 a[138:139], v234 offset:25664\n"
    "v_mfma_f32_16x16x32_bf16 a[28:31], a[176:179], v[16:19], a[28:31]\n"
    "ds_read_b64_tr_b16 a[140:141], v235 offset:20544\n"
    "v_mfma_f32_16x16x32_bf16 a[32:35], a[176:179], v[20:23], a[32:35]\n"
    "buffer_load_dwordx4 v[88:91], v248, s[40:43], s69 offen offset:3072 sc1\n"
    "v_mfma_f32_16x16x32_bf16 a[36:39], a[180:183], v[12:15], a[36:39]\n"
    "ds_read_b64_tr_b16 a[142:143], v235 offset:25664\n"
    "v_mfma_f32_16x16x32_bf16 a[40:43], a[180:183], v[16:19], a[40:43]\n"
    "ds_read_b64_tr_b16 a[144:145], v234 offset:20608\n"
    "v_mfma_f32_16x16x32_bf16 a[44:47], a[180:183], v[20:23], a[44:47]\n"
    "ds_read_b64_tr_b16 a[146:147], v234 offset:25728\n"
    "v_mfma_f32_16x16x32_bf16 a[48:51], a[184:187], v[12:15], a[48:51]\n"
    "ds_read_b64_tr_b16 a[148:149], v235 offset:20608\n"
    "v_mfma_f32_16x16x32_bf16 a[52:55], a[184:187], v[16:19], a[52:55]\n"
    "buffer_load_dwordx4 v[92:95], v248, s[40:43], s70 offen offset:3072 sc1\n"
    "v_mfma_f32_16x16x32_bf16 a[56:59], a[184:187], v[20:23], a[56:59]\n"
    "ds_read_b64_tr_b16 a[150:151], v235 offset:25728\n"
    "v_mfma_f32_16x16x32_bf16 a[60:63], a[188:191], v[12:15], a[60:63]\n"
    "ds_read_b64_tr_b16 a[152:153], v234 offset:20672\n"
    "v_mfma_f32_16x16x32_bf16 a[64:67], a[188:191], v[16:19], a[64:67]\n"
    "ds_read_b64_tr_b16 a[154:155], v234 offset:25792\n"
    "s_add_u32 m0, s80, 0x14000\n"
    "v_mfma_f32_16x16x32_bf16 a[68:71], a[188:191], v[20:23], a[68:71]\n"
    "ds_read_b64_tr_b16 a[156:157], v235 offset:20672\n"
    "s_add_u32 s62, s51, 0x0\n"
    "v_mfma_f32_16x16x32_bf16 a[72:75], a[192:195], v[12:15], a[72:75]\n"
    "buffer_load_dwordx4 v228, s[36:39], s62 offen lds\n"
    "v_mfma_f32_16x16x32_bf16 a[76:79], a[192:195], v[16:19], a[76:79]\n"
    "ds_read_b64_tr_b16 a[158:159], v235 offset:25792\n"
    "v_mfma_f32_16x16x32_bf16 a[80:83], a[192:195], v[20:23], a[80:83]\n"
    "ds_read_b64_tr_b16 a[160:161], v234 offset:20736\n"
    "v_mfma_f32_16x16x32_bf16 a[84:87], a[196:199], v[12:15], a[84:87]\n"
    "ds_read_b64_tr_b16 a[162:163], v234 offset:25856\n"
    "v_mfma_f32_16x16x32_bf16 a[88:91], a[196:199], v[16:19], a[88:91]\n"
    "ds_read_b64_tr_b16 a[164:165], v235 offset:20736\n"
    "v_mfma_f32_16x16x32_bf16 a[92:95], a[196:199], v[20:23], a[92:95]\n"
    "buffer_load_dwordx4 v229, s[36:39], s62 offen offset:1024 lds\n"
    "v_mfma_f32_16x16x32_bf16 a[96:99], a[200:203], v[12:15], a[96:99]\n"
    "ds_read_b64_tr_b16 a[166:167], v235 offset:25856\n"
    "v_mfma_f32_16x16x32_bf16 a[100:103], a[200:203], v[16:19], a[100:103]\n"
    "v_mfma_f32_16x16x32_bf16 a[104:107], a[200:203], v[20:23], a[104:107]\n"
    "v_mfma_f32_16x16x32_bf16 a[108:111], a[204:207], v[12:15], a[108:111]\n"
    "buffer_load_dwordx4 v230, s[36:39], s62 offen offset:2048 lds\n"
    "v_mfma_f32_16x16x32_bf16 a[112:115], a[204:207], v[16:19], a[112:115]\n"
    "v_mfma_f32_16x16x32_bf16 a[116:119], a[204:207], v[20:23], a[116:119]\n"
    "s_waitcnt vmcnt(28) lgkmcnt(0)\n"
    "v_mfma_f32_16x16x32_bf16 a[0:3], a[128:131], v[24:27], a[0:3]\n"
    "ds_read_b64_tr_b16 a[168:169], v234 offset:30720\n"
    "v_mfma_f32_16x16x32_bf16 a[4:7], a[128:131], v[28:31], a[4:7]\n"
    "ds_read_b64_tr_b16 a[170:171], v234 offset:35840\n"
    "v_mfma_f32_16x16x32_bf16 a[8:11], a[128:131], v[32:35], a[8:11]\n"
    "ds_read_b64_tr_b16 a[172:173], v235 offset:30720\n"
    "v_mfma_f32_16x16x32_bf16 a[12:15], a[132:135], v[24:27], a[12:15]\n"
    "buffer_load_dwordx4 v231, s[36:39], s62 offen offset:3072 lds\n"
    "v_mfma_f32_16x16x32_bf16 a[16:19], a[132:135], v[28:31], a[16:19]\n"
    "ds_read_b64_tr_b16 a[174:175], v235 offset:35840\n"
    "v_mfma_f32_16x16x32_bf16 a[20:23], a[132:135], v[32:35], a[20:23]\n"
    "ds_read_b64_tr_b16 a[176:177], v234 offset:30784\n"
    "v_mfma_f32_16x16x32_bf16 a[24:27], a[136:139], v[24:27], a[24:27]\n"
    "ds_read_b64_tr_b16 a[178:179], v234 offset:35904\n"
    "v_mfma_f32_16x16x32_bf16 a[28:31], a[136:139], v[28:31], a[28:31]\n"
    "ds_read_b64_tr_b16 a[180:181], v235 offset:30784\n"
    "s_add_u32 m0, s80, 0x15000\n"
    "v_mfma_f32_16x16x32_bf16 a[32:35], a[136:139], v[32:35], a[32:35]\n"
    "buffer_load_dwordx4 v232, s[36:39], s62 offen lds\n"
    "v_mfma_f32_16x16x32_bf16 a[36:39], a[140:143], v[24:27], a[36:39]\n"
    "ds_read_b64_tr_b16 a[182:183], v235 offset:35904\n"
    "v_mfma_f32_16x16x32_bf16 a[40:43], a[140:143], v[28:31], a[40:43]\n"
    "ds_read_b64_tr_b16 a[184:185], v234 offset:30848\n"
    "v_mfma_f32_16x16x32_bf16 a[44:47], a[140:143], v[32:35], a[44:47]\n"
    "ds_read_b64_tr_b16 a[186:187], v234 offset:35968\n"
    "s_add_u32 m0, s80, 0x19000\n"
    "v_mfma_f32_16x16x32_bf16 a[48:51], a[144:147], v[24:27], a[48:51]\n"
    "ds_read_b64_tr_b16 a[188:189], v235 offset:30848\n"
    "s_add_u32 s62, s51, 0x14000\n"
    "v_mfma_f32_16x16x32_bf16 a[52:55], a[144:147], v[28:31], a[52:55]\n"
    "buffer_load_dwordx4 v228, s[36:39], s62 offen lds\n"
    "v_mfma_f32_16x16x32_bf16 a[56:59], a[144:147], v[32:35], a[56:59]\n"
    "ds_read_b64_tr_b16 a[190:191], v235 offset:35968\n"
    "v_mfma_f32_16x16x32_bf16 a[60:63], a[148:151], v[24:27], a[60:63]\n"
    "ds_read_b64_tr_b16 a[192:193], v234 offset:30912\n"
    "v_mfma_f32_16x16x32_bf16 a[64:67], a[148:151], v[28:31], a[64:67]\n"
    "ds_read_b64_tr_b16 a[194:195], v234 offset:36032\n"
    "v_mfma_f32_16x16x32_bf16 a[68:71], a[148:151], v[32:35], a[68:71]\n"
    "ds_read_b64_tr_b16 a[196:197], v235 offset:30912\n"
    "v_mfma_f32_16x16x32_bf16 a[72:75], a[152:155], v[24:27], a[72:75]\n"
    "buffer_load_dwordx4 v229, s[36:39], s62 offen offset:1024 lds\n"
    "v_mfma_f32_16x16x32_bf16 a[76:79], a[152:155], v[28:31], a[76:79]\n"
    "ds_read_b64_tr_b16 a[198:199], v235 offset:36032\n"
    "v_mfma_f32_16x16x32_bf16 a[80:83], a[152:155], v[32:35], a[80:83]\n"
    "ds_read_b64_tr_b16 a[200:201], v234 offset:30976\n"
    "v_mfma_f32_16x16x32_bf16 a[84:87], a[156:159], v[24:27], a[84:87]\n"
    "ds_read_b64_tr_b16 a[202:203], v234 offset:36096\n"
    "v_mfma_f32_16x16x32_bf16 a[88:91], a[156:159], v[28:31], a[88:91]\n"
    "ds_read_b64_tr_b16 a[204:205], v235 offset:30976\n"
    "v_mfma_f32_16x16x32_bf16 a[92:95], a[156:159], v[32:35], a[92:95]\n"
    "buffer_load_dwordx4 v230, s[36:39], s62 offen offset:2048 lds\n"
    "v_mfma_f32_16x16x32_bf16 a[96:99], a[160:163], v[24:27], a[96:99]\n"
    "ds_read_b64_tr_b16 a[206:207], v235 offset:36096\n"
    "v_mfma_f32_16x16x32_bf16 a[100:103], a[160:163], v[28:31], a[100:103]\n"
    "v_mfma_f32_16x16x32_bf16 a[104:107], a[160:163], v[32:35], a[104:107]\n"
    "v_mfma_f32_16x16x32_bf16 a[108:111], a[164:167], v[24:27], a[108:111]\n"
    "buffer_load_dwordx4 v231, s[36:39], s62 offen offset:3072 lds\n"
    "v_mfma_f32_16x16x32_bf16 a[112:115], a[164:167], v[28:31], a[112:115]\n"
    "v_mfma_f32_16x16x32_bf16 a[116:119], a[164:167], v[32:35], a[116:119]\n"
    "s_waitcnt vmcnt(31) lgkmcnt(0)\n"
    "v_mfma_f32_16x16x32_bf16 a[0:3], a[168:171], v[36:39], a[0:3]\n"
    "s_waitcnt vmcnt(26)\n"
    "s_barrier\n"
    "v_mfma_f32_16x16x32_bf16 a[4:7], a[168:171], v[40:43], a[4:7]\n"
    "ds_read_b64_tr_b16 a[128:129], v234 offset:40960\n"
    "v_mfma_f32_16x16x32_bf16 a[8:11], a[168:171], v[44:47], a[8:11]\n"
    "ds_read_b64_tr_b16 a[130:131], v234 offset:46080\n"
    "s_add_u32 m0, s80, 0x1a000\n"
    "v_mfma_f32_16x16x32_bf16 a[12:15], a[172:175], v[36:39], a[12:15]\n"
    "buffer_load_dwordx4 v232, s[36:39], s62 offen lds\n"
    "v_mfma_f32_16x16x32_bf16 a[16:19], a[172:175], v[40:43], a[16:19]\n"
    "ds_read_b64_tr_b16 a[132:133], v235 offset:40960\n"
    "v_mfma_f32_16x16x32_bf16 a[20:23], a[172:175], v[44:47], a[20:23]\n"
    "ds_read_b64_tr_b16 a[134:135], v235 offset:46080\n"
    "v_mfma_f32_16x16x32_bf16 a[24:27], a[176:179], v[36:39], a[24:27]\n"
    "ds_read_b64_tr_b16 a[136:137], v234 offset:41024\n"
    "s_add_u32 m0, s80, 0x1e000\n"
    "v_mfma_f32_16x16x32_bf16 a[28:31], a[176:179], v[40:43], a[28:31]\n"
    "ds_read_b64_tr_b16 a[138:139], v234 offset:46144\n"
    "s_add_u32 s62, s51, 0x28000\n"
    "v_mfma_f32_16x16x32_bf16 a[32:35], a[176:179], v[44:47], a[32:35]\n"
    "buffer_load_dwordx4 v228, s[36:39], s62 offen lds\n"
    "v_mfma_f32_16x16x32_bf16 a[36:39], a[180:183], v[36:39], a[36:39]\n"
    "ds_read_b64_tr_b16 a[140:141], v235 offset:41024\n"
    "v_mfma_f32_16x16x32_bf16 a[40:43], a[180:183], v[40:43], a[40:43]\n"
    "ds_read_b64_tr_b16 a[142:143], v235 offset:46144\n"
    "v_mfma_f32_16x16x32_bf16 a[44:47], a[180:183], v[44:47], a[44:47]\n"
    "ds_read_b64_tr_b16 a[144:145], v234 offset:41088\n"
    "v_mfma_f32_16x16x32_bf16 a[48:51], a[184:187], v[36:39], a[48:51]\n"
    "ds_read_b64_tr_b16 a[146:147], v234 offset:46208\n"
    "v_mfma_f32_16x16x32_bf16 a[52:55], a[184:187], v[40:43], a[52:55]\n"
    "buffer_load_dwordx4 v229, s[36:39], s62 offen offset:1024 lds\n"
    "v_mfma_f32_16x16x32_bf16 a[56:59], a[184:187], v[44:47], a[56:59]\n"
    "ds_read_b64_tr_b16 a[148:149], v235 offset:41088\n"
    "v_mfma_f32_16x16x32_bf16 a[60:63], a[188:191], v[36:39], a[60:63]\n"
    "ds_read_b64_tr_b16 a[150:151], v235 offset:46208\n"
    "v_mfma_f32_16x16x32_bf16 a[64:67], a[188:191], v[40:43], a[64:67]\n"
    "ds_read_b64_tr_b16 a[152:153], v234 offset:41152\n"
    "v_mfma_f32_16x16x32_bf16 a[68:71], a[188:191], v[44:47], a[68:71]\n"
    "ds_read_b64_tr_b16 a[154:155], v234 offset:46272\n"
    "v_mfma_f32_16x16x32_bf16 a[72:75], a[192:195], v[36:39], a[72:75]\n"
    "buffer_load_dwordx4 v230, s[36:39], s62 offen offset:2048 lds\n"
    "v_mfma_f32_16x16x32_bf16 a[76:79], a[192:195], v[40:43], a[76:79]\n"
    "ds_read_b64_tr_b16 a[156:157], v235 offset:41152\n"
    "v_mfma_f32_16x16x32_bf16 a[80:83], a[192:195], v[44:47], a[80:83]\n"
    "ds_read_b64_tr_b16 a[158:159], v235 offset:46272\n"
    "v_mfma_f32_16x16x32_bf16 a[84:87], a[196:199], v[36:39], a[84:87]\n"
    "ds_read_b64_tr_b16 a[160:161], v234 offset:41216\n"
    "v_mfma_f32_16x16x32_bf16 a[88:91], a[196:199], v[40:43], a[88:91]\n"
    "ds_read_b64_tr_b16 a[162:163], v234 offset:46336\n"
    "v_mfma_f32_16x16x32_bf16 a[92:95], a[196:199], v[44:47], a[92:95]\n"
    "buffer_load_dwordx4 v231, s[36:39], s62 offen offset:3072 lds\n"
    "v_mfma_f32_16x16x32_bf16 a[96:99], a[200:203], v[36:39], a[96:99]\n"
    "ds_read_b64_tr_b16 a[164:165], v235 offset:41216\n"
    "v_mfma_f32_16x16x32_bf16 a[100:103], a[200:203], v[40:43], a[100:103]\n"
    "ds_read_b64_tr_b16 a[166:167], v235 offset:46336\n"
    "v_mfma_f32_16x16x32_bf16 a[104:107], a[200:203], v[44:47], a[104:107]\n"
    "s_add_u32 m0, s80, 0x1f000\n"
    "v_mfma_f32_16x16x32_bf16 a[108:111], a[204:207], v[36:39], a[108:111]\n"
    "buffer_load_dwordx4 v232, s[36:39], s62 offen lds\n"
    "v_mfma_f32_16x16x32_bf16 a[112:115], a[204:207], v[40:43], a[112:115]\n"
    "v_mfma_f32_16x16x32_bf16 a[116:119], a[204:207], v[44:47], a[116:119]\n"
    "s_waitcnt vmcnt(29) lgkmcnt(0)\n"
    "v_mfma_f32_16x16x32_bf16 a[0:3], a[128:131], v[48:51], a[0:3]\n"
    "ds_read_b64_tr_b16 a[168:169], v234 offset:51200\n"
    "v_mfma_f32_16x16x32_bf16 a[4:7], a[128:131], v[52:55], a[4:7]\n"
    "ds_read_b64_tr_b16 a[170:171], v234 offset:56320\n"
    "v_mfma_f32_16x16x32_bf16 a[8:11], a[128:131], v[56:59], a[8:11]\n"
    "ds_read_b64_tr_b16 a[172:173], v235 offset:51200\n"
    "s_add_u32 s68, s67, 0x0\n"
    "v_mfma_f32_16x16x32_bf16 a[12:15], a[132:135], v[48:51], a[12:15]\n"
    "buffer_load_dwordx4 v[96:99], v248, s[40:43], s68 offen sc1\n"
    "v_mfma_f32_16x16x32_bf16 a[16:19], a[132:135], v[52:55], a[16:19]\n"
    "ds_read_b64_tr_b16 a[174:175], v235 offset:56320\n"
    "v_mfma_f32_16x16x32_bf16 a[20:23], a[132:135], v[56:59], a[20:23]\n"
    "ds_read_b64_tr_b16 a[176:177], v234 offset:51264\n"
    "v_mfma_f32_16x16x32_bf16 a[24:27], a[136:139], v[48:51], a[24:27]\n"
    "ds_read_b64_tr_b16 a[178:179], v234 offset:56384\n"
    "v_mfma_f32_16x16x32_bf16 a[28:31], a[136:139], v[52:55], a[28:31]\n"
    "ds_read_b64_tr_b16 a[180:181], v235 offset:51264\n"
    "s_add_u32 s69, s67, 0x2000\n"
    "v_mfma_f32_16x16x32_bf16 a[32:35], a[136:139], v[56:59], a[32:35]\n"
    "buffer_load_dwordx4 v[100:103], v248, s[40:43], s69 offen sc1\n"
    "v_mfma_f32_16x16x32_bf16 a[36:39], a[140:143], v[48:51], a[36:39]\n"
    "ds_read_b64_tr_b16 a[182:183], v235 offset:56384\n"
    "v_mfma_f32_16x16x32_bf16 a[40:43], a[140:143], v[52:55], a[40:43]\n"
    "ds_read_b64_tr_b16 a[184:185], v234 offset:51328\n"
    "v_mfma_f32_16x16x32_bf16 a[44:47], a[140:143], v[56:59], a[44:47]\n"
    "ds_read_b64_tr_b16 a[186:187], v234 offset:56448\n"
    "v_mfma_f32_16x16x32_bf16 a[48:51], a[144:147], v[48:51], a[48:51]\n"
    "ds_read_b64_tr_b16 a[188:189], v235 offset:51328\n"
    "s_add_u32 s70, s67, 0x4000\n"
    "v_mfma_f32_16x16x32_bf16 a[52:55], a[144:147], v[52:55], a[52:55]\n"
    "buffer_load_dwordx4 v[104:107], v248, s[40:43], s70 offen sc1\n"
    "v_mfma_f32_16x16x32_bf16 a[56:59], a[144:147], v[56:59], a[56:59]\n"
    "ds_read_b64_tr_b16 a[190:191], v235 offset:56448\n"
    "v_mfma_f32_16x16x32_bf16 a[60:63], a[148:151], v[48:51], a[60:63]\n"
    "ds_read_b64_tr_b16 a[192:193], v234 offset:51392\n"
    "v_mfma_f32_16x16x32_bf16 a[64:67], a[148:151], v[52:55], a[64:67]\n"
    "ds_read_b64_tr_b16 a[194:195], v234 offset:56512\n"
    "v_mfma_f32_16x16x32_bf16 a[68:71], a[148:151], v[56:59], a[68:71]\n"
    "ds_read_b64_tr_b16 a[196:197], v235 offset:51392\n"
    "v_mfma_f32_16x16x32_bf16 a[72:75], a[152:155], v[48:51], a[72:75]\n"
    "buffer_load_dwordx4 v[108:111], v248, s[40:43], s68 offen offset:1024 sc1\n"
    "v_mfma_f32_16x16x32_bf16 a[76:79], a[152:155], v[52:55], a[76:79]\n"
    "ds_read_b64_tr_b16 a[198:199], v235 offset:56512\n"
    "v_mfma_f32_16x16x32_bf16 a[80:83], a[152:155], v[56:59], a[80:83]\n"
    "ds_read_b64_tr_b16 a[200:201], v234 offset:51456\n"
    "v_mfma_f32_16x16x32_bf16 a[84:87], a[156:159], v[48:51], a[84:87]\n"
    "ds_read_b64_tr_b16 a[202:203], v234 offset:56576\n"
    "v_mfma_f32_16x16x32_bf16 a[88:91], a[156:159], v[52:55], a[88:91]\n"
    "ds_read_b64_tr_b16 a[204:205], v235 offset:51456\n"
    "v_mfma_f32_16x16x32_bf16 a[92:95], a[156:159], v[56:59], a[92:95]\n"
    "buffer_load_dwordx4 v[112:115], v248, s[40:43], s69 offen offset:1024 sc1\n"
    "v_mfma_f32_16x16x32_bf16 a[96:99], a[160:163], v[48:51], a[96:99]\n"
    "ds_read_b64_tr_b16 a[206:207], v235 offset:56576\n"
    "v_mfma_f32_16x16x32_bf16 a[100:103], a[160:163], v[52:55], a[100:103]\n"
    "v_mfma_f32_16x16x32_bf16 a[104:107], a[160:163], v[56:59], a[104:107]\n"
    "v_mfma_f32_16x16x32_bf16 a[108:111], a[164:167], v[48:51], a[108:111]\n"
    "buffer_load_dwordx4 v[116:119], v248, s[40:43], s70 offen offset:1024 sc1\n"
    "v_mfma_f32_16x16x32_bf16 a[112:115], a[164:167], v[52:55], a[112:115]\n"
    "v_mfma_f32_16x16x32_bf16 a[116:119], a[164:167], v[56:59], a[116:119]\n"
    "s_waitcnt vmcnt(32) lgkmcnt(0)\n"
    "v_mfma_f32_16x16x32_bf16 a[0:3], a[168:171], v[60:63], a[0:3]\n"
    "s_waitcnt vmcnt(27)\n"
    "s_barrier\n"
    "v_mfma_f32_16x16x32_bf16 a[4:7], a[168:171], v[64:67], a[4:7]\n"
    "ds_read_b64_tr_b16 a[128:129], v234 offset:61440\n"
    "v_mfma_f32_16x16x32_bf16 a[8:11], a[168:171], v[68:71], a[8:11]\n"
    "ds_read_b64_tr_b16 a[130:131], v242 offset:1024\n"
    "v_mfma_f32_16x16x32_bf16 a[12:15], a[172:175], v[60:63], a[12:15]\n"
    "buffer_load_dwordx4 v[120:123], v248, s[40:43], s68 offen offset:2048 sc1\n"
    "v_mfma_f32_16x16x32_bf16 a[16:19], a[172:175], v[64:67], a[16:19]\n"
    "ds_read_b64_tr_b16 a[132:133], v235 offset:61440\n"
    "v_mfma_f32_16x16x32_bf16 a[20:23], a[172:175], v[68:71], a[20:23]\n"
    "ds_read_b64_tr_b16 a[134:135], v243 offset:1024\n"
    "v_mfma_f32_16x16x32_bf16 a[24:27], a[176:179], v[60:63], a[24:27]\n"
    "ds_read_b64_tr_b16 a[136:137], v234 offset:61504\n"
    "v_mfma_f32_16x16x32_bf16 a[28:31], a[176:179], v[64:67], a[28:31]\n"
    "ds_read_b64_tr_b16 a[138:139], v242 offset:1088\n"
    "v_mfma_f32_16x16x32_bf16 a[32:35], a[176:179], v[68:71], a[32:35]\n"
    "buffer_load_dwordx4 v[124:127], v248, s[40:43], s69 offen offset:2048 sc1\n"
    "v_mfma_f32_16x16x32_bf16 a[36:39], a[180:183], v[60:63], a[36:39]\n"
    "ds_read_b64_tr_b16 a[140:141], v235 offset:61504\n"
    "v_mfma_f32_16x16x32_bf16 a[40:43], a[180:183], v[64:67], a[40:43]\n"
    "ds_read_b64_tr_b16 a[142:143], v243 offset:1088\n"
    "v_mfma_f32_16x16x32_bf16 a[44:47], a[180:183], v[68:71], a[44:47]\n"
    "ds_read_b64_tr_b16 a[144:145], v234 offset:61568\n"
    "v_mfma_f32_16x16x32_bf16 a[48:51], a[184:187], v[60:63], a[48:51]\n"
    "ds_read_b64_tr_b16 a[146:147], v242 offset:1152\n"
    "v_mfma_f32_16x16x32_bf16 a[52:55], a[184:187], v[64:67], a[52:55]\n"
    "buffer_load_dwordx4 v[128:131], v248, s[40:43], s70 offen offset:2048 sc1\n"
    "v_mfma_f32_16x16x32_bf16 a[56:59], a[184:187], v[68:71], a[56:59]\n"
    "ds_read_b64_tr_b16 a[148:149], v235 offset:61568\n"
    "v_mfma_f32_16x16x32_bf16 a[60:63], a[188:191], v[60:63], a[60:63]\n"
    "ds_read_b64_tr_b16 a[150:151], v243 offset:1152\n"
    "v_mfma_f32_16x16x32_bf16 a[64:67], a[188:191], v[64:67], a[64:67]\n"
    "ds_read_b64_tr_b16 a[152:153], v234 offset:61632\n"
    "v_mfma_f32_16x16x32_bf16 a[68:71], a[188:191], v[68:71], a[68:71]\n"
    "ds_read_b64_tr_b16 a[154:155], v242 offset:1216\n"
    "v_mfma_f32_16x16x32_bf16 a[72:75], a[192:195], v[60:63], a[72:75]\n"
    "buffer_load_dwordx4 v[132:135], v248, s[40:43], s68 offen offset:3072 sc1\n"
    "v_mfma_f32_16x16x32_bf16 a[76:79], a[192:195], v[64:67], a[76:79]\n"
    "ds_read_b64_tr_b16 a[156:157], v235 offset:61632\n"
    "v_mfma_f32_16x16x32_bf16 a[80:83], a[192:195], v[68:71], a[80:83]\n"
    "ds_read_b64_tr_b16 a[158:159], v243 offset:1216\n"
    "v_mfma_f32_16x16x32_bf16 a[84:87], a[196:199], v[60:63], a[84:87]\n"
    "ds_read_b64_tr_b16 a[160:161], v234 offset:61696\n"
    "v_mfma_f32_16x16x32_bf16 a[88:91], a[196:199], v[64:67], a[88:91]\n"
    "ds_read_b64_tr_b16 a[162:163], v242 offset:1280\n"
    "v_mfma_f32_16x16x32_bf16 a[92:95], a[196:199], v[68:71], a[92:95]\n"
    "buffer_load_dwordx4 v[136:139], v248, s[40:43], s69 offen offset:3072 sc1\n"
    "v_mfma_f32_16x16x32_bf16 a[96:99], a[200:203], v[60:63], a[96:99]\n"
    "ds_read_b64_tr_b16 a[164:165], v235 offset:61696\n"
    "v_mfma_f32_16x16x32_bf16 a[100:103], a[200:203], v[64:67], a[100:103]\n"
    "ds_read_b64_tr_b16 a[166:167], v243 offset:1280\n"
    "v_mfma_f32_16x16x32_bf16 a[104:107], a[200:203], v[68:71], a[104:107]\n"
    "v_mfma_f32_16x16x32_bf16 a[108:111], a[204:207], v[60:63], a[108:111]\n"
    "buffer_load_dwordx4 v[140:143], v248, s[40:43], s70 offen offset:3072 sc1\n"
    "v_mfma_f32_16x16x32_bf16 a[112:115], a[204:207], v[64:67], a[112:115]\n"
    "v_mfma_f32_16x16x32_bf16 a[116:119], a[204:207], v[68:71], a[116:119]\n"
    "s_waitcnt vmcnt(30) lgkmcnt(0)\n"
    "v_mfma_f32_16x16x32_bf16 a[0:3], a[128:131], v[72:75], a[0:3]\n"
    "ds_read_b64_tr_b16 a[168:169], v242 offset:6144\n"
    "v_mfma_f32_16x16x32_bf16 a[4:7], a[128:131], v[76:79], a[4:7]\n"
    "ds_read_b64_tr_b16 a[170:171], v242 offset:11264\n"
    "s_add_u32 m0, s80, 0x23000\n"
    "v_mfma_f32_16x16x32_bf16 a[8:11], a[128:131], v[80:83], a[8:11]\n"
    "ds_read_b64_tr_b16 a[172:173], v243 offset:6144\n"
    "s_add_u32 s62, s51, 0x3c000\n"
    "v_mfma_f32_16x16x32_bf16 a[12:15], a[132:135], v[72:75], a[12:15]\n"
    "buffer_load_dwordx4 v228, s[36:39], s62 offen lds\n"
    "v_mfma_f32_16x16x32_bf16 a[16:19], a[132:135], v[76:79], a[16:19]\n"
    "ds_read_b64_tr_b16 a[174:175], v243 offset:11264\n"
    "v_mfma_f32_16x16x32_bf16 a[20:23], a[132:135], v[80:83], a[20:23]\n"
    "ds_read_b64_tr_b16 a[176:177], v242 offset:6208\n"
    "v_mfma_f32_16x16x32_bf16 a[24:27], a[136:139], v[72:75], a[24:27]\n"
    "ds_read_b64_tr_b16 a[178:179], v242 offset:11328\n"
    "v_mfma_f32_16x16x32_bf16 a[28:31], a[136:139], v[76:79], a[28:31]\n"
    "ds_read_b64_tr_b16 a[180:181], v243 offset:6208\n"
    "v_mfma_f32_16x16x32_bf16 a[32:35], a[136:139], v[80:83], a[32:35]\n"
    "buffer_load_dwordx4 v229, s[36:39], s62 offen offset:1024 lds\n"
    "v_mfma_f32_16x16x32_bf16 a[36:39], a[140:143], v[72:75], a[36:39]\n"
    "ds_read_b64_tr_b16 a[182:183], v243 offset:11328\n"
    "v_mfma_f32_16x16x32_bf16 a[40:43], a[140:143], v[76:79], a[40:43]\n"
    "ds_read_b64_tr_b16 a[184:185], v242 offset:6272\n"
    "v_mfma_f32_16x16x32_bf16 a[44:47], a[140:143], v[80:83], a[44:47]\n"
    "ds_read_b64_tr_b16 a[186:187], v242 offset:11392\n"
    "v_mfma_f32_16x16x32_bf16 a[48:51], a[144:147], v[72:75], a[48:51]\n"
    "ds_read_b64_tr_b16 a[188:189], v243 offset:6272\n"
    "v_mfma_f32_16x16x32_bf16 a[52:55], a[144:147], v[76:79], a[52:55]\n"
    "buffer_load_dwordx4 v230, s[36:39], s62 offen offset:2048 lds\n"
    "v_mfma_f32_16x16x32_bf16 a[56:59], a[144:147], v[80:83], a[56:59]\n"
    "ds_read_b64_tr_b16 a[190:191], v243 offset:11392\n"
    "v_mfma_f32_16x16x32_bf16 a[60:63], a[148:151], v[72:75], a[60:63]\n"
    "ds_read_b64_tr_b16 a[192:193], v242 offset:6336\n"
    "v_mfma_f32_16x16x32_bf16 a[64:67], a[148:151], v[76:79], a[64:67]\n"
    "ds_read_b64_tr_b16 a[194:195], v242 offset:11456\n"
    "v_mfma_f32_16x16x32_bf16 a[68:71], a[148:151], v[80:83], a[68:71]\n"
    "ds_read_b64_tr_b16 a[196:197], v243 offset:6336\n"
    "v_mfma_f32_16x16x32_bf16 a[72:75], a[152:155], v[72:75], a[72:75]\n"
    "buffer_load_dwordx4 v231, s[36:39], s62 offen offset:3072 lds\n"
    "v_mfma_f32_16x16x32_bf16 a[76:79], a[152:155], v[76:79], a[76:79]\n"
    "ds_read_b64_tr_b16 a[198:199], v243 offset:11456\n"
    "v_mfma_f32_16x16x32_bf16 a[80:83], a[152:155], v[80:83], a[80:83]\n"
    "ds_read_b64_tr_b16 a[200:201], v242 offset:6400\n"
    "v_mfma_f32_16x16x32_bf16 a[84:87], a[156:159], v[72:75], a[84:87]\n"
    "ds_read_b64_tr_b16 a[202:203], v242 offset:11520\n"
    "v_mfma_f32_16x16x32_bf16 a[88:91], a[156:159], v[76:79], a[88:91]\n"
    "ds_read_b64_tr_b16 a[204:205], v243 offset:6400\n"
    "s_add_u32 m0, s80, 0x24000\n"
    "v_mfma_f32_16x16x32_bf16 a[92:95], a[156:159], v[80:83], a[92:95]\n"
    "buffer_load_dwordx4 v232, s[36:39], s62 offen lds\n"
    "v_mfma_f32_16x16x32_bf16 a[96:99], a[160:163], v[72:75], a[96:99]\n"
    "ds_read_b64_tr_b16 a[206:207], v243 offset:11520\n"
    "v_mfma_f32_16x16x32_bf16 a[100:103], a[160:163], v[76:79], a[100:103]\n"
    "v_mfma_f32_16x16x32_bf16 a[104:107], a[160:163], v[80:83], a[104:107]\n"
    "s_add_u32 s68, s67, 0x1000\n"
    "v_mfma_f32_16x16x32_bf16 a[108:111], a[164:167], v[72:75], a[108:111]\n"
    "buffer_load_dwordx4 v[144:147], v248, s[40:43], s68 offen sc1\n"
    "v_mfma_f32_16x16x32_bf16 a[112:115], a[164:167], v[76:79], a[112:115]\n"
    "v_mfma_f32_16x16x32_bf16 a[116:119], a[164:167], v[80:83], a[116:119]\n"
    "s_waitcnt vmcnt(33) lgkmcnt(0)\n"
    "v_mfma_f32_16x16x32_bf16 a[0:3], a[168:171], v[84:87], a[0:3]\n"
    "s_waitcnt vmcnt(18)\n"
    "s_barrier\n"
    "v_mfma_f32_16x16x32_bf16 a[4:7], a[168:171], v[88:91], a[4:7]\n"
    "ds_read_b64_tr_b16 a[128:129], v242 offset:16448\n"
    "v_mfma_f32_16x16x32_bf16 a[8:11], a[168:171], v[92:95], a[8:11]\n"
    "ds_read_b64_tr_b16 a[130:131], v242 offset:21568\n"
    "s_add_u32 s69, s67, 0x3000\n"
    "v_mfma_f32_16x16x32_bf16 a[12:15], a[172:175], v[84:87], a[12:15]\n"
    "buffer_load_dwordx4 v[148:151], v248, s[40:43], s69 offen sc1\n"
    "v_mfma_f32_16x16x32_bf16 a[16:19], a[172:175], v[88:91], a[16:19]\n"
    "ds_read_b64_tr_b16 a[132:133], v243 offset:16448\n"
    "v_mfma_f32_16x16x32_bf16 a[20:23], a[172:175], v[92:95], a[20:23]\n"
    "ds_read_b64_tr_b16 a[134:135], v243 offset:21568\n"
    "v_mfma_f32_16x16x32_bf16 a[24:27], a[176:179], v[84:87], a[24:27]\n"
    "ds_read_b64_tr_b16 a[136:137], v242 offset:26688\n"
    "v_mfma_f32_16x16x32_bf16 a[28:31], a[176:179], v[88:91], a[28:31]\n"
    "ds_read_b64_tr_b16 a[138:139], v242 offset:31808\n"
    "s_add_u32 s70, s67, 0x5000\n"
    "v_mfma_f32_16x16x32_bf16 a[32:35], a[176:179], v[92:95], a[32:35]\n"
    "buffer_load_dwordx4 v[152:155], v248, s[40:43], s70 offen sc1\n"
    "v_mfma_f32_16x16x32_bf16 a[36:39], a[180:183], v[84:87], a[36:39]\n"
    "ds_read_b64_tr_b16 a[140:141], v243 offset:26688\n"
    "v_mfma_f32_16x16x32_bf16 a[40:43], a[180:183], v[88:91], a[40:43]\n"
    "ds_read_b64_tr_b16 a[142:143], v243 offset:31808\n"
    "v_mfma_f32_16x16x32_bf16 a[44:47], a[180:183], v[92:95], a[44:47]\n"
    "ds_read_b64_tr_b16 a[144:145], v242 offset:36928\n"
    "v_mfma_f32_16x16x32_bf16 a[48:51], a[184:187], v[84:87], a[48:51]\n"
    "ds_read_b64_tr_b16 a[146:147], v242 offset:42048\n"
    "v_mfma_f32_16x16x32_bf16 a[52:55], a[184:187], v[88:91], a[52:55]\n"
    "buffer_load_dwordx4 v[156:159], v248, s[40:43], s68 offen offset:1024 sc1\n"
    "v_mfma_f32_16x16x32_bf16 a[56:59], a[184:187], v[92:95], a[56:59]\n"
    "ds_read_b64_tr_b16 a[148:149], v243 offset:36928\n"
    "v_mfma_f32_16x16x32_bf16 a[60:63], a[188:191], v[84:87], a[60:63]\n"
    "ds_read_b64_tr_b16 a[150:151], v243 offset:42048\n"
    "v_mfma_f32_16x16x32_bf16 a[64:67], a[188:191], v[88:91], a[64:67]\n"
    "ds_read_b64_tr_b16 a[152:153], v242 offset:47168\n"
    "v_mfma_f32_16x16x32_bf16 a[68:71], a[188:191], v[92:95], a[68:71]\n"
    "ds_read_b64_tr_b16 a[154:155], v242 offset:52288\n"
    "v_mfma_f32_16x16x32_bf16 a[72:75], a[192:195], v[84:87], a[72:75]\n"
    "buffer_load_dwordx4 v[160:163], v248, s[40:43], s69 offen offset:1024 sc1\n"
    "v_mfma_f32_16x16x32_bf16 a[76:79], a[192:195], v[88:91], a[76:79]\n"
    "ds_read_b64_tr_b16 a[156:157], v243 offset:47168\n"
    "v_mfma_f32_16x16x32_bf16 a[80:83], a[192:195], v[92:95], a[80:83]\n"
    "ds_read_b64_tr_b16 a[158:159], v243 offset:52288\n"
    "v_mfma_f32_16x16x32_bf16 a[84:87], a[196:199], v[84:87], a[84:87]\n"
    "ds_read_b64_tr_b16 a[160:161], v242 offset:57408\n"
    "v_mfma_f32_16x16x32_bf16 a[88:91], a[196:199], v[88:91], a[88:91]\n"
    "ds_read_b64_tr_b16 a[162:163], v242 offset:62528\n"
    "v_mfma_f32_16x16x32_bf16 a[92:95], a[196:199], v[92:95], a[92:95]\n"
    "buffer_load_dwordx4 v[164:167], v248, s[40:43], s70 offen offset:1024 sc1\n"
    "v_mfma_f32_16x16x32_bf16 a[96:99], a[200:203], v[84:87], a[96:99]\n"
    "ds_read_b64_tr_b16 a[164:165], v243 offset:57408\n"
    "v_mfma_f32_16x16x32_bf16 a[100:103], a[200:203], v[88:91], a[100:103]\n"
    "ds_read_b64_tr_b16 a[166:167], v243 offset:62528\n"
    "v_mfma_f32_16x16x32_bf16 a[104:107], a[200:203], v[92:95], a[104:107]\n"
    "v_mfma_f32_16x16x32_bf16 a[108:111], a[204:207], v[84:87], a[108:111]\n"
    "buffer_load_dwordx4 v[168:171], v248, s[40:43], s68 offen offset:2048 sc1\n"
    "v_mfma_f32_16x16x32_bf16 a[112:115], a[204:207], v[88:91], a[112:115]\n"
    "v_mfma_f32_16x16x32_bf16 a[116:119], a[204:207], v[92:95], a[116:119]\n"
    "s_waitcnt vmcnt(21) lgkmcnt(15)\n"
    "v_mfma_f32_16x16x32_bf16 a[24:27], a[128:131], v[96:99], a[24:27]\n"
    "ds_read_b64_tr_b16 a[168:169], v244 offset:2112\n"
    "v_mfma_f32_16x16x32_bf16 a[28:31], a[128:131], v[100:103], a[28:31]\n"
    "ds_read_b64_tr_b16 a[170:171], v244 offset:7232\n"
    "v_mfma_f32_16x16x32_bf16 a[32:35], a[128:131], v[104:107], a[32:35]\n"
    "buffer_load_dwordx4 v[172:175], v248, s[40:43], s69 offen offset:2048 sc1\n"
    "v_mfma_f32_16x16x32_bf16 a[36:39], a[132:135], v[96:99], a[36:39]\n"
    "ds_read_b64_tr_b16 a[172:173], v245 offset:2112\n"
    "v_mfma_f32_16x16x32_bf16 a[40:43], a[132:135], v[100:103], a[40:43]\n"
    "ds_read_b64_tr_b16 a[174:175], v245 offset:7232\n"
    "v_mfma_f32_16x16x32_bf16 a[44:47], a[132:135], v[104:107], a[44:47]\n"
    "buffer_load_dwordx4 v[176:179], v248, s[40:43], s70 offen offset:2048 sc1\n"
    "s_waitcnt vmcnt(20) lgkmcnt(15)\n"
    "v_mfma_f32_16x16x32_bf16 a[24:27], a[136:139], v[108:111], a[24:27]\n"
    "s_waitcnt vmcnt(9)\n"
    "s_barrier\n"
    "v_mfma_f32_16x16x32_bf16 a[28:31], a[136:139], v[112:115], a[28:31]\n"
    "ds_read_b64_tr_b16 a[176:177], v244 offset:12352\n"
    "ds_read_b64_tr_b16 a[178:179], v244 offset:17472\n"
    "v_mfma_f32_16x16x32_bf16 a[32:35], a[136:139], v[116:119], a[32:35]\n"
    "buffer_load_dwordx4 v[180:183], v248, s[40:43], s68 offen offset:3072 sc1\n"
    "v_mfma_f32_16x16x32_bf16 a[36:39], a[140:143], v[108:111], a[36:39]\n"
    "ds_read_b64_tr_b16 a[180:181], v245 offset:12352\n"
    "v_mfma_f32_16x16x32_bf16 a[40:43], a[140:143], v[112:115], a[40:43]\n"
    "ds_read_b64_tr_b16 a[182:183], v245 offset:17472\n"
    "v_mfma_f32_16x16x32_bf16 a[44:47], a[140:143], v[116:119], a[44:47]\n"
    "buffer_load_dwordx4 v[184:187], v248, s[40:43], s69 offen offset:3072 sc1\n"
    "s_waitcnt lgkmcnt(15)\n"
    "v_mfma_f32_16x16x32_bf16 a[24:27], a[144:147], v[120:123], a[24:27]\n"
    "ds_read_b64_tr_b16 a[184:185], v244 offset:22592\n"
    "v_mfma_f32_16x16x32_bf16 a[28:31], a[144:147], v[124:127], a[28:31]\n"
    "ds_read_b64_tr_b16 a[186:187], v244 offset:27712\n"
    "v_mfma_f32_16x16x32_bf16 a[32:35], a[144:147], v[128:131], a[32:35]\n"
    "buffer_load_dwordx4 v[188:191], v248, s[40:43], s70 offen offset:3072 sc1\n"
    "v_mfma_f32_16x16x32_bf16 a[36:39], a[148:151], v[120:123], a[36:39]\n"
    "ds_read_b64_tr_b16 a[188:189], v245 offset:22592\n"
    "v_mfma_f32_16x16x32_bf16 a[40:43], a[148:151], v[124:127], a[40:43]\n"
    "ds_read_b64_tr_b16 a[190:191], v245 offset:27712\n"
    "v_mfma_f32_16x16x32_bf16 a[44:47], a[148:151], v[128:131], a[44:47]\n"
    "s_waitcnt lgkmcnt(15)\n"
    "v_mfma_f32_16x16x32_bf16 a[24:27], a[152:155], v[132:135], a[24:27]\n"
    "ds_read_b64_tr_b16 a[192:193], v242 offset:16512\n"
    "v_mfma_f32_16x16x32_bf16 a[28:31], a[152:155], v[136:139], a[28:31]\n"
    "ds_read_b64_tr_b16 a[194:195], v242 offset:21632\n"
    "v_mfma_f32_16x16x32_bf16 a[32:35], a[152:155], v[140:143], a[32:35]\n"
    "v_mfma_f32_16x16x32_bf16 a[36:39], a[156:159], v[132:135], a[36:39]\n"
    "ds_read_b64_tr_b16 a[196:197], v243 offset:16512\n"
    "v_mfma_f32_16x16x32_bf16 a[40:43], a[156:159], v[136:139], a[40:43]\n"
    "ds_read_b64_tr_b16 a[198:199], v243 offset:21632\n"
    "v_mfma_f32_16x16x32_bf16 a[44:47], a[156:159], v[140:143], a[44:47]\n"
    "s_waitcnt vmcnt(9) lgkmcnt(15)\n"
    "v_mfma_f32_16x16x32_bf16 a[24:27], a[160:163], v[144:147], a[24:27]\n"
    "ds_read_b64_tr_b16 a[200:201], v242 offset:26752\n"
    "v_mfma_f32_16x16x32_bf16 a[28:31], a[160:163], v[148:151], a[28:31]\n"
    "ds_read_b64_tr_b16 a[202:203], v242 offset:31872\n"
    "v_mfma_f32_16x16x32_bf16 a[32:35], a[160:163], v[152:155], a[32:35]\n"
    "v_mfma_f32_16x16x32_bf16 a[36:39], a[164:167], v[144:147], a[36:39]\n"
    "ds_read_b64_tr_b16 a[204:205], v243 offset:26752\n"
    "v_mfma_f32_16x16x32_bf16 a[40:43], a[164:167], v[148:151], a[40:43]\n"
    "ds_read_b64_tr_b16 a[206:207], v243 offset:31872\n"
    "v_mfma_f32_16x16x32_bf16 a[44:47], a[164:167], v[152:155], a[44:47]\n"
    "s_waitcnt vmcnt(6) lgkmcnt(15)\n"
    "v_mfma_f32_16x16x32_bf16 a[24:27], a[168:171], v[156:159], a[24:27]\n"
    "ds_read_b64_tr_b16 a[128:129], v242 offset:36992\n"
    "v_mfma_f32_16x16x32_bf16 a[28:31], a[168:171], v[160:163], a[28:31]\n"
    "ds_read_b64_tr_b16 a[130:131], v242 offset:42112\n"
    "v_mfma_f32_16x16x32_bf16 a[32:35], a[168:171], v[164:167], a[32:35]\n"
    "v_mfma_f32_16x16x32_bf16 a[36:39], a[172:175], v[156:159], a[36:39]\n"
    "ds_read_b64_tr_b16 a[132:133], v243 offset:36992\n"
    "v_mfma_f32_16x16x32_bf16 a[40:43], a[172:175], v[160:163], a[40:43]\n"
    "ds_read_b64_tr_b16 a[134:135], v243 offset:42112\n"
    "v_mfma_f32_16x16x32_bf16 a[44:47], a[172:175], v[164:167], a[44:47]\n"
    "s_waitcnt vmcnt(3) lgkmcnt(15)\n"
    "v_mfma_f32_16x16x32_bf16 a[24:27], a[176:179], v[168:171], a[24:27]\n"
    "ds_read_b64_tr_b16 a[136:137], v242 offset:47232\n"
    "v_mfma_f32_16x16x32_bf16 a[28:31], a[176:179], v[172:175], a[28:31]\n"
    "ds_read_b64_tr_b16 a[138:139], v242 offset:52352\n"
    "v_mfma_f32_16x16x32_bf16 a[32:35], a[176:179], v[176:179], a[32:35]\n"
    "v_mfma_f32_16x16x32_bf16 a[36:39], a[180:183], v[168:171], a[36:39]\n"
    "ds_read_b64_tr_b16 a[140:141], v243 offset:47232\n"
    "v_mfma_f32_16x16x32_bf16 a[40:43], a[180:183], v[172:175], a[40:43]\n"
    "ds_read_b64_tr_b16 a[142:143], v243 offset:52352\n"
    "v_mfma_f32_16x16x32_bf16 a[44:47], a[180:183], v[176:179], a[44:47]\n"
    "s_waitcnt vmcnt(0) lgkmcnt(15)\n"
    "v_mfma_f32_16x16x32_bf16 a[24:27], a[184:187], v[180:183], a[24:27]\n"
    "ds_read_b64_tr_b16 a[144:145], v242 offset:57472\n"
    "v_mfma_f32_16x16x32_bf16 a[28:31], a[184:187], v[184:187], a[28:31]\n"
    "ds_read_b64_tr_b16 a[146:147], v242 offset:62592\n"
    "v_mfma_f32_16x16x32_bf16 a[32:35], a[184:187], v[188:191], a[32:35]\n"
    "v_mfma_f32_16x16x32_bf16 a[36:39], a[188:191], v[180:183], a[36:39]\n"
    "ds_read_b64_tr_b16 a[148:149], v243 offset:57472\n"
    "v_mfma_f32_16x16x32_bf16 a[40:43], a[188:191], v[184:187], a[40:43]\n"
    "ds_read_b64_tr_b16 a[150:151], v243 offset:62592\n"
    "v_mfma_f32_16x16x32_bf16 a[44:47], a[188:191], v[188:191], a[44:47]\n"
    "s_waitcnt lgkmcnt(15)\n"
    "v_mfma_f32_16x16x32_bf16 a[48:51], a[192:195], v[96:99], a[48:51]\n"
    "ds_read_b64_tr_b16 a[152:153], v244 offset:2176\n"
    "v_mfma_f32_16x16x32_bf16 a[52:55], a[192:195], v[100:103], a[52:55]\n"
    "ds_read_b64_tr_b16 a[154:155], v244 offset:7296\n"
    "v_mfma_f32_16x16x32_bf16 a[56:59], a[192:195], v[104:107], a[56:59]\n"
    "v_mfma_f32_16x16x32_bf16 a[60:63], a[196:199], v[96:99], a[60:63]\n"
    "ds_read_b64_tr_b16 a[156:157], v245 offset:2176\n"
    "v_mfma_f32_16x16x32_bf16 a[64:67], a[196:199], v[100:103], a[64:67]\n"
    "ds_read_b64_tr_b16 a[158:159], v245 offset:7296\n"
    "v_mfma_f32_16x16x32_bf16 a[68:71], a[196:199], v[104:107], a[68:71]\n"
    "s_waitcnt lgkmcnt(15)\n"
    "v_mfma_f32_16x16x32_bf16 a[48:51], a[200:203], v[108:111], a[48:51]\n"
    "ds_read_b64_tr_b16 a[160:161], v244 offset:12416\n"
    "v_accvgpr_read_b32 v192, a24\n"
    "v_mfma_f32_16x16x32_bf16 a[52:55], a[200:203], v[112:115], a[52:55]\n"
    "ds_read_b64_tr_b16 a[162:163], v244 offset:17536\n"
    "v_accvgpr_read_b32 v193, a25\n"
    "v_mfma_f32_16x16x32_bf16 a[56:59], a[200:203], v[116:119], a[56:59]\n"
    "v_accvgpr_read_b32 v194, a26\n"
    "v_accvgpr_read_b32 v195, a27\n"
    "v_mfma_f32_16x16x32_bf16 a[60:63], a[204:207], v[108:111], a[60:63]\n"
    "ds_read_b64_tr_b16 a[164:165], v245 offset:12416\n"
    "v_accvgpr_read_b32 v196, a36\n"
    "v_mfma_f32_16x16x32_bf16 a[64:67], a[204:207], v[112:115], a[64:67]\n"
    "ds_read_b64_tr_b16 a[166:167], v245 offset:17536\n"
    "v_accvgpr_read_b32 v197, a37\n"
    "v_mfma_f32_16x16x32_bf16 a[68:71], a[204:207], v[116:119], a[68:71]\n"
    "s_waitcnt lgkmcnt(15)\n"
    "v_mfma_f32_16x16x32_bf16 a[48:51], a[128:131], v[120:123], a[48:51]\n"
    "ds_read_b64_tr_b16 a[168:169], v244 offset:22656\n"
    "v_accvgpr_read_b32 v198, a38\n"
    "v_mfma_f32_16x16x32_bf16 a[52:55], a[128:131], v[124:127], a[52:55]\n"
    "ds_read_b64_tr_b16 a[170:171], v244 offset:27776\n"
    "v_accvgpr_read_b32 v199, a39\n"
    "v_mfma_f32_16x16x32_bf16 a[56:59], a[128:131], v[128:131], a[56:59]\n"
    "v_cvt_pk_bf16_f32 v216, v192, v193\n"
    "v_cvt_pk_bf16_f32 v217, v194, v195\n"
    "v_mfma_f32_16x16x32_bf16 a[60:63], a[132:135], v[120:123], a[60:63]\n"
    "ds_read_b64_tr_b16 a[172:173], v245 offset:22656\n"
    "v_cvt_pk_bf16_f32 v218, v196, v197\n"
    "v_mfma_f32_16x16x32_bf16 a[64:67], a[132:135], v[124:127], a[64:67]\n"
    "ds_read_b64_tr_b16 a[174:175], v245 offset:27776\n"
    "v_cvt_pk_bf16_f32 v219, v198, v199\n"
    "v_mfma_f32_16x16x32_bf16 a[68:71], a[132:135], v[128:131], a[68:71]\n"
    "s_lshr_b32 s62, s26, 1\n"
    "s_add_u32 s62, s62, 1\n"
    "s_and_b32 s62, s62, 3\n"
    "s_mul_i32 s62, s62, 0xc00\n"
    "buffer_store_dwordx4 v[216:219], v248, s[44:47], s62 offen sc1\n"
    "s_waitcnt lgkmcnt(15)\n"
    "v_mfma_f32_16x16x32_bf16 a[48:51], a[136:139], v[132:135], a[48:51]\n"
    "ds_read_b64_tr_b16 a[176:177], v242 offset:16576\n"
    "v_accvgpr_read_b32 v200, a28\n"
    "v_mfma_f32_16x16x32_bf16 a[52:55], a[136:139], v[136:139], a[52:55]\n"
    "ds_read_b64_tr_b16 a[178:179], v242 offset:21696\n"
    "v_accvgpr_read_b32 v201, a29\n"
    "v_mfma_f32_16x16x32_bf16 a[56:59], a[136:139], v[140:143], a[56:59]\n"
    "v_accvgpr_read_b32 v202, a30\n"
    "v_accvgpr_read_b32 v203, a31\n"
    "v_mfma_f32_16x16x32_bf16 a[60:63], a[140:143], v[132:135], a[60:63]\n"
    "ds_read_b64_tr_b16 a[180:181], v243 offset:16576\n"
    "v_accvgpr_read_b32 v204, a40\n"
    "v_mfma_f32_16x16x32_bf16 a[64:67], a[140:143], v[136:139], a[64:67]\n"
    "ds_read_b64_tr_b16 a[182:183], v243 offset:21696\n"
    "v_accvgpr_read_b32 v205, a41\n"
    "v_mfma_f32_16x16x32_bf16 a[68:71], a[140:143], v[140:143], a[68:71]\n"
    "s_waitcnt lgkmcnt(15)\n"
    "v_mfma_f32_16x16x32_bf16 a[48:51], a[144:147], v[144:147], a[48:51]\n"
    "ds_read_b64_tr_b16 a[184:185], v242 offset:26816\n"
    "v_accvgpr_read_b32 v206, a42\n"
    "v_mfma_f32_16x16x32_bf16 a[52:55], a[144:147], v[148:151], a[52:55]\n"
    "ds_read_b64_tr_b16 a[186:187], v242 offset:31936\n"
    "v_accvgpr_read_b32 v207, a43\n"
    "v_mfma_f32_16x16x32_bf16 a[56:59], a[144:147], v[152:155], a[56:59]\n"
    "v_cvt_pk_bf16_f32 v220, v200, v201\n"
    "v_cvt_pk_bf16_f32 v221, v202, v203\n"
    "v_mfma_f32_16x16x32_bf16 a[60:63], a[148:151], v[144:147], a[60:63]\n"
    "ds_read_b64_tr_b16 a[188:189], v243 offset:26816\n"
    "v_cvt_pk_bf16_f32 v222, v204, v205\n"
    "v_mfma_f32_16x16x32_bf16 a[64:67], a[148:151], v[148:151], a[64:67]\n"
    "ds_read_b64_tr_b16 a[190:191], v243 offset:31936\n"
    "v_cvt_pk_bf16_f32 v223, v206, v207\n"
    "v_mfma_f32_16x16x32_bf16 a[68:71], a[148:151], v[152:155], a[68:71]\n"
    "buffer_store_dwordx4 v[220:223], v248, s[44:47], s62 offen offset:1024 sc1\n"
    "s_waitcnt lgkmcnt(15)\n"
    "v_mfma_f32_16x16x32_bf16 a[48:51], a[152:155], v[156:159], a[48:51]\n"
    "ds_read_b64_tr_b16 a[192:193], v242 offset:37056\n"
    "v_accvgpr_read_b32 v208, a32\n"
    "v_mfma_f32_16x16x32_bf16 a[52:55], a[152:155], v[160:163], a[52:55]\n"
    "ds_read_b64_tr_b16 a[194:195], v242 offset:42176\n"
    "v_accvgpr_read_b32 v209, a33\n"
    "v_mfma_f32_16x16x32_bf16 a[56:59], a[152:155], v[164:167], a[56:59]\n"
    "v_accvgpr_read_b32 v210, a34\n"
    "v_accvgpr_read_b32 v211, a35\n"
    "v_mfma_f32_16x16x32_bf16 a[60:63], a[156:159], v[156:159], a[60:63]\n"
    "ds_read_b64_tr_b16 a[196:197], v243 offset:37056\n"
    "v_accvgpr_read_b32 v212, a44\n"
    "v_mfma_f32_16x16x32_bf16 a[64:67], a[156:159], v[160:163], a[64:67]\n"
    "ds_read_b64_tr_b16 a[198:199], v243 offset:42176\n"
    "v_accvgpr_read_b32 v213, a45\n"
    "v_mfma_f32_16x16x32_bf16 a[68:71], a[156:159], v[164:167], a[68:71]\n"
    "s_waitcnt lgkmcnt(15)\n"
    "v_mfma_f32_16x16x32_bf16 a[48:51], a[160:163], v[168:171], a[48:51]\n"
    "ds_read_b64_tr_b16 a[200:201], v242 offset:47296\n"
    "v_accvgpr_read_b32 v214, a46\n"
    "v_mfma_f32_16x16x32_bf16 a[52:55], a[160:163], v[172:175], a[52:55]\n"
    "ds_read_b64_tr_b16 a[202:203], v242 offset:52416\n"
    "v_accvgpr_read_b32 v215, a47\n"
    "v_mfma_f32_16x16x32_bf16 a[56:59], a[160:163], v[176:179], a[56:59]\n"
    "v_cvt_pk_bf16_f32 v224, v208, v209\n"
    "v_cvt_pk_bf16_f32 v225, v210, v211\n"
    "v_mfma_f32_16x16x32_bf16 a[60:63], a[164:167], v[168:171], a[60:63]\n"
    "ds_read_b64_tr_b16 a[204:205], v243 offset:47296\n"
    "v_cvt_pk_bf16_f32 v226, v212, v213\n"
    "v_mfma_f32_16x16x32_bf16 a[64:67], a[164:167], v[172:175], a[64:67]\n"
    "ds_read_b64_tr_b16 a[206:207], v243 offset:52416\n"
    "v_cvt_pk_bf16_f32 v227, v214, v215\n"
    "v_mfma_f32_16x16x32_bf16 a[68:71], a[164:167], v[176:179], a[68:71]\n"
    "buffer_store_dwordx4 v[224:227], v248, s[44:47], s62 offen offset:2048 sc1\n"
    "s_waitcnt lgkmcnt(15)\n"
    "v_mfma_f32_16x16x32_bf16 a[48:51], a[168:171], v[180:183], a[48:51]\n"
    "ds_read_b64_tr_b16 a[128:129], v242 offset:57536\n"
    "v_mfma_f32_16x16x32_bf16 a[52:55], a[168:171], v[184:187], a[52:55]\n"
    "ds_read_b64_tr_b16 a[130:131], v242 offset:62656\n"
    "v_mfma_f32_16x16x32_bf16 a[56:59], a[168:171], v[188:191], a[56:59]\n"
    "v_mfma_f32_16x16x32_bf16 a[60:63], a[172:175], v[180:183], a[60:63]\n"
    "ds_read_b64_tr_b16 a[132:133], v243 offset:57536\n"
    "v_mfma_f32_16x16x32_bf16 a[64:67], a[172:175], v[184:187], a[64:67]\n"
    "ds_read_b64_tr_b16 a[134:135], v243 offset:62656\n"
    "v_mfma_f32_16x16x32_bf16 a[68:71], a[172:175], v[188:191], a[68:71]\n"
    "s_waitcnt lgkmcnt(15)\n"
    "v_mfma_f32_16x16x32_bf16 a[72:75], a[176:179], v[96:99], a[72:75]\n"
    "ds_read_b64_tr_b16 a[136:137], v244 offset:2240\n"
    "v_mfma_f32_16x16x32_bf16 a[76:79], a[176:179], v[100:103], a[76:79]\n"
    "ds_read_b64_tr_b16 a[138:139], v244 offset:7360\n"
    "v_mfma_f32_16x16x32_bf16 a[80:83], a[176:179], v[104:107], a[80:83]\n"
    "v_mfma_f32_16x16x32_bf16 a[84:87], a[180:183], v[96:99], a[84:87]\n"
    "ds_read_b64_tr_b16 a[140:141], v245 offset:2240\n"
    "v_mfma_f32_16x16x32_bf16 a[88:91], a[180:183], v[100:103], a[88:91]\n"
    "ds_read_b64_tr_b16 a[142:143], v245 offset:7360\n"
    "v_mfma_f32_16x16x32_bf16 a[92:95], a[180:183], v[104:107], a[92:95]\n"
    "s_waitcnt lgkmcnt(15)\n"
    "v_mfma_f32_16x16x32_bf16 a[72:75], a[184:187], v[108:111], a[72:75]\n"
    "ds_read_b64_tr_b16 a[144:145], v244 offset:12480\n"
    "v_accvgpr_read_b32 v192, a48\n"
    "v_mfma_f32_16x16x32_bf16 a[76:79], a[184:187], v[112:115], a[76:79]\n"
    "ds_read_b64_tr_b16 a[146:147], v244 offset:17600\n"
    "v_accvgpr_read_b32 v193, a49\n"
    "v_mfma_f32_16x16x32_bf16 a[80:83], a[184:187], v[116:119], a[80:83]\n"
    "v_accvgpr_read_b32 v194, a50\n"
    "v_accvgpr_read_b32 v195, a51\n"
    "v_mfma_f32_16x16x32_bf16 a[84:87], a[188:191], v[108:111], a[84:87]\n"
    "ds_read_b64_tr_b16 a[148:149], v245 offset:12480\n"
    "v_accvgpr_read_b32 v196, a60\n"
    "v_mfma_f32_16x16x32_bf16 a[88:91], a[188:191], v[112:115], a[88:91]\n"
    "ds_read_b64_tr_b16 a[150:151], v245 offset:17600\n"
    "v_accvgpr_read_b32 v197, a61\n"
    "v_mfma_f32_16x16x32_bf16 a[92:95], a[188:191], v[116:119], a[92:95]\n"
    "s_waitcnt lgkmcnt(15)\n"
    "v_mfma_f32_16x16x32_bf16 a[72:75], a[192:195], v[120:123], a[72:75]\n"
    "ds_read_b64_tr_b16 a[152:153], v244 offset:22720\n"
    "v_accvgpr_read_b32 v198, a62\n"
    "v_mfma_f32_16x16x32_bf16 a[76:79], a[192:195], v[124:127], a[76:79]\n"
    "ds_read_b64_tr_b16 a[154:155], v244 offset:27840\n"
    "v_accvgpr_read_b32 v199, a63\n"
    "v_mfma_f32_16x16x32_bf16 a[80:83], a[192:195], v[128:131], a[80:83]\n"
    "v_cvt_pk_bf16_f32 v216, v192, v193\n"
    "v_cvt_pk_bf16_f32 v217, v194, v195\n"
    "v_mfma_f32_16x16x32_bf16 a[84:87], a[196:199], v[120:123], a[84:87]\n"
    "ds_read_b64_tr_b16 a[156:157], v245 offset:22720\n"
    "v_cvt_pk_bf16_f32 v218, v196, v197\n"
    "v_mfma_f32_16x16x32_bf16 a[88:91], a[196:199], v[124:127], a[88:91]\n"
    "ds_read_b64_tr_b16 a[158:159], v245 offset:27840\n"
    "v_cvt_pk_bf16_f32 v219, v198, v199\n"
    "v_mfma_f32_16x16x32_bf16 a[92:95], a[196:199], v[128:131], a[92:95]\n"
    "s_lshr_b32 s62, s26, 1\n"
    "s_add_u32 s62, s62, 2\n"
    "s_and_b32 s62, s62, 3\n"
    "s_mul_i32 s62, s62, 0xc00\n"
    "buffer_store_dwordx4 v[216:219], v248, s[44:47], s62 offen sc1\n"
    "s_waitcnt lgkmcnt(15)\n"
    "v_mfma_f32_16x16x32_bf16 a[72:75], a[200:203], v[132:135], a[72:75]\n"
    "ds_read_b64_tr_b16 a[160:161], v242 offset:16640\n"
    "v_accvgpr_read_b32 v200, a52\n"
    "v_mfma_f32_16x16x32_bf16 a[76:79], a[200:203], v[136:139], a[76:79]\n"
    "ds_read_b64_tr_b16 a[162:163], v242 offset:21760\n"
    "v_accvgpr_read_b32 v201, a53\n"
    "v_mfma_f32_16x16x32_bf16 a[80:83], a[200:203], v[140:143], a[80:83]\n"
    "v_accvgpr_read_b32 v202, a54\n"
    "v_accvgpr_read_b32 v203, a55\n"
    "v_mfma_f32_16x16x32_bf16 a[84:87], a[204:207], v[132:135], a[84:87]\n"
    "ds_read_b64_tr_b16 a[164:165], v243 offset:16640\n"
    "v_accvgpr_read_b32 v204, a64\n"
    "v_mfma_f32_16x16x32_bf16 a[88:91], a[204:207], v[136:139], a[88:91]\n"
    "ds_read_b64_tr_b16 a[166:167], v243 offset:21760\n"
    "v_accvgpr_read_b32 v205, a65\n"
    "v_mfma_f32_16x16x32_bf16 a[92:95], a[204:207], v[140:143], a[92:95]\n"
    "s_waitcnt lgkmcnt(15)\n"
    "v_mfma_f32_16x16x32_bf16 a[72:75], a[128:131], v[144:147], a[72:75]\n"
    "ds_read_b64_tr_b16 a[168:169], v242 offset:26880\n"
    "v_accvgpr_read_b32 v206, a66\n"
    "v_mfma_f32_16x16x32_bf16 a[76:79], a[128:131], v[148:151], a[76:79]\n"
    "ds_read_b64_tr_b16 a[170:171], v242 offset:32000\n"
    "v_accvgpr_read_b32 v207, a67\n"
    "v_mfma_f32_16x16x32_bf16 a[80:83], a[128:131], v[152:155], a[80:83]\n"
    "v_cvt_pk_bf16_f32 v220, v200, v201\n"
    "v_cvt_pk_bf16_f32 v221, v202, v203\n"
    "v_mfma_f32_16x16x32_bf16 a[84:87], a[132:135], v[144:147], a[84:87]\n"
    "ds_read_b64_tr_b16 a[172:173], v243 offset:26880\n"
    "v_cvt_pk_bf16_f32 v222, v204, v205\n"
    "v_mfma_f32_16x16x32_bf16 a[88:91], a[132:135], v[148:151], a[88:91]\n"
    "ds_read_b64_tr_b16 a[174:175], v243 offset:32000\n"
    "v_cvt_pk_bf16_f32 v223, v206, v207\n"
    "v_mfma_f32_16x16x32_bf16 a[92:95], a[132:135], v[152:155], a[92:95]\n"
    "buffer_store_dwordx4 v[220:223], v248, s[44:47], s62 offen offset:1024 sc1\n"
    "s_waitcnt lgkmcnt(15)\n"
    "v_mfma_f32_16x16x32_bf16 a[72:75], a[136:139], v[156:159], a[72:75]\n"
    "ds_read_b64_tr_b16 a[176:177], v242 offset:37120\n"
    "v_accvgpr_read_b32 v208, a56\n"
    "v_mfma_f32_16x16x32_bf16 a[76:79], a[136:139], v[160:163], a[76:79]\n"
    "ds_read_b64_tr_b16 a[178:179], v242 offset:42240\n"
    "v_accvgpr_read_b32 v209, a57\n"
    "v_mfma_f32_16x16x32_bf16 a[80:83], a[136:139], v[164:167], a[80:83]\n"
    "v_accvgpr_read_b32 v210, a58\n"
    "v_accvgpr_read_b32 v211, a59\n"
    "v_mfma_f32_16x16x32_bf16 a[84:87], a[140:143], v[156:159], a[84:87]\n"
    "ds_read_b64_tr_b16 a[180:181], v243 offset:37120\n"
    "v_accvgpr_read_b32 v212, a68\n"
    "v_mfma_f32_16x16x32_bf16 a[88:91], a[140:143], v[160:163], a[88:91]\n"
    "ds_read_b64_tr_b16 a[182:183], v243 offset:42240\n"
    "v_accvgpr_read_b32 v213, a69\n"
    "v_mfma_f32_16x16x32_bf16 a[92:95], a[140:143], v[164:167], a[92:95]\n"
    "s_waitcnt lgkmcnt(15)\n"
    "v_mfma_f32_16x16x32_bf16 a[72:75], a[144:147], v[168:171], a[72:75]\n"
    "ds_read_b64_tr_b16 a[184:185], v242 offset:47360\n"
    "v_accvgpr_read_b32 v214, a70\n"
    "v_mfma_f32_16x16x32_bf16 a[76:79], a[144:147], v[172:175], a[76:79]\n"
    "ds_read_b64_tr_b16 a[186:187], v242 offset:52480\n"
    "v_accvgpr_read_b32 v215, a71\n"
    "v_mfma_f32_16x16x32_bf16 a[80:83], a[144:147], v[176:179], a[80:83]\n"
    "v_cvt_pk_bf16_f32 v224, v208, v209\n"
    "v_cvt_pk_bf16_f32 v225, v210, v211\n"
    "v_mfma_f32_16x16x32_bf16 a[84:87], a[148:151], v[168:171], a[84:87]\n"
    "ds_read_b64_tr_b16 a[188:189], v243 offset:47360\n"
    "v_cvt_pk_bf16_f32 v226, v212, v213\n"
    "v_mfma_f32_16x16x32_bf16 a[88:91], a[148:151], v[172:175], a[88:91]\n"
    "ds_read_b64_tr_b16 a[190:191], v243 offset:52480\n"
    "v_cvt_pk_bf16_f32 v227, v214, v215\n"
    "v_mfma_f32_16x16x32_bf16 a[92:95], a[148:151], v[176:179], a[92:95]\n"
    "buffer_store_dwordx4 v[224:227], v248, s[44:47], s62 offen offset:2048 sc1\n"
    "s_waitcnt lgkmcnt(15)\n"
    "v_mfma_f32_16x16x32_bf16 a[72:75], a[152:155], v[180:183], a[72:75]\n"
    "ds_read_b64_tr_b16 a[192:193], v242 offset:57600\n"
    "v_mfma_f32_16x16x32_bf16 a[76:79], a[152:155], v[184:187], a[76:79]\n"
    "ds_read_b64_tr_b16 a[194:195], v242 offset:62720\n"
    "v_mfma_f32_16x16x32_bf16 a[80:83], a[152:155], v[188:191], a[80:83]\n"
    "v_mfma_f32_16x16x32_bf16 a[84:87], a[156:159], v[180:183], a[84:87]\n"
    "ds_read_b64_tr_b16 a[196:197], v243 offset:57600\n"
    "v_mfma_f32_16x16x32_bf16 a[88:91], a[156:159], v[184:187], a[88:91]\n"
    "ds_read_b64_tr_b16 a[198:199], v243 offset:62720\n"
    "v_mfma_f32_16x16x32_bf16 a[92:95], a[156:159], v[188:191], a[92:95]\n"
    "s_waitcnt lgkmcnt(15)\n"
    "v_mfma_f32_16x16x32_bf16 a[96:99], a[160:163], v[96:99], a[96:99]\n"
    "ds_read_b64_tr_b16 a[200:201], v244 offset:2304\n"
    "v_mfma_f32_16x16x32_bf16 a[100:103], a[160:163], v[100:103], a[100:103]\n"
    "ds_read_b64_tr_b16 a[202:203], v244 offset:7424\n"
    "v_mfma_f32_16x16x32_bf16 a[104:107], a[160:163], v[104:107], a[104:107]\n"
    "v_mfma_f32_16x16x32_bf16 a[108:111], a[164:167], v[96:99], a[108:111]\n"
    "ds_read_b64_tr_b16 a[204:205], v245 offset:2304\n"
    "v_mfma_f32_16x16x32_bf16 a[112:115], a[164:167], v[100:103], a[112:115]\n"
    "ds_read_b64_tr_b16 a[206:207], v245 offset:7424\n"
    "v_mfma_f32_16x16x32_bf16 a[116:119], a[164:167], v[104:107], a[116:119]\n"
    "s_waitcnt lgkmcnt(15)\n"
    "v_mfma_f32_16x16x32_bf16 a[96:99], a[168:171], v[108:111], a[96:99]\n"
    "ds_read_b64_tr_b16 a[128:129], v244 offset:12544\n"
    "v_accvgpr_read_b32 v192, a72\n"
    "v_mfma_f32_16x16x32_bf16 a[100:103], a[168:171], v[112:115], a[100:103]\n"
    "ds_read_b64_tr_b16 a[130:131], v244 offset:17664\n"
    "v_accvgpr_read_b32 v193, a73\n"
    "v_mfma_f32_16x16x32_bf16 a[104:107], a[168:171], v[116:119], a[104:107]\n"
    "v_accvgpr_read_b32 v194, a74\n"
    "v_accvgpr_read_b32 v195, a75\n"
    "v_mfma_f32_16x16x32_bf16 a[108:111], a[172:175], v[108:111], a[108:111]\n"
    "ds_read_b64_tr_b16 a[132:133], v245 offset:12544\n"
    "v_accvgpr_read_b32 v196, a84\n"
    "v_mfma_f32_16x16x32_bf16 a[112:115], a[172:175], v[112:115], a[112:115]\n"
    "ds_read_b64_tr_b16 a[134:135], v245 offset:17664\n"
    "v_accvgpr_read_b32 v197, a85\n"
    "v_mfma_f32_16x16x32_bf16 a[116:119], a[172:175], v[116:119], a[116:119]\n"
    "s_waitcnt lgkmcnt(15)\n"
    "v_mfma_f32_16x16x32_bf16 a[96:99], a[176:179], v[120:123], a[96:99]\n"
    "ds_read_b64_tr_b16 a[136:137], v244 offset:22784\n"
    "v_accvgpr_read_b32 v198, a86\n"
    "v_mfma_f32_16x16x32_bf16 a[100:103], a[176:179], v[124:127], a[100:103]\n"
    "ds_read_b64_tr_b16 a[138:139], v244 offset:27904\n"
    "v_accvgpr_read_b32 v199, a87\n"
    "v_mfma_f32_16x16x32_bf16 a[104:107], a[176:179], v[128:131], a[104:107]\n"
    "v_cvt_pk_bf16_f32 v216, v192, v193\n"
    "v_cvt_pk_bf16_f32 v217, v194, v195\n"
    "v_mfma_f32_16x16x32_bf16 a[108:111], a[180:183], v[120:123], a[108:111]\n"
    "ds_read_b64_tr_b16 a[140:141], v245 offset:22784\n"
    "v_cvt_pk_bf16_f32 v218, v196, v197\n"
    "v_mfma_f32_16x16x32_bf16 a[112:115], a[180:183], v[124:127], a[112:115]\n"
    "ds_read_b64_tr_b16 a[142:143], v245 offset:27904\n"
    "v_cvt_pk_bf16_f32 v219, v198, v199\n"
    "v_mfma_f32_16x16x32_bf16 a[116:119], a[180:183], v[128:131], a[116:119]\n"
    "s_lshr_b32 s62, s26, 1\n"
    "s_add_u32 s62, s62, 3\n"
    "s_and_b32 s62, s62, 3\n"
    "s_mul_i32 s62, s62, 0xc00\n"
    "buffer_store_dwordx4 v[216:219], v248, s[44:47], s62 offen sc1\n"
    "s_waitcnt lgkmcnt(15)\n"
    "v_mfma_f32_16x16x32_bf16 a[96:99], a[184:187], v[132:135], a[96:99]\n"
    "ds_read_b64_tr_b16 a[144:145], v242 offset:16384\n"
    "v_accvgpr_read_b32 v200, a76\n"
    "v_mfma_f32_16x16x32_bf16 a[100:103], a[184:187], v[136:139], a[100:103]\n"
    "ds_read_b64_tr_b16 a[146:147], v242 offset:21504\n"
    "v_accvgpr_read_b32 v201, a77\n"
    "v_mfma_f32_16x16x32_bf16 a[104:107], a[184:187], v[140:143], a[104:107]\n"
    "v_accvgpr_read_b32 v202, a78\n"
    "v_accvgpr_read_b32 v203, a79\n"
    "v_mfma_f32_16x16x32_bf16 a[108:111], a[188:191], v[132:135], a[108:111]\n"
    "ds_read_b64_tr_b16 a[148:149], v243 offset:16384\n"
    "v_accvgpr_read_b32 v204, a88\n"
    "v_mfma_f32_16x16x32_bf16 a[112:115], a[188:191], v[136:139], a[112:115]\n"
    "ds_read_b64_tr_b16 a[150:151], v243 offset:21504\n"
    "v_accvgpr_read_b32 v205, a89\n"
    "v_mfma_f32_16x16x32_bf16 a[116:119], a[188:191], v[140:143], a[116:119]\n"
    "s_waitcnt lgkmcnt(15)\n"
    "v_mfma_f32_16x16x32_bf16 a[96:99], a[192:195], v[144:147], a[96:99]\n"
    "ds_read_b64_tr_b16 a[152:153], v242 offset:26624\n"
    "v_accvgpr_read_b32 v206, a90\n"
    "v_mfma_f32_16x16x32_bf16 a[100:103], a[192:195], v[148:151], a[100:103]\n"
    "ds_read_b64_tr_b16 a[154:155], v242 offset:31744\n"
    "v_accvgpr_read_b32 v207, a91\n"
    "v_mfma_f32_16x16x32_bf16 a[104:107], a[192:195], v[152:155], a[104:107]\n"
    "v_cvt_pk_bf16_f32 v220, v200, v201\n"
    "v_cvt_pk_bf16_f32 v221, v202, v203\n"
    "v_mfma_f32_16x16x32_bf16 a[108:111], a[196:199], v[144:147], a[108:111]\n"
    "ds_read_b64_tr_b16 a[156:157], v243 offset:26624\n"
    "v_cvt_pk_bf16_f32 v222, v204, v205\n"
    "v_mfma_f32_16x16x32_bf16 a[112:115], a[196:199], v[148:151], a[112:115]\n"
    "ds_read_b64_tr_b16 a[158:159], v243 offset:31744\n"
    "v_cvt_pk_bf16_f32 v223, v206, v207\n"
    "v_mfma_f32_16x16x32_bf16 a[116:119], a[196:199], v[152:155], a[116:119]\n"
    "buffer_store_dwordx4 v[220:223], v248, s[44:47], s62 offen offset:1024 sc1\n"
    "s_waitcnt lgkmcnt(15)\n"
    "v_mfma_f32_16x16x32_bf16 a[96:99], a[200:203], v[156:159], a[96:99]\n"
    "ds_read_b64_tr_b16 a[160:161], v242 offset:36864\n"
    "v_accvgpr_read_b32 v208, a80\n"
    "v_mfma_f32_16x16x32_bf16 a[100:103], a[200:203], v[160:163], a[100:103]\n"
    "ds_read_b64_tr_b16 a[162:163], v242 offset:41984\n"
    "v_accvgpr_read_b32 v209, a81\n"
    "v_mfma_f32_16x16x32_bf16 a[104:107], a[200:203], v[164:167], a[104:107]\n"
    "v_accvgpr_read_b32 v210, a82\n"
    "v_accvgpr_read_b32 v211, a83\n"
    "v_mfma_f32_16x16x32_bf16 a[108:111], a[204:207], v[156:159], a[108:111]\n"
    "ds_read_b64_tr_b16 a[164:165], v243 offset:36864\n"
    "v_accvgpr_read_b32 v212, a92\n"
    "v_mfma_f32_16x16x32_bf16 a[112:115], a[204:207], v[160:163], a[112:115]\n"
    "ds_read_b64_tr_b16 a[166:167], v243 offset:41984\n"
    "v_accvgpr_read_b32 v213, a93\n"
    "v_mfma_f32_16x16x32_bf16 a[116:119], a[204:207], v[164:167], a[116:119]\n"
    "s_waitcnt lgkmcnt(15)\n"
    "v_mfma_f32_16x16x32_bf16 a[96:99], a[128:131], v[168:171], a[96:99]\n"
    "ds_read_b64_tr_b16 a[168:169], v242 offset:47104\n"
    "v_accvgpr_read_b32 v214, a94\n"
    "v_mfma_f32_16x16x32_bf16 a[100:103], a[128:131], v[172:175], a[100:103]\n"
    "ds_read_b64_tr_b16 a[170:171], v242 offset:52224\n"
    "v_accvgpr_read_b32 v215, a95\n"
    "v_mfma_f32_16x16x32_bf16 a[104:107], a[128:131], v[176:179], a[104:107]\n"
    "v_cvt_pk_bf16_f32 v224, v208, v209\n"
    "v_cvt_pk_bf16_f32 v225, v210, v211\n"
    "v_mfma_f32_16x16x32_bf16 a[108:111], a[132:135], v[168:171], a[108:111]\n"
    "ds_read_b64_tr_b16 a[172:173], v243 offset:47104\n"
    "v_cvt_pk_bf16_f32 v226, v212, v213\n"
    "v_mfma_f32_16x16x32_bf16 a[112:115], a[132:135], v[172:175], a[112:115]\n"
    "ds_read_b64_tr_b16 a[174:175], v243 offset:52224\n"
    "v_cvt_pk_bf16_f32 v227, v214, v215\n"
    "v_mfma_f32_16x16x32_bf16 a[116:119], a[132:135], v[176:179], a[116:119]\n"
    "buffer_store_dwordx4 v[224:227], v248, s[44:47], s62 offen offset:2048 sc1\n"
    "s_waitcnt lgkmcnt(15)\n"
    "v_mfma_f32_16x16x32_bf16 a[96:99], a[136:139], v[180:183], a[96:99]\n"
    "ds_read_b64_tr_b16 a[176:177], v242 offset:57344\n"
    "v_mfma_f32_16x16x32_bf16 a[100:103], a[136:139], v[184:187], a[100:103]\n"
    "ds_read_b64_tr_b16 a[178:179], v242 offset:62464\n"
    "v_mfma_f32_16x16x32_bf16 a[104:107], a[136:139], v[188:191], a[104:107]\n"
    "v_mfma_f32_16x16x32_bf16 a[108:111], a[140:143], v[180:183], a[108:111]\n"
    "ds_read_b64_tr_b16 a[180:181], v243 offset:57344\n"
    "v_mfma_f32_16x16x32_bf16 a[112:115], a[140:143], v[184:187], a[112:115]\n"
    "ds_read_b64_tr_b16 a[182:183], v243 offset:62464\n"
    "v_mfma_f32_16x16x32_bf16 a[116:119], a[140:143], v[188:191], a[116:119]\n"
    "s_waitcnt lgkmcnt(15)\n"
    "v_mfma_f32_16x16x32_bf16 a[0:3], a[144:147], v[96:99], a[0:3]\n"
    "ds_read_b64_tr_b16 a[184:185], v244 offset:2048\n"
    "v_mfma_f32_16x16x32_bf16 a[4:7], a[144:147], v[100:103], a[4:7]\n"
    "ds_read_b64_tr_b16 a[186:187], v244 offset:7168\n"
    "v_mfma_f32_16x16x32_bf16 a[8:11], a[144:147], v[104:107], a[8:11]\n"
    "v_mfma_f32_16x16x32_bf16 a[12:15], a[148:151], v[96:99], a[12:15]\n"
    "ds_read_b64_tr_b16 a[188:189], v245 offset:2048\n"
    "v_mfma_f32_16x16x32_bf16 a[16:19], a[148:151], v[100:103], a[16:19]\n"
    "ds_read_b64_tr_b16 a[190:191], v245 offset:7168\n"
    "v_mfma_f32_16x16x32_bf16 a[20:23], a[148:151], v[104:107], a[20:23]\n"
    "s_waitcnt lgkmcnt(15)\n"
    "v_mfma_f32_16x16x32_bf16 a[0:3], a[152:155], v[108:111], a[0:3]\n"
    "ds_read_b64_tr_b16 a[192:193], v244 offset:12288\n"
    "v_accvgpr_read_b32 v192, a96\n"
    "v_mfma_f32_16x16x32_bf16 a[4:7], a[152:155], v[112:115], a[4:7]\n"
    "ds_read_b64_tr_b16 a[194:195], v244 offset:17408\n"
    "v_accvgpr_read_b32 v193, a97\n"
    "v_mfma_f32_16x16x32_bf16 a[8:11], a[152:155], v[116:119], a[8:11]\n"
    "v_accvgpr_read_b32 v194, a98\n"
    "v_accvgpr_read_b32 v195, a99\n"
    "v_mfma_f32_16x16x32_bf16 a[12:15], a[156:159], v[108:111], a[12:15]\n"
    "ds_read_b64_tr_b16 a[196:197], v245 offset:12288\n"
    "v_accvgpr_read_b32 v196, a108\n"
    "v_mfma_f32_16x16x32_bf16 a[16:19], a[156:159], v[112:115], a[16:19]\n"
    "ds_read_b64_tr_b16 a[198:199], v245 offset:17408\n"
    "v_accvgpr_read_b32 v197, a109\n"
    "v_mfma_f32_16x16x32_bf16 a[20:23], a[156:159], v[116:119], a[20:23]\n"
    "s_waitcnt lgkmcnt(15)\n"
    "v_mfma_f32_16x16x32_bf16 a[0:3], a[160:163], v[120:123], a[0:3]\n"
    "ds_read_b64_tr_b16 a[200:201], v244 offset:22528\n"
    "v_accvgpr_read_b32 v198, a110\n"
    "v_mfma_f32_16x16x32_bf16 a[4:7], a[160:163], v[124:127], a[4:7]\n"
    "ds_read_b64_tr_b16 a[202:203], v244 offset:27648\n"
    "v_accvgpr_read_b32 v199, a111\n"
    "v_mfma_f32_16x16x32_bf16 a[8:11], a[160:163], v[128:131], a[8:11]\n"
    "v_cvt_pk_bf16_f32 v216, v192, v193\n"
    "v_cvt_pk_bf16_f32 v217, v194, v195\n"
    "v_mfma_f32_16x16x32_bf16 a[12:15], a[164:167], v[120:123], a[12:15]\n"
    "ds_read_b64_tr_b16 a[204:205], v245 offset:22528\n"
    "v_cvt_pk_bf16_f32 v218, v196, v197\n"
    "v_mfma_f32_16x16x32_bf16 a[16:19], a[164:167], v[124:127], a[16:19]\n"
    "ds_read_b64_tr_b16 a[206:207], v245 offset:27648\n"
    "v_cvt_pk_bf16_f32 v219, v198, v199\n"
    "v_mfma_f32_16x16x32_bf16 a[20:23], a[164:167], v[128:131], a[20:23]\n"
    "s_mov_b32 s62, 0x3000\n"
    "buffer_store_dwordx4 v[216:219], v248, s[44:47], s62 offen sc1\n"
    "s_waitcnt lgkmcnt(15)\n"
    "v_mfma_f32_16x16x32_bf16 a[0:3], a[168:171], v[132:135], a[0:3]\n"
    "v_accvgpr_read_b32 v200, a100\n"
    "v_mfma_f32_16x16x32_bf16 a[4:7], a[168:171], v[136:139], a[4:7]\n"
    "v_accvgpr_read_b32 v201, a101\n"
    "v_mfma_f32_16x16x32_bf16 a[8:11], a[168:171], v[140:143], a[8:11]\n"
    "v_accvgpr_read_b32 v202, a102\n"
    "v_accvgpr_read_b32 v203, a103\n"
    "v_mfma_f32_16x16x32_bf16 a[12:15], a[172:175], v[132:135], a[12:15]\n"
    "v_accvgpr_read_b32 v204, a112\n"
    "v_mfma_f32_16x16x32_bf16 a[16:19], a[172:175], v[136:139], a[16:19]\n"
    "v_accvgpr_read_b32 v205, a113\n"
    "v_mfma_f32_16x16x32_bf16 a[20:23], a[172:175], v[140:143], a[20:23]\n"
    "s_waitcnt lgkmcnt(12)\n"
    "v_mfma_f32_16x16x32_bf16 a[0:3], a[176:179], v[144:147], a[0:3]\n"
    "v_accvgpr_read_b32 v206, a114\n"
    "v_mfma_f32_16x16x32_bf16 a[4:7], a[176:179], v[148:151], a[4:7]\n"
    "v_accvgpr_read_b32 v207, a115\n"
    "v_mfma_f32_16x16x32_bf16 a[8:11], a[176:179], v[152:155], a[8:11]\n"
    "v_cvt_pk_bf16_f32 v220, v200, v201\n"
    "v_cvt_pk_bf16_f32 v221, v202, v203\n"
    "v_mfma_f32_16x16x32_bf16 a[12:15], a[180:183], v[144:147], a[12:15]\n"
    "v_cvt_pk_bf16_f32 v222, v204, v205\n"
    "v_mfma_f32_16x16x32_bf16 a[16:19], a[180:183], v[148:151], a[16:19]\n"
    "v_cvt_pk_bf16_f32 v223, v206, v207\n"
    "v_mfma_f32_16x16x32_bf16 a[20:23], a[180:183], v[152:155], a[20:23]\n"
    "buffer_store_dwordx4 v[220:223], v248, s[44:47], s62 offen offset:1024 sc1\n"
    "s_waitcnt lgkmcnt(8)\n"
    "v_mfma_f32_16x16x32_bf16 a[0:3], a[184:187], v[156:159], a[0:3]\n"
    "v_accvgpr_read_b32 v208, a104\n"
    "v_mfma_f32_16x16x32_bf16 a[4:7], a[184:187], v[160:163], a[4:7]\n"
    "v_accvgpr_read_b32 v209, a105\n"
    "v_mfma_f32_16x16x32_bf16 a[8:11], a[184:187], v[164:167], a[8:11]\n"
    "v_accvgpr_read_b32 v210, a106\n"
    "v_accvgpr_read_b32 v211, a107\n"
    "v_mfma_f32_16x16x32_bf16 a[12:15], a[188:191], v[156:159], a[12:15]\n"
    "v_accvgpr_read_b32 v212, a116\n"
    "v_mfma_f32_16x16x32_bf16 a[16:19], a[188:191], v[160:163], a[16:19]\n"
    "v_accvgpr_read_b32 v213, a117\n"
    "v_mfma_f32_16x16x32_bf16 a[20:23], a[188:191], v[164:167], a[20:23]\n"
    "s_waitcnt lgkmcnt(4)\n"
    "v_mfma_f32_16x16x32_bf16 a[0:3], a[192:195], v[168:171], a[0:3]\n"
    "v_accvgpr_read_b32 v214, a118\n"
    "v_mfma_f32_16x16x32_bf16 a[4:7], a[192:195], v[172:175], a[4:7]\n"
    "v_accvgpr_read_b32 v215, a119\n"
    "v_mfma_f32_16x16x32_bf16 a[8:11], a[192:195], v[176:179], a[8:11]\n"
    "v_cvt_pk_bf16_f32 v224, v208, v209\n"
    "v_cvt_pk_bf16_f32 v225, v210, v211\n"
    "v_mfma_f32_16x16x32_bf16 a[12:15], a[196:199], v[168:171], a[12:15]\n"
    "v_cvt_pk_bf16_f32 v226, v212, v213\n"
    "v_mfma_f32_16x16x32_bf16 a[16:19], a[196:199], v[172:175], a[16:19]\n"
    "v_cvt_pk_bf16_f32 v227, v214, v215\n"
    "v_mfma_f32_16x16x32_bf16 a[20:23], a[196:199], v[176:179], a[20:23]\n"
    "buffer_store_dwordx4 v[224:227], v248, s[44:47], s62 offen offset:2048 sc1\n"
    "s_waitcnt lgkmcnt(0)\n"
    "v_mfma_f32_16x16x32_bf16 a[0:3], a[200:203], v[180:183], a[0:3]\n"
    "v_mfma_f32_16x16x32_bf16 a[4:7], a[200:203], v[184:187], a[4:7]\n"
    "v_mfma_f32_16x16x32_bf16 a[8:11], a[200:203], v[188:191], a[8:11]\n"
    "v_mfma_f32_16x16x32_bf16 a[12:15], a[204:207], v[180:183], a[12:15]\n"
    "v_mfma_f32_16x16x32_bf16 a[16:19], a[204:207], v[184:187], a[16:19]\n"
    "v_mfma_f32_16x16x32_bf16 a[20:23], a[204:207], v[188:191], a[20:23]\n"
    "s_cmp_lg_u32 s81, 0\n"
    "s_cbranch_scc1 LF%=\n"
    "s_lshl_b32 s62, s26, 1\n"
    "s_add_u32 s62, s62, s77\n"
    "s_mul_i32 s63, s62, 0x3c00\n"
    "s_sub_u32 s44, s44, s63\n"
    "s_subb_u32 s45, s45, 0\n"
    "s_mov_b32 s46, 0x3c000\n"
    "s_mov_b32 s40, s8\n"
    "s_mov_b32 s41, s9\n"
    "s_mov_b32 s42, 0x78000\n"
    "s_lshr_b32 s62, s26, 1\n"
    "s_mul_i32 s63, s77, 0x3c00\n"
    "s_mul_i32 s72, s62, 0xc00\n"
    "s_add_u32 s63, s63, s72\n"
    "s_add_u32 s72, s62, 1\n"
    "s_and_b32 s72, s72, 3\n"
    "s_mul_i32 s72, s72, 0xf000\n"
    "s_add_u32 s64, s72, s63\n"
    "s_add_u32 s72, s62, 2\n"
    "s_and_b32 s72, s72, 3\n"
    "s_mul_i32 s72, s72, 0xf000\n"
    "s_add_u32 s65, s72, s63\n"
    "s_add_u32 s72, s62, 3\n"
    "s_and_b32 s72, s72, 3\n"
    "s_mul_i32 s72, s72, 0xf000\n"
    "s_add_u32 s66, s72, s63\n"
    "s_mul_i32 s72, s62, 0x3c00\n"
    "s_lshl_b32 s73, s77, 10\n"
    "s_add_u32 s67, s72, s73\n"
    "s_add_u32 s67, s67, 0x3000\n"
    "v_bfe_u32 v40, v248, 4, 4\n"
    "v_mul_u32_u24 v40, 0xa00, v40\n"
    "v_bfe_u32 v41, v248, 8, 1\n"
    "v_lshl_add_u32 v40, v41, 5, v40\n"
    "v_bfe_u32 v41, v248, 9, 1\n"
    "v_lshl_add_u32 v40, v41, 4, v40\n"
    "s_mul_i32 s68, s77, 0x1e000\n"
    "s_lshl_b32 s72, s62, 6\n"
    "s_add_u32 s68, s68, s72\n"
    "s_mul_i32 s69, s62, 0x1e000\n"
    "s_mul_i32 s72, s77, 0xa000\n"
    "s_add_u32 s69, s69, s72\n"
    "s_add_u32 s69, s69, 0x100\n"
    "v_mov_b32 v42, s10\n"
    "v_mov_b32 v43, 1\n"
    "v_accvgpr_read_b32 v48, a0\n"
    "v_accvgpr_read_b32 v49, a1\n"
    "v_accvgpr_read_b32 v50, a2\n"
    "v_accvgpr_read_b32 v51, a3\n"
    "v_accvgpr_read_b32 v52, a4\n"
    "v_accvgpr_read_b32 v53, a5\n"
    "v_accvgpr_read_b32 v54, a6\n"
    "v_accvgpr_read_b32 v55, a7\n"
    "v_accvgpr_read_b32 v56, a8\n"
    "v_accvgpr_read_b32 v57, a9\n"
    "v_accvgpr_read_b32 v58, a10\n"
    "v_accvgpr_read_b32 v59, a11\n"
    "v_accvgpr_read_b32 v60, a12\n"
    "v_accvgpr_read_b32 v61, a13\n"
    "v_accvgpr_read_b32 v62, a14\n"
    "v_accvgpr_read_b32 v63, a15\n"
    "v_accvgpr_read_b32 v64, a16\n"
    "v_accvgpr_read_b32 v65, a17\n"
    "v_accvgpr_read_b32 v66, a18\n"
    "v_accvgpr_read_b32 v67, a19\n"
    "v_accvgpr_read_b32 v68, a20\n"
    "v_accvgpr_read_b32 v69, a21\n"
    "v_accvgpr_read_b32 v70, a22\n"
    "v_accvgpr_read_b32 v71, a23\n"
    "s_waitcnt vmcnt(0)\n"
    "s_barrier\n"
    "s_cmp_lg_u32 s77, 0\n"
    "s_cbranch_scc1 LD%=\n"
    "s_mov_b64 exec, 1\n"
    "global_atomic_add v42, v43, s[78:79]\n"
    "s_mov_b64 exec, -1\n"
    "LD%=:\n"
    "s_memrealtime s[72:73]\n"
    "global_load_dword v44, v42, s[78:79] sc1\n"
    "s_nop 15\n"
    "s_nop 15\n"
    "global_load_dword v45, v42, s[78:79] sc1\n"
    "LE%=:\n"
    "s_waitcnt vmcnt(1)\n"
    "v_readfirstlane_b32 s62, v44\n"
    "global_load_dword v44, v42, s[78:79] sc1\n"
    "s_nop 0\n"
    "s_sub_u32 s62, s62, s25\n"
    "s_cmp_eq_u32 s62, 4\n"
    "s_cbranch_scc1 LO%=\n"
    "s_waitcnt vmcnt(1)\n"
    "v_readfirstlane_b32 s62, v45\n"
    "global_load_dword v45, v42, s[78:79] sc1\n"
    "s_nop 0\n"
    "s_sub_u32 s62, s62, s25\n"
    "s_cmp_eq_u32 s62, 4\n"
    "s_cbranch_scc1 LO%=\n"
    "s_memrealtime s[74:75]\n"
    "s_waitcnt lgkmcnt(0)\n"
    "s_sub_u32 s74, s74, s72\n"
    "s_cmp_lt_u32 s74, 0x400000\n"
    "s_cbranch_scc1 LE%=\n"
    "s_mov_b32 s81, 2\n"
    "s_waitcnt vmcnt(0)\n"
    "s_branch LF%=\n"
    "LO%=:\n"
    "buffer_load_dwordx4 v[80:83], v248, s[44:47], s64 offen offset:0 sc1\n"
    "buffer_load_dwordx4 v[84:87], v248, s[44:47], s64 offen offset:1024 sc1\n"
    "buffer_load_dwordx4 v[88:91], v248, s[44:47], s64 offen offset:2048 sc1\n"
    "buffer_load_dwordx4 v[92:95], v248, s[44:47], s65 offen offset:0 sc1\n"
    "buffer_load_dwordx4 v[96:99], v248, s[44:47], s65 offen offset:1024 sc1\n"
    "buffer_load_dwordx4 v[100:103], v248, s[44:47], s65 offen offset:2048 sc1\n"
    "buffer_load_dwordx4 v[104:107], v248, s[44:47], s66 offen offset:0 sc1\n"
    "buffer_load_dwordx4 v[108:111], v248, s[44:47], s66 offen offset:1024 sc1\n"
    "buffer_load_dwordx4 v[112:115], v248, s[44:47], s66 offen offset:2048 sc1\n"
    "s_cmp_eq_u32 s77, 3\n"
    "s_cselect_b32 s46, 0, s46\n"
    "buffer_load_dwordx4 v[116:119], v248, s[44:47], s67 offen sc1\n"
    "s_add_u32 s67, s67, 0xf000\n"
    "buffer_load_dwordx4 v[120:123], v248, s[44:47], s67 offen sc1\n"
    "s_add_u32 s67, s67, 0xf000\n"
    "buffer_load_dwordx4 v[124:127], v248, s[44:47], s67 offen sc1\n"
    "s_add_u32 s67, s67, 0xf000\n"
    "buffer_load_dwordx4 v[128:131], v248, s[44:47], s67 offen sc1\n"
    "s_add_u32 s67, s67, 0xf000\n"
    "s_waitcnt vmcnt(6)\n"
    "v_lshlrev_b32 v160, 16, v80\n"
    "v_and_b32 v161, 0xffff0000, v80\n"
    "v_lshlrev_b32 v162, 16, v81\n"
    "v_and_b32 v163, 0xffff0000, v81\n"
    "v_lshlrev_b32 v164, 16, v82\n"
    "v_and_b32 v165, 0xffff0000, v82\n"
    "v_lshlrev_b32 v166, 16, v83\n"
    "v_and_b32 v167, 0xffff0000, v83\n"
    "v_lshlrev_b32 v168, 16, v92\n"
    "v_and_b32 v169, 0xffff0000, v92\n"
    "v_lshlrev_b32 v170, 16, v93\n"
    "v_and_b32 v171, 0xffff0000, v93\n"
    "v_lshlrev_b32 v172, 16, v94\n"
    "v_and_b32 v173, 0xffff0000, v94\n"
    "v_lshlrev_b32 v174, 16, v95\n"
    "v_and_b32 v175, 0xffff0000, v95\n"
    "v_lshlrev_b32 v176, 16, v104\n"
    "v_and_b32 v177, 0xffff0000, v104\n"
    "v_lshlrev_b32 v178, 16, v105\n"
    "v_and_b32 v179, 0xffff0000, v105\n"
    "v_lshlrev_b32 v180, 16, v106\n"
    "v_and_b32 v181, 0xffff0000, v106\n"
    "v_lshlrev_b32 v182, 16, v107\n"
    "v_and_b32 v183, 0xffff0000, v107\n"
    "v_add_f32 v136, v48, v160\n"
    "v_add_f32 v168, v168, v176\n"
    "v_add_f32 v137, v49, v161\n"
    "v_add_f32 v169, v169, v177\n"
    "v_add_f32 v138, v50, v162\n"
    "v_add_f32 v170, v170, v178\n"
    "v_add_f32 v139, v51, v163\n"
    "v_add_f32 v171, v171, v179\n"
    "v_add_f32 v140, v60, v164\n"
    "v_add_f32 v172, v172, v180\n"
    "v_add_f32 v141, v61, v165\n"
    "v_add_f32 v173, v173, v181\n"
    "v_add_f32 v142, v62, v166\n"
    "v_add_f32 v174, v174, v182\n"
    "v_add_f32 v143, v63, v167\n"
    "v_add_f32 v175, v175, v183\n"
    "v_add_f32 v136, v136, v168\n"
    "v_add_f32 v137, v137, v169\n"
    "v_add_f32 v138, v138, v170\n"
    "v_add_f32 v139, v139, v171\n"
    "v_add_f32 v140, v140, v172\n"
    "v_add_f32 v141, v141, v173\n"
    "v_add_f32 v142, v142, v174\n"
    "v_add_f32 v143, v143, v175\n"
    "v_cvt_pk_bf16_f32 v136, v136, v137\n"
    "v_cvt_pk_bf16_f32 v137, v138, v139\n"
    "v_cvt_pk_bf16_f32 v138, v140, v141\n"
    "v_cvt_pk_bf16_f32 v139, v142, v143\n"
    "s_nop 1\n"
    "v_permlane16_swap_b32 v136, v138\n"
    "v_permlane16_swap_b32 v137, v139\n"
    "buffer_store_dwordx4 v[136:139], v40, s[40:43], s68 offen sc1\n"
    "s_add_u32 s68, s68, 0xa000\n"
    "s_waitcnt vmcnt(6)\n"
    "v_lshlrev_b32 v160, 16, v84\n"
    "v_and_b32 v161, 0xffff0000, v84\n"
    "v_lshlrev_b32 v162, 16, v85\n"
    "v_and_b32 v163, 0xffff0000, v85\n"
    "v_lshlrev_b32 v164, 16, v86\n"
    "v_and_b32 v165, 0xffff0000, v86\n"
    "v_lshlrev_b32 v166, 16, v87\n"
    "v_and_b32 v167, 0xffff0000, v87\n"
    "v_lshlrev_b32 v168, 16, v96\n"
    "v_and_b32 v169, 0xffff0000, v96\n"
    "v_lshlrev_b32 v170, 16, v97\n"
    "v_and_b32 v171, 0xffff0000, v97\n"
    "v_lshlrev_b32 v172, 16, v98\n"
    "v_and_b32 v173, 0xffff0000, v98\n"
    "v_lshlrev_b32 v174, 16, v99\n"
    "v_and_b32 v175, 0xffff0000, v99\n"
    "v_lshlrev_b32 v176, 16, v108\n"
    "v_and_b32 v177, 0xffff0000, v108\n"
    "v_lshlrev_b32 v178, 16, v109\n"
    "v_and_b32 v179, 0xffff0000, v109\n"
    "v_lshlrev_b32 v180, 16, v110\n"
    "v_and_b32 v181, 0xffff0000, v110\n"
    "v_lshlrev_b32 v182, 16, v111\n"
    "v_and_b32 v183, 0xffff0000, v111\n"
    "v_add_f32 v144, v52, v160\n"
    "v_add_f32 v168, v168, v176\n"
    "v_add_f32 v145, v53, v161\n"
    "v_add_f32 v169, v169, v177\n"
    "v_add_f32 v146, v54, v162\n"
    "v_add_f32 v170, v170, v178\n"
    "v_add_f32 v147, v55, v163\n"
    "v_add_f32 v171, v171, v179\n"
    "v_add_f32 v148, v64, v164\n"
    "v_add_f32 v172, v172, v180\n"
    "v_add_f32 v149, v65, v165\n"
    "v_add_f32 v173, v173, v181\n"
    "v_add_f32 v150, v66, v166\n"
    "v_add_f32 v174, v174, v182\n"
    "v_add_f32 v151, v67, v167\n"
    "v_add_f32 v175, v175, v183\n"
    "v_add_f32 v144, v144, v168\n"
    "v_add_f32 v145, v145, v169\n"
    "v_add_f32 v146, v146, v170\n"
    "v_add_f32 v147, v147, v171\n"
    "v_add_f32 v148, v148, v172\n"
    "v_add_f32 v149, v149, v173\n"
    "v_add_f32 v150, v150, v174\n"
    "v_add_f32 v151, v151, v175\n"
    "v_cvt_pk_bf16_f32 v144, v144, v145\n"
    "v_cvt_pk_bf16_f32 v145, v146, v147\n"
    "v_cvt_pk_bf16_f32 v146, v148, v149\n"
    "v_cvt_pk_bf16_f32 v147, v150, v151\n"
    "s_nop 1\n"
    "v_permlane16_swap_b32 v144, v146\n"
    "v_permlane16_swap_b32 v145, v147\n"
    "buffer_store_dwordx4 v[144:147], v40, s[40:43], s68 offen sc1\n"
    "s_add_u32 s68, s68, 0xa000\n"
    "s_waitcnt vmcnt(6)\n"
    "v_lshlrev_b32 v160, 16, v88\n"
    "v_and_b32 v161, 0xffff0000, v88\n"
    "v_lshlrev_b32 v162, 16, v89\n"
    "v_and_b32 v163, 0xffff0000, v89\n"
    "v_lshlrev_b32 v164, 16, v90\n"
    "v_and_b32 v165, 0xffff0000, v90\n"
    "v_lshlrev_b32 v166, 16, v91\n"
    "v_and_b32 v167, 0xffff0000, v91\n"
    "v_lshlrev_b32 v168, 16, v100\n"
    "v_and_b32 v169, 0xffff0000, v100\n"
    "v_lshlrev_b32 v170, 16, v101\n"
    "v_and_b32 v171, 0xffff0000, v101\n"
    "v_lshlrev_b32 v172, 16, v102\n"
    "v_and_b32 v173, 0xffff0000, v102\n"
    "v_lshlrev_b32 v174, 16, v103\n"
    "v_and_b32 v175, 0xffff0000, v103\n"
    "v_lshlrev_b32 v176, 16, v112\n"
    "v_and_b32 v177, 0xffff0000, v112\n"
    "v_lshlrev_b32 v178, 16, v113\n"
    "v_and_b32 v179, 0xffff0000, v113\n"
    "v_lshlrev_b32 v180, 16, v114\n"
    "v_and_b32 v181, 0xffff0000, v114\n"
    "v_lshlrev_b32 v182, 16, v115\n"
    "v_and_b32 v183, 0xffff0000, v115\n"
    "v_add_f32 v152, v56, v160\n"
    "v_add_f32 v168, v168, v176\n"
    "v_add_f32 v153, v57, v161\n"
    "v_add_f32 v169, v169, v177\n"
    "v_add_f32 v154, v58, v162\n"
    "v_add_f32 v170, v170, v178\n"
    "v_add_f32 v155, v59, v163\n"
    "v_add_f32 v171, v171, v179\n"
    "v_add_f32 v156, v68, v164\n"
    "v_add_f32 v172, v172, v180\n"
    "v_add_f32 v157, v69, v165\n"
    "v_add_f32 v173, v173, v181\n"
    "v_add_f32 v158, v70, v166\n"
    "v_add_f32 v174, v174, v182\n"
    "v_add_f32 v159, v71, v167\n"
    "v_add_f32 v175, v175, v183\n"
    "v_add_f32 v152, v152, v168\n"
    "v_add_f32 v153, v153, v169\n"
    "v_add_f32 v154, v154, v170\n"
    "v_add_f32 v155, v155, v171\n"
    "v_add_f32 v156, v156, v172\n"
    "v_add_f32 v157, v157, v173\n"
    "v_add_f32 v158, v158, v174\n"
    "v_add_f32 v159, v159, v175\n"
    "v_cvt_pk_bf16_f32 v152, v152, v153\n"
    "v_cvt_pk_bf16_f32 v153, v154, v155\n"
    "v_cvt_pk_bf16_f32 v154, v156, v157\n"
    "v_cvt_pk_bf16_f32 v155, v158, v159\n"
    "s_nop 1\n"
    "v_permlane16_swap_b32 v152, v154\n"
    "v_permlane16_swap_b32 v153, v155\n"
    "buffer_store_dwordx4 v[152:155], v40, s[40:43], s68 offen sc1\n"
    "s_add_u32 s68, s68, 0xa000\n"
    "s_waitcnt vmcnt(3)\n"
    "s_cmp_eq_u32 s77, 3\n"
    "s_cselect_b32 s42, 0, s42\n"
    "v_lshlrev_b32 v160, 16, v116\n"
    "v_and_b32 v161, 0xffff0000, v116\n"
    "v_lshlrev_b32 v162, 16, v117\n"
    "v_and_b32 v163, 0xffff0000, v117\n"
    "v_lshlrev_b32 v164, 16, v118\n"
    "v_and_b32 v165, 0xffff0000, v118\n"
    "v_lshlrev_b32 v166, 16, v119\n"
    "v_and_b32 v167, 0xffff0000, v119\n"
    "v_lshlrev_b32 v168, 16, v120\n"
    "v_and_b32 v169, 0xffff0000, v120\n"
    "v_lshlrev_b32 v170, 16, v121\n"
    "v_and_b32 v171, 0xffff0000, v121\n"
    "v_lshlrev_b32 v172, 16, v122\n"
    "v_and_b32 v173, 0xffff0000, v122\n"
    "v_lshlrev_b32 v174, 16, v123\n"
    "v_and_b32 v175, 0xffff0000, v123\n"
    "v_lshlrev_b32 v176, 16, v124\n"
    "v_and_b32 v177, 0xffff0000, v124\n"
    "v_lshlrev_b32 v178, 16, v125\n"
    "v_and_b32 v179, 0xffff0000, v125\n"
    "v_lshlrev_b32 v180, 16, v126\n"
    "v_and_b32 v181, 0xffff0000, v126\n"
    "v_lshlrev_b32 v182, 16, v127\n"
    "v_and_b32 v183, 0xffff0000, v127\n"
    "v_lshlrev_b32 v184, 16, v128\n"
    "v_and_b32 v185, 0xffff0000, v128\n"
    "v_lshlrev_b32 v186, 16, v129\n"
    "v_and_b32 v187, 0xffff0000, v129\n"
    "v_lshlrev_b32 v188, 16, v130\n"
    "v_and_b32 v189, 0xffff0000, v130\n"
    "v_lshlrev_b32 v190, 16, v131\n"
    "v_and_b32 v191, 0xffff0000, v131\n"
    "v_add_f32 v160, v160, v168\n"
    "v_add_f32 v176, v176, v184\n"
    "v_add_f32 v161, v161, v169\n"
    "v_add_f32 v177, v177, v185\n"
    "v_add_f32 v162, v162, v170\n"
    "v_add_f32 v178, v178, v186\n"
    "v_add_f32 v163, v163, v171\n"
    "v_add_f32 v179, v179, v187\n"
    "v_add_f32 v164, v164, v172\n"
    "v_add_f32 v180, v180, v188\n"
    "v_add_f32 v165, v165, v173\n"
    "v_add_f32 v181, v181, v189\n"
    "v_add_f32 v166, v166, v174\n"
    "v_add_f32 v182, v182, v190\n"
    "v_add_f32 v167, v167, v175\n"
    "v_add_f32 v183, v183, v191\n"
    "v_add_f32 v160, v160, v176\n"
    "v_add_f32 v161, v161, v177\n"
    "v_add_f32 v162, v162, v178\n"
    "v_add_f32 v163, v163, v179\n"
    "v_add_f32 v164, v164, v180\n"
    "v_add_f32 v165, v165, v181\n"
    "v_add_f32 v166, v166, v182\n"
    "v_add_f32 v167, v167, v183\n"
    "v_cvt_pk_bf16_f32 v160, v160, v161\n"
    "v_cvt_pk_bf16_f32 v161, v162, v163\n"
    "v_cvt_pk_bf16_f32 v162, v164, v165\n"
    "v_cvt_pk_bf16_f32 v163, v166, v167\n"
    "s_nop 1\n"
    "v_permlane16_swap_b32 v160, v162\n"
    "v_permlane16_swap_b32 v161, v163\n"
    "buffer_store_dwordx4 v[160:163], v40, s[40:43], s69 offen sc1\n"
    "s_nop 1\n"
    "LF%=:\n"
    "s_nop 7\n"
    : "={s81}"(stat)
    : "{v228}"(w0), "{v229}"(w1), "{v230}"(w2), "{v231}"(w3), "{v232}"(w4), "{v234}"(rd0), "{v235}"(rd1), "{v236}"(ocb), "{v248}"(l16), "{v250}"(opb),
      "{s[36:39]}"(sWO), "{s[56:59]}"(sO), "{s[78:79]}"(sCnt), "{s80}"(om0), "{s26}"(rot2), "{s28}"(dO), "{s29}"(dP), "{s34}"(iiv), "{s85}"(samp), "{s77}"(wid0), "{s[8:9]}"(sOut), "{s10}"(dC), "{s25}"(samp2)
    : "v0", "v1", "v2", "v3", "v4", "v5", "v6", "v7", "v8", "v9", "v10", "v11", "v12", "v13", "v14", "v15", "v16", "v17", "v18", "v19", "v20", "v21", "v22", "v23", "v24", "v25", "v26", "v27", "v28", "v29", "v30", "v31", "v32", "v33", "v34", "v35", "v36", "v37", "v38", "v39", "v40", "v41", "v42", "v43", "v44", "v45", "v46", "v47", "v48", "v49", "v50", "v51", "v52", "v53", "v54", "v55", "v56", "v57", "v58", "v59", "v60", "v61", "v62", "v63", "v64", "v65", "v66", "v67", "v68", "v69", "v70", "v71", "v72", "v73", "v74", "v75", "v76", "v77", "v78", "v79", "v80", "v81", "v82", "v83", "v84", "v85", "v86", "v87", "v88", "v89", "v90", "v91", "v92", "v93", "v94", "v95", "v96", "v97", "v98", "v99", "v100", "v101", "v102", "v103", "v104", "v105", "v106", "v107", "v108", "v109", "v110", "v111", "v112", "v113", "v114", "v115", "v116", "v117", "v118", "v119", "v120", "v121", "v122", "v123", "v124", "v125", "v126", "v127", "v128", "v129", "v130", "v131", "v132", "v133", "v134", "v135", "v136", "v137", "v138", "v139", "v140", "v141", "v142", "v143", "v144", "v145", "v146", "v147", "v148", "v149", "v150", "v151", "v152", "v153", "v154", "v155", "v156", "v157", "v158", "v159", "v160", "v161", "v162", "v163", "v164", "v165", "v166", "v167", "v168", "v169", "v170", "v171", "v172", "v173", "v174", "v175", "v176", "v177", "v178", "v179", "v180", "v181", "v182", "v183", "v184", "v185", "v186", "v187", "v188", "v189", "v190", "v191", "v192", "v193", "v194", "v195", "v196", "v197", "v198", "v199", "v200", "v201", "v202", "v203", "v204", "v205", "v206", "v207", "v208", "v209", "v210", "v211", "v212", "v213", "v214", "v215", "v216", "v217", "v218", "v219", "v220", "v221", "v222", "v223", "v224", "v225", "v226", "v227", "v242", "v243", "v244", "v245", "v252", "v253", "a0", "a1", "a2", "a3", "a4", "a5", "a6", "a7", "a8", "a9", "a10", "a11", "a12", "a13", "a14", "a15", "a16", "a17", "a18", "a19", "a20", "a21", "a22", "a23", "a24", "a25", "a26", "a27", "a28", "a29", "a30", "a31", "a32", "a33", "a34", "a35", "a36", "a37", "a38", "a39", "a40", "a41", "a42", "a43", "a44", "a45", "a46", "a47", "a48", "a49", "a50", "a51", "a52", "a53", "a54", "a55", "a56", "a57", "a58", "a59", "a60", "a61", "a62", "a63", "a64", "a65", "a66", "a67", "a68", "a69", "a70", "a71", "a72", "a73", "a74", "a75", "a76", "a77", "a78", "a79", "a80", "a81", "a82", "a83", "a84", "a85", "a86", "a87", "a88", "a89", "a90", "a91", "a92", "a93", "a94", "a95", "a96", "a97", "a98", "a99", "a100", "a101", "a102", "a103", "a104", "a105", "a106", "a107", "a108", "a109", "a110", "a111", "a112", "a113", "a114", "a115", "a116", "a117", "a118", "a119", "a120", "a121", "a122", "a123", "a124", "a125", "a126", "a127", "a128", "a129", "a130", "a131", "a132", "a133", "a134", "a135", "a136", "a137", "a138", "a139", "a140", "a141", "a142", "a143", "a144", "a145", "a146", "a147", "a148", "a149", "a150", "a151", "a152", "a153", "a154", "a155", "a156", "a157", "a158", "a159", "a160", "a161", "a162", "a163", "a164", "a165", "a166", "a167", "a168", "a169", "a170", "a171", "a172", "a173", "a174", "a175", "a176", "a177", "a178", "a179", "a180", "a181", "a182", "a183", "a184", "a185", "a186", "a187", "a188", "a189", "a190", "a191", "a192", "a193", "a194", "a195", "a196", "a197", "a198", "a199", "a200", "a201", "a202", "a203", "a204", "a205", "a206", "a207", "s40", "s41", "s42", "s43", "s44", "s45", "s46", "s47", "s48", "s49", "s50", "s51", "s62", "s63", "s64", "s65", "s66", "s67", "s68", "s69", "s70", "s72", "s73", "s74", "s75", "s76", "scc", "memory");
  if (stat == 0) return;
  const Geo G(wid0, bid);
  const unsigned wid = G.wid, lane = lane_id(), col0 = G.ii * 160u;
  if (stat == 1) {
    asm volatile("s_waitcnt vmcnt(0)" ::: "memory");
    for (unsigned t = 0; t < 15; ++t) {
      const unsigned q = t % 3u, nu = (t / 3u) * 2u;
      f32x4 a0, a1;
      G.slow_pair(wid * 48u + q * 16u, col0 + nu * 16u, G.xq * 8u, G.xq * 8u + 8u, lane, a0, a1);
      st16_sc1(G.ps() + (G.xq * 4u + wid) * 15360u + nu * 1536u + q * 1024u + lane * 16u,
               u32x4{cvtpk(a0[0], a0[1]), cvtpk(a0[2], a0[3]), cvtpk(a1[0], a1[1]), cvtpk(a1[2], a1[3])});
    }
    asm volatile("s_waitcnt vmcnt(0)\n\ts_barrier" ::: "memory");
    if (wid == 0 && lane == 0) __hip_atomic_fetch_add((gu32*)G.cntD(), 1u, __ATOMIC_RELAXED, __HIP_MEMORY_SCOPE_AGENT);
  }
  for (unsigned t = 0; t < (wid < 3u ? 4u : 3u); ++t) {
    const unsigned row = t < 3u ? wid * 48u + t * 16u : G.xq * 48u + wid * 16u, col = t < 3u ? col0 + G.xq * 32u : col0 + 128u;
    f32x4 a0, a1;
    G.slow_pair(row, col, 0u, 32u, lane, a0, a1);
    G.store_pair(row, col, lane, a0, a1);
  }
}

extern "C" void kernel_launch(void* const* d_in, const int* in_sizes, int n_in, void* d_out, int out_size, void* d_ws, size_t ws_size, hipStream_t stream) {
  (void)in_sizes; (void)n_in; (void)out_size; (void)ws_size;
  (void)hipFuncSetAttribute((const void*)fused_attn, hipFuncAttributeMaxDynamicSharedMemorySize, 163840);
  Params P;
  for (int p = 0; p < 2; ++p) {
    P.x[p] = (const u16*)d_in[5 * p]; P.k[p] = (const u16*)d_in[5 * p + 1]; P.v[p] = (const u16*)d_in[5 * p + 2]; P.wq[p] = (const u16*)d_in[5 * p + 3]; P.wo[p] = (const u16*)d_in[5 * p + 4];
  }
  P.ws = (char*)d_ws; P.out = (u16*)d_out;
  fused_attn<<<256, 256, 163840, stream>>>(P);
}
```
